```python
import torch

def _softmax(x, axis=-1):
    return torch.softmax(x, dim=axis)
def _gelu(x):
    return torch.nn.functional.gelu(x, approximate="tanh")
def _silu(x):
    return torch.nn.functional.silu(x)
def _sigmoid(x):
    return torch.sigmoid(x)
def _layernorm(x, axis=-1, eps=1e-5):
    m = x.mean(dim=axis, keepdim=True)
    v = x.var(dim=axis, keepdim=True, unbiased=False)
    return (x - m) / torch.sqrt(v + eps)
def _scatter_add(tgt, idx, src, axis=0):
    return tgt.clone().index_add_(axis, idx, src)
def _segment_sum(src, seg, n):
    out = torch.zeros((n,) + src.shape[1:], dtype=src.dtype)
    return out.index_add_(0, seg, src)
def _pad(x, pw):
    flat = tuple(v for pair in reversed(pw) for v in pair)
    return torch.nn.functional.pad(x, flat)

kernel_name = 'standalone__geglu_ffn__prompt'

def setup_inputs():

    _w = (lambda a: (lambda t: t.to(torch.bfloat16) if t.is_floating_point() else t)(torch.as_tensor(a)))
    B, S, D, F = 1, 2048, 576, 4608
    return {
        "x": _w((lambda *s: torch.randn(*s, dtype=torch.bfloat16) * 0.05)(B, S, D)),
        "w_gate": _w((lambda *s: torch.randn(*s, dtype=torch.bfloat16) * 0.05)(D, F)),
        "w_up": _w((lambda *s: torch.randn(*s, dtype=torch.bfloat16) * 0.05)(D, F)),
        "w_down": _w((lambda *s: torch.randn(*s, dtype=torch.bfloat16) * 0.05)(F, D)),
    }

def reference(x, w_gate, w_up, w_down):
    return (_gelu(x @ w_gate) * (x @ w_up)) @ w_down
```

```cpp
#include <hip/hip_runtime.h>
#include <hip/hip_bf16.h>
typedef __attribute__((ext_vector_type(8))) short bf16x8;
typedef __attribute__((ext_vector_type(4))) float f32x4;
constexpr int S=2048, D=576, F=4608;
__device__ inline float bf2f(unsigned short v){ return __uint_as_float(((unsigned)v)<<16); }
__device__ inline unsigned short f2bf(float x){ unsigned u=__float_as_uint(x); u += 0x7fffu + ((u>>16)&1u); return (unsigned short)(u>>16); }
__global__ void k1(const unsigned short* x, const unsigned short* wg, const unsigned short* wu, unsigned short* h){
  int lane=threadIdx.x&63; int wave=threadIdx.x>>6;
  int tile=blockIdx.x*4+wave; int tn=tile%(F/16), tm=tile/(F/16);
  int r=lane&15, q=lane>>4;
  f32x4 ag={0,0,0,0}, au={0,0,0,0};
  for(int k=0;k<D;k+=32){
    bf16x8 a=*(const bf16x8*)(x+(size_t)(tm*16+r)*D+k+8*q);
    bf16x8 bg,bu;
    for(int j=0;j<8;j++){ bg[j]=wg[(size_t)(k+8*q+j)*F+tn*16+r]; bu[j]=wu[(size_t)(k+8*q+j)*F+tn*16+r]; }
    ag=__builtin_amdgcn_mfma_f32_16x16x32_bf16(a,bg,ag,0,0,0);
    au=__builtin_amdgcn_mfma_f32_16x16x32_bf16(a,bu,au,0,0,0);
  }
  for(int j=0;j<4;j++){
    float g=ag[j], u=au[j];
    float z=-2.f*0.7978845608028654f*1.4426950408889634f*(g+0.044715f*g*g*g);
    float e=__builtin_amdgcn_exp2f(z);
    float hv=g*u*__builtin_amdgcn_rcpf(1.f+e);
    h[(size_t)(tm*16+q*4+j)*F+tn*16+r]=f2bf(hv);
  }
}
__global__ void k2(const unsigned short* h, const unsigned short* wd, unsigned short* out){
  int lane=threadIdx.x&63; int wave=threadIdx.x>>6;
  int tile=blockIdx.x*4+wave; int tn=tile%(D/16), tm=tile/(D/16);
  int r=lane&15, q=lane>>4;
  f32x4 a4={0,0,0,0};
  for(int k=0;k<F;k+=32){
    bf16x8 a=*(const bf16x8*)(h+(size_t)(tm*16+r)*F+k+8*q);
    bf16x8 b;
    for(int j=0;j<8;j++){ b[j]=wd[(size_t)(k+8*q+j)*D+tn*16+r]; }
    a4=__builtin_amdgcn_mfma_f32_16x16x32_bf16(a,b,a4,0,0,0);
  }
  for(int j=0;j<4;j++) out[(size_t)(tm*16+q*4+j)*D+tn*16+r]=f2bf(a4[j]);
}
extern "C" void kernel_launch(void* const* d_in, const int* in_sizes, int n_in,
                              void* d_out, int out_size, void* d_ws, size_t ws_size,
                              hipStream_t stream) {
  const unsigned short* x=(const unsigned short*)d_in[0];
  const unsigned short* wg=(const unsigned short*)d_in[1];
  const unsigned short* wu=(const unsigned short*)d_in[2];
  const unsigned short* wd=(const unsigned short*)d_in[3];
  unsigned short* h=(unsigned short*)d_ws;
  k1<<<(S/16)*(F/16)/4,256,0,stream>>>(x,wg,wu,h);
  k2<<<(S/16)*(D/16)/4,256,0,stream>>>(h,wd,(unsigned short*)d_out);
}
```

```cpp
#include <hip/hip_runtime.h>
#include <hip/hip_bf16.h>

__global__ __launch_bounds__(256, 1) void geglu_fused(const void* x, const void* wg, const void* wu, const void* wd, void* out, void* ws) {
  __shared__ __attribute__((aligned(16))) char lds[163840];
  unsigned tid = threadIdx.x;
  unsigned lp = (unsigned)(size_t)lds;
  asm volatile(
"s_load_dwordx2 s[36:37], %1, 0x0\n"
"s_load_dwordx2 s[40:41], %1, 0x8\n"
"s_load_dwordx2 s[44:45], %1, 0x10\n"
"s_load_dwordx2 s[48:49], %1, 0x18\n"
"s_load_dwordx2 s[4:5], %1, 0x20\n"
"s_load_dwordx2 s[52:53], %1, 0x28\n"
"s_mov_b32 s96, %2\n"
"v_mov_b32 v1, %0\n"
"v_and_b32 v2, 63, v1\n"
"v_lshrrev_b32 v3, 6, v1\n"
"s_nop 0\n"
"v_readfirstlane_b32 s28, v3\n"
"s_and_b32 s97, s96, 7\n"
"s_lshr_b32 s98, s96, 3\n"
"s_and_b32 s99, s97, 7\n"
"s_lshl_b32 s30, s99, 1\n"
"s_and_b32 s99, s98, 1\n"
"s_add_u32 s30, s30, s99\n"
"s_lshr_b32 s31, s98, 1\n"
"s_lshr_b32 s29, s97, 3\n"
"s_lshl_b32 s29, s29, 4\n"
"s_add_u32 s29, s29, s31\n"
"s_getpc_b64 s[60:61]\n"
"Lpc%=:\n"
"s_and_b32 s61, s61, 0xffff\n"
"s_mov_b32 s62, Lend%=-Lpc%=\n"
"s_mov_b32 s63, 0x20000\n"
"s_lshl_b32 s99, s98, 2\n"
"s_add_u32 s99, s99, s28\n"
"s_mul_i32 s97, s99, 37\n"
"s_lshr_b32 s97, s97, 8\n"
"s_mul_i32 s97, s97, 7\n"
"s_sub_u32 s99, s99, s97\n"
"s_lshl_b32 s99, s99, 13\n"
"v_lshlrev_b32 v12, 7, v2\n"
"v_add_u32 v12, s99, v12\n"
"buffer_load_dword v197, v12, s[60:63], 0 offen offset:0\n"
"v_lshrrev_b32 v3, 3, v2\n"
"v_and_b32 v4, 7, v2\n"
"s_lshr_b32 s58, s28, 1\n"
"s_and_b32 s57, s28, 1\n"
"s_lshl_b32 s91, s28, 12\n"
"s_mul_i32 s17, s31, 9\n"
"s_lshr_b32 s17, s17, 4\n"
"s_mul_i32 s56, s30, 0x120\n"
"s_and_b32 s99, s30, 1\n"
"s_lshl_b32 s96, s99, 5\n"
"s_xor_b32 s99, s99, 1\n"
"s_lshl_b32 s99, s99, 8\n"
"s_add_u32 s59, s56, s99\n"
"s_add_u32 s56, s56, s96\n"
"s_lshl_b32 s96, s29, 7\n"
"s_lshl_b32 s97, s28, 5\n"
"s_add_u32 s96, s96, s97\n"
"v_add_u32 v9, s96, v3\n"
"v_mul_u32_u24 v9, 0x480, v9\n"
"v_lshrrev_b32 v10, 1, v3\n"
"v_add_u32 v11, 0, v10\n"
"v_and_b32 v11, 7, v11\n"
"v_xor_b32 v11, v11, v4\n"
"v_lshl_add_u32 v11, v11, 4, v9\n"
"v_add_u32 v181, 0, v11\n"
"v_add_u32 v11, 4, v10\n"
"v_and_b32 v11, 7, v11\n"
"v_xor_b32 v11, v11, v4\n"
"v_lshl_add_u32 v11, v11, 4, v9\n"
"v_add_u32 v182, 0x2000, v11\n"
"v_add_u32 v11, 8, v10\n"
"v_and_b32 v11, 7, v11\n"
"v_xor_b32 v11, v11, v4\n"
"v_lshl_add_u32 v11, v11, 4, v9\n"
"v_add_u32 v183, 0x4000, v11\n"
"v_add_u32 v11, 12, v10\n"
"v_and_b32 v11, 7, v11\n"
"v_xor_b32 v11, v11, v4\n"
"v_lshl_add_u32 v11, v11, 4, v9\n"
"v_add_u32 v184, 0x6000, v11\n"
"v_and_b32 v10, 1, v10\n"
"s_lshl_b32 s96, s57, 1\n"
"v_or_b32 v10, s96, v10\n"
"v_lshrrev_b32 v11, 1, v4\n"
"v_xor_b32 v11, v11, v10\n"
"v_lshlrev_b32 v11, 5, v11\n"
"v_and_b32 v12, 1, v4\n"
"v_lshl_add_u32 v11, v12, 4, v11\n"
"s_lshl_b32 s96, s28, 3\n"
"v_add_u32 v9, s96, v3\n"
"v_mul_u32_u24 v9, 0x2400, v9\n"
"v_add_u32 v9, v9, v11\n"
"s_lshl_b32 s96, s56, 1\n"
"v_add_u32 v176, s96, v9\n"
"v_add_u32 v177, 0x47800, v176\n"
"s_add_u32 s97, s17, 0\n"
"s_sub_u32 s98, s97, 9\n"
"s_cmp_ge_u32 s97, 9\n"
"s_cselect_b32 s97, s98, s97\n"
"s_mul_i32 s64, s97, 0x90000\n"
"s_lshl_b32 s73, s97, 7\n"
"s_add_u32 s97, s17, 1\n"
"s_sub_u32 s98, s97, 9\n"
"s_cmp_ge_u32 s97, 9\n"
"s_cselect_b32 s97, s98, s97\n"
"s_mul_i32 s65, s97, 0x90000\n"
"s_lshl_b32 s74, s97, 7\n"
"s_add_u32 s97, s17, 2\n"
"s_sub_u32 s98, s97, 9\n"
"s_cmp_ge_u32 s97, 9\n"
"s_cselect_b32 s97, s98, s97\n"
"s_mul_i32 s66, s97, 0x90000\n"
"s_lshl_b32 s75, s97, 7\n"
"s_add_u32 s97, s17, 3\n"
"s_sub_u32 s98, s97, 9\n"
"s_cmp_ge_u32 s97, 9\n"
"s_cselect_b32 s97, s98, s97\n"
"s_mul_i32 s67, s97, 0x90000\n"
"s_lshl_b32 s76, s97, 7\n"
"s_add_u32 s97, s17, 4\n"
"s_sub_u32 s98, s97, 9\n"
"s_cmp_ge_u32 s97, 9\n"
"s_cselect_b32 s97, s98, s97\n"
"s_mul_i32 s68, s97, 0x90000\n"
"s_lshl_b32 s77, s97, 7\n"
"s_add_u32 s97, s17, 5\n"
"s_sub_u32 s98, s97, 9\n"
"s_cmp_ge_u32 s97, 9\n"
"s_cselect_b32 s97, s98, s97\n"
"s_mul_i32 s69, s97, 0x90000\n"
"s_lshl_b32 s78, s97, 7\n"
"s_add_u32 s97, s17, 6\n"
"s_sub_u32 s98, s97, 9\n"
"s_cmp_ge_u32 s97, 9\n"
"s_cselect_b32 s97, s98, s97\n"
"s_mul_i32 s70, s97, 0x90000\n"
"s_lshl_b32 s79, s97, 7\n"
"s_add_u32 s97, s17, 7\n"
"s_sub_u32 s98, s97, 9\n"
"s_cmp_ge_u32 s97, 9\n"
"s_cselect_b32 s97, s98, s97\n"
"s_mul_i32 s71, s97, 0x90000\n"
"s_lshl_b32 s80, s97, 7\n"
"s_add_u32 s97, s17, 8\n"
"s_sub_u32 s98, s97, 9\n"
"s_cmp_ge_u32 s97, 9\n"
"s_cselect_b32 s97, s98, s97\n"
"s_mul_i32 s72, s97, 0x90000\n"
"s_lshl_b32 s81, s97, 7\n"
"s_waitcnt lgkmcnt(0)\n"
"s_and_b32 s37, s37, 0xffff\n"
"s_mov_b32 s38, 0x240000\n"
"s_mov_b32 s39, 0x20000\n"
"s_and_b32 s41, s41, 0xffff\n"
"s_mov_b32 s42, 0x510000\n"
"s_mov_b32 s43, 0x20000\n"
"s_sub_u32 s44, s44, 0x400\n"
"s_subb_u32 s45, s45, 0\n"
"s_and_b32 s45, s45, 0xffff\n"
"s_mov_b32 s46, 0x510400\n"
"s_mov_b32 s47, 0x20000\n"
"s_add_u32 m0, s91, 0x16000\n"
"s_nop 0\n"
"buffer_load_dwordx4 v181, s[36:39], s73 offen offset:0 lds\n"
"s_and_b32 s49, s49, 0xffff\n"
"s_mov_b32 s50, 0x510000\n"
"s_mov_b32 s51, 0x20000\n"
"s_and_b32 s5, s5, 0xffff\n"
"s_mov_b32 s6, 0x240000\n"
"s_mov_b32 s7, 0x20000\n"
"s_mov_b32 s8, s52\n"
"s_mov_b32 s9, s53\n"
"s_and_b32 s53, s53, 0xffff\n"
"s_mov_b32 s54, 0x2400000\n"
"s_mov_b32 s55, 0x20000\n"
"s_and_b32 s9, s9, 0xffff\n"
"buffer_load_dwordx4 v182, s[36:39], s73 offen offset:1024 lds\n"
"s_mov_b32 s10, 0x2512800\n"
"s_mov_b32 s11, 0x20000\n"
"s_lshl_b32 s96, s29, 7\n"
"s_add_u32 s96, s96, 0x2412000\n"
"s_add_u32 s18, s8, s96\n"
"s_addc_u32 s19, s9, 0\n"
"s_load_dwordx2 s[12:13], s[18:19], 0x0\n"
"s_lshl_b32 s92, s28, 11\n"
"s_mul_i32 s93, s28, 0x2400\n"
"s_mov_b32 s94, 0xbdd2d3e8\n"
"s_mov_b32 s95, 0\n"
"v_mov_b32 v193, 0xc0135761\n"
"buffer_load_dwordx4 v183, s[36:39], s73 offen offset:2048 lds\n"
"s_add_u32 s99, s56, 0\n"
"s_mul_i32 s99, s99, 0x480\n"
"s_sub_u32 s82, s99, 0\n"
"s_add_u32 s99, s56, 32\n"
"s_mul_i32 s99, s99, 0x480\n"
"s_sub_u32 s83, s99, 0x400\n"
"s_add_u32 s99, s56, 64\n"
"s_mul_i32 s99, s99, 0x480\n"
"s_sub_u32 s84, s99, 0x800\n"
"s_add_u32 s99, s56, 0x60\n"
"s_mul_i32 s99, s99, 0x480\n"
"s_sub_u32 s85, s99, 0xc00\n"
"buffer_load_dwordx4 v184, s[36:39], s73 offen offset:3072 lds\n"
"s_add_u32 s99, s56, 0x80\n"
"s_mul_i32 s99, s99, 0x480\n"
"s_sub_u32 s86, s99, 0\n"
"s_add_u32 s99, s56, 0xa0\n"
"s_mul_i32 s99, s99, 0x480\n"
"s_sub_u32 s87, s99, 0x400\n"
"s_add_u32 s99, s56, 0xc0\n"
"s_mul_i32 s99, s99, 0x480\n"
"s_sub_u32 s88, s99, 0x800\n"
"s_add_u32 s99, s56, 0xe0\n"
"s_mul_i32 s99, s99, 0x480\n"
"s_sub_u32 s89, s99, 0xc00\n"
"s_add_u32 m0, s91, 0\n"
"s_nop 0\n"
"buffer_load_dwordx4 v176, s[40:43], s64 offen offset:0 lds\n"
"s_mul_i32 s90, s59, 0x480\n"
"v_lshrrev_b32 v5, 4, v2\n"
"v_and_b32 v8, 15, v2\n"
"v_lshrrev_b32 v6, 2, v8\n"
"v_and_b32 v7, 3, v2\n"
"v_lshrrev_b32 v9, 1, v6\n"
"v_and_b32 v10, 1, v5\n"
"v_lshl_add_u32 v9, v10, 1, v9\n"
"v_lshlrev_b32 v10, 12, v5\n"
"v_lshl_add_u32 v10, v6, 7, v10\n"
"v_lshl_add_u32 v10, v7, 3, v10\n"
"s_lshl_b32 s96, s57, 1\n"
"buffer_load_dwordx4 v176, s[44:47], s64 offen offset:1024 lds\n"
"s_add_u32 s97, s96, 0\n"
"v_xor_b32 v11, s97, v9\n"
"v_lshl_add_u32 v168, v11, 5, v10\n"
"v_add_u32 v169, 0x8000, v168\n"
"s_add_u32 s97, s96, 1\n"
"v_xor_b32 v11, s97, v9\n"
"v_lshl_add_u32 v170, v11, 5, v10\n"
"v_add_u32 v171, 0x8000, v170\n"
"s_lshl_b32 s96, s58, 6\n"
"v_add_u32 v9, s96, v8\n"
"v_lshlrev_b32 v9, 7, v9\n"
"v_add_u32 v9, 0x16000, v9\n"
"buffer_load_dwordx4 v177, s[40:43], s64 offen offset:2048 lds\n"
"v_lshrrev_b32 v10, 1, v8\n"
"v_add_u32 v11, 0, v5\n"
"v_xor_b32 v11, v11, v10\n"
"v_lshl_add_u32 v179, v11, 4, v9\n"
"v_add_u32 v11, 4, v5\n"
"v_xor_b32 v11, v11, v10\n"
"v_lshl_add_u32 v180, v11, 4, v9\n"
"v_lshrrev_b32 v9, 2, v2\n"
"s_lshl_b32 s96, s28, 4\n"
"v_add_u32 v9, s96, v9\n"
"v_mul_u32_u24 v9, 0x2400, v9\n"
"v_lshrrev_b32 v10, 1, v7\n"
"buffer_load_dwordx4 v177, s[44:47], s64 offen offset:3072 lds\n"
"v_lshrrev_b32 v11, 5, v2\n"
"v_xor_b32 v10, v10, v11\n"
"v_lshlrev_b32 v10, 5, v10\n"
"v_and_b32 v11, 1, v2\n"
"v_lshl_add_u32 v10, v11, 4, v10\n"
"v_add_u32 v9, v9, v10\n"
"s_lshl_b32 s96, s59, 1\n"
"v_add_u32 v178, s96, v9\n"
"s_lshl_b32 s96, s28, 3\n"
"v_add_u32 v9, s96, v3\n"
"v_mul_u32_u24 v9, 0x480, v9\n"
"v_lshrrev_b32 v10, 1, v3\n"
"s_add_u32 m0, s91, 0x1a000\n"
"s_nop 0\n"
"buffer_load_dwordx4 v181, s[36:39], s74 offen offset:0 lds\n"
"v_and_b32 v10, 3, v10\n"
"v_lshrrev_b32 v11, 1, v4\n"
"v_xor_b32 v10, v10, v11\n"
"v_lshlrev_b32 v10, 5, v10\n"
"v_and_b32 v11, 1, v4\n"
"v_lshl_add_u32 v10, v11, 4, v10\n"
"v_add_u32 v189, v9, v10\n"
"v_lshrrev_b32 v9, 1, v5\n"
"buffer_load_dwordx4 v182, s[36:39], s74 offen offset:1024 lds\n"
"v_lshlrev_b32 v9, 11, v9\n"
"v_and_b32 v10, 1, v5\n"
"v_lshl_add_u32 v11, v10, 3, v6\n"
"v_lshl_add_u32 v9, v11, 6, v9\n"
"v_xor_b32 v10, s57, v10\n"
"v_lshl_add_u32 v9, v10, 5, v9\n"
"v_lshl_add_u32 v172, v7, 3, v9\n"
"v_add_u32 v173, 0x8000, v172\n"
"buffer_load_dwordx4 v183, s[36:39], s74 offen offset:2048 lds\n"
"v_lshrrev_b32 v9, 1, v5\n"
"v_mul_u32_u24 v9, 0x2400, v9\n"
"v_and_b32 v10, 1, v5\n"
"v_lshl_add_u32 v11, v10, 2, v6\n"
"v_lshl_add_u32 v9, v11, 7, v9\n"
"v_lshl_add_u32 v9, v7, 3, v9\n"
"v_lshrrev_b32 v11, 1, v6\n"
"v_lshl_add_u32 v10, v10, 1, v11\n"
"buffer_load_dwordx4 v184, s[36:39], s74 offen offset:3072 lds\n"
"s_lshl_b32 s96, s57, 1\n"
"s_add_u32 s97, s96, 0\n"
"v_xor_b32 v11, s97, v10\n"
"v_lshl_add_u32 v185, v11, 5, v9\n"
"v_add_u32 v186, 0x16000, v185\n"
"s_add_u32 s97, s96, 1\n"
"v_xor_b32 v11, s97, v10\n"
"v_lshl_add_u32 v187, v11, 5, v9\n"
"s_add_u32 m0, s91, 0x4000\n"
"s_nop 0\n"
"buffer_load_dwordx4 v176, s[40:43], s65 offen offset:0 lds\n"
"v_add_u32 v188, 0x16000, v187\n"
"v_lshlrev_b32 v9, 4, v2\n"
"s_lshl_b32 s96, s58, 12\n"
"s_add_u32 s96, s96, 0x16000\n"
"v_add_u32 v190, s96, v9\n"
"v_add_u32 v191, 0x10000, v190\n"
"s_lshl_b32 s97, s57, 13\n"
"v_add_u32 v174, s97, v190\n"
"buffer_load_dwordx4 v176, s[44:47], s65 offen offset:1024 lds\n"
"s_lshl_b32 s97, s57, 3\n"
"v_add_u32 v175, s97, v191\n"
"s_lshl_b32 s96, s29, 4\n"
"s_add_u32 s96, s96, s30\n"
"s_mul_i32 s96, s96, 0x24000\n"
"s_add_u32 s96, s96, s91\n"
"v_add_u32 v192, s96, v9\n"
"s_lshr_b32 s20, s30, 3\n"
"buffer_load_dwordx4 v177, s[40:43], s65 offen offset:2048 lds\n"
"s_lshr_b32 s21, s30, 1\n"
"s_and_b32 s21, s21, 3\n"
"s_and_b32 s22, s30, 1\n"
"v_lshrrev_b32 v13, 3, v8\n"
"s_lshl_b32 s96, s57, 1\n"
"v_add_u32 v13, s96, v13\n"
"v_and_b32 v14, 7, v2\n"
"v_lshrrev_b32 v15, 4, v2\n"
"buffer_load_dwordx4 v177, s[44:47], s65 offen offset:3072 lds\n"
"s_lshl_b32 s23, s20, 1\n"
"s_add_u32 s23, s23, s58\n"
"s_mul_i32 s96, s29, 0x240000\n"
"s_lshl_b32 s97, s23, 12\n"
"s_add_u32 s96, s96, s97\n"
"s_lshl_b32 s97, s21, 10\n"
"s_add_u32 s96, s96, s97\n"
"s_lshl_b32 s97, s22, 7\n"
"s_add_u32 m0, s91, 0x1e000\n"
"s_nop 0\n"
"buffer_load_dwordx4 v181, s[36:39], s75 offen offset:0 lds\n"
"s_add_u32 s96, s96, s97\n"
"s_mov_b32 s97, 0x24000\n"
"v_mul_lo_u32 v9, s97, v15\n"
"v_lshl_add_u32 v9, v13, 8, v9\n"
"v_lshl_add_u32 v9, v14, 4, v9\n"
"v_add_u32 v202, s96, v9\n"
"s_lshl_b32 s96, s29, 7\n"
"s_lshl_b32 s97, s20, 6\n"
"buffer_load_dwordx4 v182, s[36:39], s75 offen offset:1024 lds\n"
"s_add_u32 s96, s96, s97\n"
"s_lshl_b32 s97, s21, 4\n"
"s_add_u32 s96, s96, s97\n"
"s_lshl_b32 s97, s22, 3\n"
"s_add_u32 s96, s96, s97\n"
"v_add_u32 v9, s96, v14\n"
"v_mul_u32_u24 v9, 0x480, v9\n"
"v_lshrrev_b32 v10, 1, v15\n"
"buffer_load_dwordx4 v183, s[36:39], s75 offen offset:2048 lds\n"
"v_lshlrev_b32 v10, 5, v10\n"
"v_lshl_add_u32 v10, v13, 3, v10\n"
"v_and_b32 v11, 1, v15\n"
"v_lshl_add_u32 v10, v11, 2, v10\n"
"v_add_u32 v9, v9, v10\n"
"s_lshl_b32 s96, s58, 6\n"
"v_add_u32 v203, s96, v9\n"
"s_lshl_b32 s96, s29, 11\n"
"buffer_load_dwordx4 v184, s[36:39], s75 offen offset:3072 lds\n"
"s_add_u32 s96, s96, 0x2400000\n"
"s_lshl_b32 s97, s23, 7\n"
"s_add_u32 s97, s97, s96\n"
"v_lshl_add_u32 v200, v8, 3, s97\n"
"s_lshl_b32 s97, s28, 7\n"
"s_add_u32 s97, s97, s96\n"
"s_lshl_b32 s98, s30, 3\n"
"s_add_u32 s97, s97, s98\n"
"s_add_u32 m0, s91, 0x8000\n"
"s_nop 0\n"
"buffer_load_dwordx4 v176, s[40:43], s66 offen offset:0 lds\n"
"v_mov_b32 v201, s97\n"
"s_lshl_b32 s96, s29, 7\n"
"s_add_u32 s96, s96, 0x2412000\n"
"s_cmp_eq_u32 s30, 0\n"
"s_cselect_b32 s96, s96, 0xfffffff0\n"
"v_mov_b32 v204, s96\n"
"s_waitcnt lgkmcnt(0)\n"
"s_add_u32 s12, s12, 1\n"
"buffer_load_dwordx4 v176, s[44:47], s66 offen offset:1024 lds\n"
"s_addc_u32 s13, s13, 0\n"
"v_mov_b32 v198, s12\n"
"v_mov_b32 v199, s13\n"
"s_lshl_b32 s96, s31, 1\n"
"s_add_u32 s96, s96, s58\n"
"s_mul_i32 s96, s96, 18\n"
"v_min_u32 v9, 17, v2\n"
"v_add_u32 v9, s96, v9\n"
"buffer_load_dwordx4 v177, s[40:43], s66 offen offset:2048 lds\n"
"v_mul_u32_u24 v9, 0x2400, v9\n"
"s_lshl_b32 s96, s56, 1\n"
"v_add_u32 v194, s96, v9\n"
"s_lshl_b32 s96, s59, 1\n"
"v_add_u32 v195, s96, v9\n"
"s_lshl_b32 s96, s31, 2\n"
"s_add_u32 s96, s96, s28\n"
"s_mul_i32 s96, s96, 41\n"
"buffer_load_dwordx4 v177, s[44:47], s66 offen offset:3072 lds\n"
"v_min_u32 v9, 40, v2\n"
"v_add_u32 v9, s96, v9\n"
"v_min_u32 v9, 0xa1f, v9\n"
"v_lshlrev_b32 v9, 7, v9\n"
"s_mul_i32 s96, s30, 0x51000\n"
"v_add_u32 v196, s96, v9\n"
"s_add_u32 s96, s44, 0x400\n"
"s_addc_u32 s97, s45, 0\n"
"s_cmp_eq_u32 s57, 0\n"
"s_cselect_b32 s60, s40, s96\n"
"s_cselect_b32 s61, s41, s97\n"
"s_mov_b32 s62, 0x510000\n"
"s_mov_b32 s63, 0x20000\n"
"s_waitcnt vmcnt(16)\n"
"s_barrier\n"
"ds_read_b64_tr_b16 v[128:129], v168 offset:0\n"
"ds_read_b64_tr_b16 v[130:131], v168 offset:512\n"
"ds_read_b64_tr_b16 v[132:133], v168 offset:2048\n"
"ds_read_b64_tr_b16 v[134:135], v168 offset:2560\n"
"ds_read_b64_tr_b16 v[136:137], v168 offset:1024\n"
"ds_read_b64_tr_b16 v[138:139], v168 offset:1536\n"
"ds_read_b64_tr_b16 v[140:141], v168 offset:3072\n"
"ds_read_b64_tr_b16 v[142:143], v168 offset:3584\n"
"ds_read_b128 a[0:3], v179 offset:0\n"
"ds_read_b128 a[16:19], v180 offset:0\n"
"ds_read_b128 a[4:7], v179 offset:2048\n"
"ds_read_b128 a[20:23], v180 offset:2048\n"
"ds_read_b128 a[8:11], v179 offset:4096\n"
"ds_read_b128 a[24:27], v180 offset:4096\n"
"ds_read_b128 a[12:15], v179 offset:6144\n"
"s_waitcnt lgkmcnt(14)\n"
"ds_read_b128 a[28:31], v180 offset:6144\n"
"s_waitcnt lgkmcnt(7)\n"
"v_mfma_f32_16x16x32_bf16 v[0:3], v[128:131], a[0:3], 0\n"
"ds_read_b64_tr_b16 v[144:145], v170 offset:0\n"
"s_waitcnt lgkmcnt(7)\n"
"v_mfma_f32_16x16x32_bf16 v[0:3], v[132:135], a[16:19], v[0:3]\n"
"ds_read_b64_tr_b16 v[146:147], v170 offset:512\n"
"s_waitcnt lgkmcnt(7)\n"
"v_mfma_f32_16x16x32_bf16 v[4:7], v[128:131], a[4:7], 0\n"
"ds_read_b64_tr_b16 v[148:149], v170 offset:2048\n"
"s_waitcnt lgkmcnt(7)\n"
"v_mfma_f32_16x16x32_bf16 v[4:7], v[132:135], a[20:23], v[4:7]\n"
"ds_read_b64_tr_b16 v[150:151], v170 offset:2560\n"
"s_waitcnt lgkmcnt(7)\n"
"v_mfma_f32_16x16x32_bf16 v[8:11], v[128:131], a[8:11], 0\n"
"s_waitcnt lgkmcnt(6)\n"
"v_mfma_f32_16x16x32_bf16 v[8:11], v[132:135], a[24:27], v[8:11]\n"
"s_waitcnt lgkmcnt(5)\n"
"v_mfma_f32_16x16x32_bf16 v[12:15], v[128:131], a[12:15], 0\n"
"s_waitcnt lgkmcnt(4)\n"
"v_mfma_f32_16x16x32_bf16 v[12:15], v[132:135], a[28:31], v[12:15]\n"
"v_mfma_f32_16x16x32_bf16 v[16:19], v[136:139], a[0:3], 0\n"
"ds_read_b64_tr_b16 v[128:129], v170 offset:1024\n"
"v_mfma_f32_16x16x32_bf16 v[16:19], v[140:143], a[16:19], v[16:19]\n"
"ds_read_b64_tr_b16 v[130:131], v170 offset:1536\n"
"v_mfma_f32_16x16x32_bf16 v[20:23], v[136:139], a[4:7], 0\n"
"ds_read_b64_tr_b16 v[132:133], v170 offset:3072\n"
"v_mfma_f32_16x16x32_bf16 v[20:23], v[140:143], a[20:23], v[20:23]\n"
"ds_read_b64_tr_b16 v[134:135], v170 offset:3584\n"
"v_mfma_f32_16x16x32_bf16 v[24:27], v[136:139], a[8:11], 0\n"
"v_mfma_f32_16x16x32_bf16 v[24:27], v[140:143], a[24:27], v[24:27]\n"
"v_mfma_f32_16x16x32_bf16 v[28:31], v[136:139], a[12:15], 0\n"
"v_mfma_f32_16x16x32_bf16 v[28:31], v[140:143], a[28:31], v[28:31]\n"
"s_waitcnt lgkmcnt(4)\n"
"v_mfma_f32_16x16x32_bf16 v[32:35], v[144:147], a[0:3], 0\n"
"v_mfma_f32_16x16x32_bf16 v[32:35], v[148:151], a[16:19], v[32:35]\n"
"v_mfma_f32_16x16x32_bf16 v[36:39], v[144:147], a[4:7], 0\n"
"v_mfma_f32_16x16x32_bf16 v[36:39], v[148:151], a[20:23], v[36:39]\n"
"s_waitcnt lgkmcnt(0)\n"
"s_waitcnt vmcnt(8)\n"
"s_barrier\n"
"v_mfma_f32_16x16x32_bf16 v[40:43], v[144:147], a[8:11], 0\n"
"ds_read_b64_tr_b16 v[136:137], v168 offset:16384\n"
"ds_read_b128 a[32:35], v179 offset:16384\n"
"s_add_u32 m0, s91, 0x22000\n"
"v_mfma_f32_16x16x32_bf16 v[40:43], v[148:151], a[24:27], v[40:43]\n"
"ds_read_b64_tr_b16 v[138:139], v168 offset:16896\n"
"ds_read_b128 a[48:51], v180 offset:16384\n"
"buffer_load_dwordx4 v181, s[36:39], s76 offen offset:0 lds\n"
"v_mfma_f32_16x16x32_bf16 v[44:47], v[144:147], a[12:15], 0\n"
"ds_read_b64_tr_b16 v[140:141], v168 offset:18432\n"
"ds_read_b128 a[36:39], v179 offset:18432\n"
"v_mfma_f32_16x16x32_bf16 v[44:47], v[148:151], a[28:31], v[44:47]\n"
"ds_read_b64_tr_b16 v[142:143], v168 offset:18944\n"
"ds_read_b128 a[52:55], v180 offset:18432\n"
"v_mfma_f32_16x16x32_bf16 v[48:51], v[128:131], a[0:3], 0\n"
"ds_read_b64_tr_b16 v[144:145], v168 offset:17408\n"
"buffer_load_dwordx4 v182, s[36:39], s76 offen offset:1024 lds\n"
"v_mfma_f32_16x16x32_bf16 v[48:51], v[132:135], a[16:19], v[48:51]\n"
"ds_read_b64_tr_b16 v[146:147], v168 offset:17920\n"
"v_mfma_f32_16x16x32_bf16 v[52:55], v[128:131], a[4:7], 0\n"
"ds_read_b64_tr_b16 v[148:149], v168 offset:19456\n"
"v_mfma_f32_16x16x32_bf16 v[52:55], v[132:135], a[20:23], v[52:55]\n"
"ds_read_b64_tr_b16 v[150:151], v168 offset:19968\n"
"buffer_load_dwordx4 v183, s[36:39], s76 offen offset:2048 lds\n"
"v_mfma_f32_16x16x32_bf16 v[56:59], v[128:131], a[8:11], 0\n"
"ds_read_b128 a[40:43], v179 offset:20480\n"
"v_mfma_f32_16x16x32_bf16 v[56:59], v[132:135], a[24:27], v[56:59]\n"
"ds_read_b128 a[56:59], v180 offset:20480\n"
"v_mfma_f32_16x16x32_bf16 v[60:63], v[128:131], a[12:15], 0\n"
"ds_read_b128 a[44:47], v179 offset:22528\n"
"buffer_load_dwordx4 v184, s[36:39], s76 offen offset:3072 lds\n"
"v_mfma_f32_16x16x32_bf16 v[60:63], v[132:135], a[28:31], v[60:63]\n"
"s_waitcnt lgkmcnt(14)\n"
"ds_read_b128 a[60:63], v180 offset:22528\n"
"s_waitcnt lgkmcnt(9)\n"
"v_mfma_f32_16x16x32_bf16 v[0:3], v[136:139], a[32:35], v[0:3]\n"
"ds_read_b64_tr_b16 v[128:129], v170 offset:16384\n"
"s_add_u32 m0, s91, 0xc000\n"
"v_mfma_f32_16x16x32_bf16 v[0:3], v[140:143], a[48:51], v[0:3]\n"
"ds_read_b64_tr_b16 v[130:131], v170 offset:16896\n"
"buffer_load_dwordx4 v176, s[40:43], s67 offen offset:0 lds\n"
"v_mfma_f32_16x16x32_bf16 v[4:7], v[136:139], a[36:39], v[4:7]\n"
"ds_read_b64_tr_b16 v[132:133], v170 offset:18432\n"
"s_waitcnt lgkmcnt(11)\n"
"v_mfma_f32_16x16x32_bf16 v[4:7], v[140:143], a[52:55], v[4:7]\n"
"ds_read_b64_tr_b16 v[134:135], v170 offset:18944\n"
"s_waitcnt lgkmcnt(7)\n"
"v_mfma_f32_16x16x32_bf16 v[8:11], v[136:139], a[40:43], v[8:11]\n"
"buffer_load_dwordx4 v176, s[44:47], s67 offen offset:1024 lds\n"
"s_waitcnt lgkmcnt(6)\n"
"v_mfma_f32_16x16x32_bf16 v[8:11], v[140:143], a[56:59], v[8:11]\n"
"s_waitcnt lgkmcnt(5)\n"
"v_mfma_f32_16x16x32_bf16 v[12:15], v[136:139], a[44:47], v[12:15]\n"
"s_waitcnt lgkmcnt(4)\n"
"v_mfma_f32_16x16x32_bf16 v[12:15], v[140:143], a[60:63], v[12:15]\n"
"buffer_load_dwordx4 v177, s[40:43], s67 offen offset:2048 lds\n"
"v_mfma_f32_16x16x32_bf16 v[16:19], v[144:147], a[32:35], v[16:19]\n"
"ds_read_b64_tr_b16 v[136:137], v170 offset:17408\n"
"v_mfma_f32_16x16x32_bf16 v[16:19], v[148:151], a[48:51], v[16:19]\n"
"ds_read_b64_tr_b16 v[138:139], v170 offset:17920\n"
"v_mfma_f32_16x16x32_bf16 v[20:23], v[144:147], a[36:39], v[20:23]\n"
"ds_read_b64_tr_b16 v[140:141], v170 offset:19456\n"
"buffer_load_dwordx4 v177, s[44:47], s67 offen offset:3072 lds\n"
"v_mfma_f32_16x16x32_bf16 v[20:23], v[148:151], a[52:55], v[20:23]\n"
"ds_read_b64_tr_b16 v[142:143], v170 offset:19968\n"
"v_mfma_f32_16x16x32_bf16 v[24:27], v[144:147], a[40:43], v[24:27]\n"
"v_mfma_f32_16x16x32_bf16 v[24:27], v[148:151], a[56:59], v[24:27]\n"
"v_mfma_f32_16x16x32_bf16 v[28:31], v[144:147], a[44:47], v[28:31]\n"
"v_mfma_f32_16x16x32_bf16 v[28:31], v[148:151], a[60:63], v[28:31]\n"
"s_waitcnt lgkmcnt(4)\n"
"v_mfma_f32_16x16x32_bf16 v[32:35], v[128:131], a[32:35], v[32:35]\n"
"v_mfma_f32_16x16x32_bf16 v[32:35], v[132:135], a[48:51], v[32:35]\n"
"v_mfma_f32_16x16x32_bf16 v[36:39], v[128:131], a[36:39], v[36:39]\n"
"v_mfma_f32_16x16x32_bf16 v[36:39], v[132:135], a[52:55], v[36:39]\n"
"s_waitcnt lgkmcnt(0)\n"
"s_waitcnt vmcnt(8)\n"
"s_barrier\n"
"v_mfma_f32_16x16x32_bf16 v[40:43], v[128:131], a[40:43], v[40:43]\n"
"ds_read_b64_tr_b16 v[144:145], v169 offset:0\n"
"ds_read_b128 a[64:67], v179 offset:32768\n"
"s_add_u32 m0, s91, 0x16000\n"
"v_mfma_f32_16x16x32_bf16 v[40:43], v[132:135], a[56:59], v[40:43]\n"
"ds_read_b64_tr_b16 v[146:147], v169 offset:512\n"
"ds_read_b128 a[80:83], v180 offset:32768\n"
"buffer_load_dwordx4 v181, s[36:39], s77 offen offset:0 lds\n"
"v_mfma_f32_16x16x32_bf16 v[44:47], v[128:131], a[44:47], v[44:47]\n"
"ds_read_b64_tr_b16 v[148:149], v169 offset:2048\n"
"ds_read_b128 a[68:71], v179 offset:34816\n"
"v_mfma_f32_16x16x32_bf16 v[44:47], v[132:135], a[60:63], v[44:47]\n"
"ds_read_b64_tr_b16 v[150:151], v169 offset:2560\n"
"ds_read_b128 a[84:87], v180 offset:34816\n"
"v_mfma_f32_16x16x32_bf16 v[48:51], v[136:139], a[32:35], v[48:51]\n"
"ds_read_b64_tr_b16 v[128:129], v169 offset:1024\n"
"buffer_load_dwordx4 v182, s[36:39], s77 offen offset:1024 lds\n"
"v_mfma_f32_16x16x32_bf16 v[48:51], v[140:143], a[48:51], v[48:51]\n"
"ds_read_b64_tr_b16 v[130:131], v169 offset:1536\n"
"v_mfma_f32_16x16x32_bf16 v[52:55], v[136:139], a[36:39], v[52:55]\n"
"ds_read_b64_tr_b16 v[132:133], v169 offset:3072\n"
"v_mfma_f32_16x16x32_bf16 v[52:55], v[140:143], a[52:55], v[52:55]\n"
"ds_read_b64_tr_b16 v[134:135], v169 offset:3584\n"
"buffer_load_dwordx4 v183, s[36:39], s77 offen offset:2048 lds\n"
"v_mfma_f32_16x16x32_bf16 v[56:59], v[136:139], a[40:43], v[56:59]\n"
"ds_read_b128 a[72:75], v179 offset:36864\n"
"v_mfma_f32_16x16x32_bf16 v[56:59], v[140:143], a[56:59], v[56:59]\n"
"ds_read_b128 a[88:91], v180 offset:36864\n"
"v_mfma_f32_16x16x32_bf16 v[60:63], v[136:139], a[44:47], v[60:63]\n"
"ds_read_b128 a[76:79], v179 offset:38912\n"
"buffer_load_dwordx4 v184, s[36:39], s77 offen offset:3072 lds\n"
"v_mfma_f32_16x16x32_bf16 v[60:63], v[140:143], a[60:63], v[60:63]\n"
"s_waitcnt lgkmcnt(14)\n"
"ds_read_b128 a[92:95], v180 offset:38912\n"
"s_waitcnt lgkmcnt(9)\n"
"v_mfma_f32_16x16x32_bf16 v[0:3], v[144:147], a[64:67], v[0:3]\n"
"ds_read_b64_tr_b16 v[136:137], v171 offset:0\n"
"s_add_u32 m0, s91, 0x10000\n"
"v_mfma_f32_16x16x32_bf16 v[0:3], v[148:151], a[80:83], v[0:3]\n"
"ds_read_b64_tr_b16 v[138:139], v171 offset:512\n"
"buffer_load_dwordx4 v176, s[40:43], s68 offen offset:0 lds\n"
"v_mfma_f32_16x16x32_bf16 v[4:7], v[144:147], a[68:71], v[4:7]\n"
"ds_read_b64_tr_b16 v[140:141], v171 offset:2048\n"
"s_waitcnt lgkmcnt(11)\n"
"v_mfma_f32_16x16x32_bf16 v[4:7], v[148:151], a[84:87], v[4:7]\n"
"ds_read_b64_tr_b16 v[142:143], v171 offset:2560\n"
"s_waitcnt lgkmcnt(7)\n"
"v_mfma_f32_16x16x32_bf16 v[8:11], v[144:147], a[72:75], v[8:11]\n"
"buffer_load_dwordx4 v176, s[44:47], s68 offen offset:1024 lds\n"
"s_waitcnt lgkmcnt(6)\n"
"v_mfma_f32_16x16x32_bf16 v[8:11], v[148:151], a[88:91], v[8:11]\n"
"s_waitcnt lgkmcnt(5)\n"
"v_mfma_f32_16x16x32_bf16 v[12:15], v[144:147], a[76:79], v[12:15]\n"
"s_waitcnt lgkmcnt(4)\n"
"v_mfma_f32_16x16x32_bf16 v[12:15], v[148:151], a[92:95], v[12:15]\n"
"buffer_load_dwordx4 v177, s[40:43], s68 offen offset:2048 lds\n"
"v_mfma_f32_16x16x32_bf16 v[16:19], v[128:131], a[64:67], v[16:19]\n"
"ds_read_b64_tr_b16 v[144:145], v171 offset:1024\n"
"v_mfma_f32_16x16x32_bf16 v[16:19], v[132:135], a[80:83], v[16:19]\n"
"ds_read_b64_tr_b16 v[146:147], v171 offset:1536\n"
"v_mfma_f32_16x16x32_bf16 v[20:23], v[128:131], a[68:71], v[20:23]\n"
"ds_read_b64_tr_b16 v[148:149], v171 offset:3072\n"
"buffer_load_dwordx4 v177, s[44:47], s68 offen offset:3072 lds\n"
"v_mfma_f32_16x16x32_bf16 v[20:23], v[132:135], a[84:87], v[20:23]\n"
"ds_read_b64_tr_b16 v[150:151], v171 offset:3584\n"
"v_mfma_f32_16x16x32_bf16 v[24:27], v[128:131], a[72:75], v[24:27]\n"
"v_mfma_f32_16x16x32_bf16 v[24:27], v[132:135], a[88:91], v[24:27]\n"
"v_mfma_f32_16x16x32_bf16 v[28:31], v[128:131], a[76:79], v[28:31]\n"
"v_mfma_f32_16x16x32_bf16 v[28:31], v[132:135], a[92:95], v[28:31]\n"
"s_waitcnt lgkmcnt(4)\n"
"v_mfma_f32_16x16x32_bf16 v[32:35], v[136:139], a[64:67], v[32:35]\n"
"v_mfma_f32_16x16x32_bf16 v[32:35], v[140:143], a[80:83], v[32:35]\n"
"v_mfma_f32_16x16x32_bf16 v[36:39], v[136:139], a[68:71], v[36:39]\n"
"v_mfma_f32_16x16x32_bf16 v[36:39], v[140:143], a[84:87], v[36:39]\n"
"s_waitcnt lgkmcnt(0)\n"
"s_waitcnt vmcnt(8)\n"
"s_barrier\n"
"v_mfma_f32_16x16x32_bf16 v[40:43], v[136:139], a[72:75], v[40:43]\n"
"ds_read_b64_tr_b16 v[128:129], v169 offset:16384\n"
"ds_read_b128 a[96:99], v179 offset:49152\n"
"s_add_u32 m0, s91, 0x1a000\n"
"v_mfma_f32_16x16x32_bf16 v[40:43], v[140:143], a[88:91], v[40:43]\n"
"ds_read_b64_tr_b16 v[130:131], v169 offset:16896\n"
"ds_read_b128 a[112:115], v180 offset:49152\n"
"buffer_load_dwordx4 v181, s[36:39], s78 offen offset:0 lds\n"
"v_mfma_f32_16x16x32_bf16 v[44:47], v[136:139], a[76:79], v[44:47]\n"
"ds_read_b64_tr_b16 v[132:133], v169 offset:18432\n"
"ds_read_b128 a[100:103], v179 offset:51200\n"
"v_mfma_f32_16x16x32_bf16 v[44:47], v[140:143], a[92:95], v[44:47]\n"
"ds_read_b64_tr_b16 v[134:135], v169 offset:18944\n"
"ds_read_b128 a[116:119], v180 offset:51200\n"
"v_mfma_f32_16x16x32_bf16 v[48:51], v[144:147], a[64:67], v[48:51]\n"
"ds_read_b64_tr_b16 v[136:137], v169 offset:17408\n"
"buffer_load_dwordx4 v182, s[36:39], s78 offen offset:1024 lds\n"
"v_mfma_f32_16x16x32_bf16 v[48:51], v[148:151], a[80:83], v[48:51]\n"
"ds_read_b64_tr_b16 v[138:139], v169 offset:17920\n"
"v_mfma_f32_16x16x32_bf16 v[52:55], v[144:147], a[68:71], v[52:55]\n"
"ds_read_b64_tr_b16 v[140:141], v169 offset:19456\n"
"v_mfma_f32_16x16x32_bf16 v[52:55], v[148:151], a[84:87], v[52:55]\n"
"ds_read_b64_tr_b16 v[142:143], v169 offset:19968\n"
"buffer_load_dwordx4 v183, s[36:39], s78 offen offset:2048 lds\n"
"v_mfma_f32_16x16x32_bf16 v[56:59], v[144:147], a[72:75], v[56:59]\n"
"ds_read_b128 a[104:107], v179 offset:53248\n"
"v_mfma_f32_16x16x32_bf16 v[56:59], v[148:151], a[88:91], v[56:59]\n"
"ds_read_b128 a[120:123], v180 offset:53248\n"
"v_mfma_f32_16x16x32_bf16 v[60:63], v[144:147], a[76:79], v[60:63]\n"
"ds_read_b128 a[108:111], v179 offset:55296\n"
"buffer_load_dwordx4 v184, s[36:39], s78 offen offset:3072 lds\n"
"v_mfma_f32_16x16x32_bf16 v[60:63], v[148:151], a[92:95], v[60:63]\n"
"s_waitcnt lgkmcnt(14)\n"
"ds_read_b128 a[124:127], v180 offset:55296\n"
"s_waitcnt lgkmcnt(9)\n"
"v_mfma_f32_16x16x32_bf16 v[0:3], v[128:131], a[96:99], v[0:3]\n"
"ds_read_b64_tr_b16 v[144:145], v171 offset:16384\n"
"s_add_u32 m0, s91, 0\n"
"v_mfma_f32_16x16x32_bf16 v[0:3], v[132:135], a[112:115], v[0:3]\n"
"ds_read_b64_tr_b16 v[146:147], v171 offset:16896\n"
"buffer_load_dwordx4 v176, s[40:43], s69 offen offset:0 lds\n"
"v_mfma_f32_16x16x32_bf16 v[4:7], v[128:131], a[100:103], v[4:7]\n"
"ds_read_b64_tr_b16 v[148:149], v171 offset:18432\n"
"s_waitcnt lgkmcnt(11)\n"
"v_mfma_f32_16x16x32_bf16 v[4:7], v[132:135], a[116:119], v[4:7]\n"
"ds_read_b64_tr_b16 v[150:151], v171 offset:18944\n"
"s_waitcnt lgkmcnt(7)\n"
"v_mfma_f32_16x16x32_bf16 v[8:11], v[128:131], a[104:107], v[8:11]\n"
"buffer_load_dwordx4 v176, s[44:47], s69 offen offset:1024 lds\n"
"s_waitcnt lgkmcnt(6)\n"
"v_mfma_f32_16x16x32_bf16 v[8:11], v[132:135], a[120:123], v[8:11]\n"
"s_waitcnt lgkmcnt(5)\n"
"v_mfma_f32_16x16x32_bf16 v[12:15], v[128:131], a[108:111], v[12:15]\n"
"s_waitcnt lgkmcnt(4)\n"
"v_mfma_f32_16x16x32_bf16 v[12:15], v[132:135], a[124:127], v[12:15]\n"
"buffer_load_dwordx4 v177, s[40:43], s69 offen offset:2048 lds\n"
"v_mfma_f32_16x16x32_bf16 v[16:19], v[136:139], a[96:99], v[16:19]\n"
"ds_read_b64_tr_b16 v[128:129], v171 offset:17408\n"
"v_mfma_f32_16x16x32_bf16 v[16:19], v[140:143], a[112:115], v[16:19]\n"
"ds_read_b64_tr_b16 v[130:131], v171 offset:17920\n"
"v_mfma_f32_16x16x32_bf16 v[20:23], v[136:139], a[100:103], v[20:23]\n"
"ds_read_b64_tr_b16 v[132:133], v171 offset:19456\n"
"buffer_load_dwordx4 v177, s[44:47], s69 offen offset:3072 lds\n"
"v_mfma_f32_16x16x32_bf16 v[20:23], v[140:143], a[116:119], v[20:23]\n"
"ds_read_b64_tr_b16 v[134:135], v171 offset:19968\n"
"v_mfma_f32_16x16x32_bf16 v[24:27], v[136:139], a[104:107], v[24:27]\n"
"s_mov_b32 s17, 0\n"
"v_mfma_f32_16x16x32_bf16 v[24:27], v[140:143], a[120:123], v[24:27]\n"
"buffer_load_dword v197, v194, s[60:63], s17 offen offset:128\n"
"v_mfma_f32_16x16x32_bf16 v[28:31], v[136:139], a[108:111], v[28:31]\n"
"v_mfma_f32_16x16x32_bf16 v[28:31], v[140:143], a[124:127], v[28:31]\n"
"s_waitcnt lgkmcnt(4)\n"
"v_mfma_f32_16x16x32_bf16 v[32:35], v[144:147], a[96:99], v[32:35]\n"
"v_mfma_f32_16x16x32_bf16 v[32:35], v[148:151], a[112:115], v[32:35]\n"
"v_mfma_f32_16x16x32_bf16 v[36:39], v[144:147], a[100:103], v[36:39]\n"
"v_mfma_f32_16x16x32_bf16 v[36:39], v[148:151], a[116:119], v[36:39]\n"
"s_waitcnt lgkmcnt(0)\n"
"s_waitcnt vmcnt(9)\n"
"s_barrier\n"
"v_mfma_f32_16x16x32_bf16 v[40:43], v[144:147], a[104:107], v[40:43]\n"
"ds_read_b64_tr_b16 v[136:137], v169 offset:32768\n"
"ds_read_b128 a[128:131], v179 offset:0\n"
"s_add_u32 m0, s91, 0x1e000\n"
"v_mfma_f32_16x16x32_bf16 v[40:43], v[148:151], a[120:123], v[40:43]\n"
"ds_read_b64_tr_b16 v[138:139], v169 offset:33280\n"
"ds_read_b128 a[144:147], v180 offset:0\n"
"buffer_load_dwordx4 v181, s[36:39], s79 offen offset:0 lds\n"
"v_mfma_f32_16x16x32_bf16 v[44:47], v[144:147], a[108:111], v[44:47]\n"
"ds_read_b64_tr_b16 v[140:141], v169 offset:34816\n"
"ds_read_b128 a[132:135], v179 offset:2048\n"
"v_mfma_f32_16x16x32_bf16 v[44:47], v[148:151], a[124:127], v[44:47]\n"
"ds_read_b64_tr_b16 v[142:143], v169 offset:35328\n"
"ds_read_b128 a[148:151], v180 offset:2048\n"
"v_mfma_f32_16x16x32_bf16 v[48:51], v[128:131], a[96:99], v[48:51]\n"
"ds_read_b64_tr_b16 v[144:145], v169 offset:33792\n"
"buffer_load_dwordx4 v182, s[36:39], s79 offen offset:1024 lds\n"
"v_mfma_f32_16x16x32_bf16 v[48:51], v[132:135], a[112:115], v[48:51]\n"
"ds_read_b64_tr_b16 v[146:147], v169 offset:34304\n"
"v_mfma_f32_16x16x32_bf16 v[52:55], v[128:131], a[100:103], v[52:55]\n"
"ds_read_b64_tr_b16 v[148:149], v169 offset:35840\n"
"v_mfma_f32_16x16x32_bf16 v[52:55], v[132:135], a[116:119], v[52:55]\n"
"ds_read_b64_tr_b16 v[150:151], v169 offset:36352\n"
"buffer_load_dwordx4 v183, s[36:39], s79 offen offset:2048 lds\n"
"v_mfma_f32_16x16x32_bf16 v[56:59], v[128:131], a[104:107], v[56:59]\n"
"ds_read_b128 a[136:139], v179 offset:4096\n"
"v_mfma_f32_16x16x32_bf16 v[56:59], v[132:135], a[120:123], v[56:59]\n"
"ds_read_b128 a[152:155], v180 offset:4096\n"
"v_mfma_f32_16x16x32_bf16 v[60:63], v[128:131], a[108:111], v[60:63]\n"
"ds_read_b128 a[140:143], v179 offset:6144\n"
"buffer_load_dwordx4 v184, s[36:39], s79 offen offset:3072 lds\n"
"v_mfma_f32_16x16x32_bf16 v[60:63], v[132:135], a[124:127], v[60:63]\n"
"s_waitcnt lgkmcnt(14)\n"
"ds_read_b128 a[156:159], v180 offset:6144\n"
"s_waitcnt lgkmcnt(9)\n"
"v_mfma_f32_16x16x32_bf16 v[0:3], v[136:139], a[128:131], v[0:3]\n"
"ds_read_b64_tr_b16 v[128:129], v171 offset:32768\n"
"s_add_u32 m0, s91, 0x4000\n"
"v_mfma_f32_16x16x32_bf16 v[0:3], v[140:143], a[144:147], v[0:3]\n"
"ds_read_b64_tr_b16 v[130:131], v171 offset:33280\n"
"buffer_load_dwordx4 v176, s[40:43], s70 offen offset:0 lds\n"
"v_mfma_f32_16x16x32_bf16 v[4:7], v[136:139], a[132:135], v[4:7]\n"
"ds_read_b64_tr_b16 v[132:133], v171 offset:34816\n"
"s_waitcnt lgkmcnt(11)\n"
"v_mfma_f32_16x16x32_bf16 v[4:7], v[140:143], a[148:151], v[4:7]\n"
"ds_read_b64_tr_b16 v[134:135], v171 offset:35328\n"
"s_waitcnt lgkmcnt(7)\n"
"v_mfma_f32_16x16x32_bf16 v[8:11], v[136:139], a[136:139], v[8:11]\n"
"buffer_load_dwordx4 v176, s[44:47], s70 offen offset:1024 lds\n"
"s_waitcnt lgkmcnt(6)\n"
"v_mfma_f32_16x16x32_bf16 v[8:11], v[140:143], a[152:155], v[8:11]\n"
"s_waitcnt lgkmcnt(5)\n"
"v_mfma_f32_16x16x32_bf16 v[12:15], v[136:139], a[140:143], v[12:15]\n"
"s_waitcnt lgkmcnt(4)\n"
"v_mfma_f32_16x16x32_bf16 v[12:15], v[140:143], a[156:159], v[12:15]\n"
"buffer_load_dwordx4 v177, s[40:43], s70 offen offset:2048 lds\n"
"v_mfma_f32_16x16x32_bf16 v[16:19], v[144:147], a[128:131], v[16:19]\n"
"ds_read_b64_tr_b16 v[136:137], v171 offset:33792\n"
"v_mfma_f32_16x16x32_bf16 v[16:19], v[148:151], a[144:147], v[16:19]\n"
"ds_read_b64_tr_b16 v[138:139], v171 offset:34304\n"
"v_mfma_f32_16x16x32_bf16 v[20:23], v[144:147], a[132:135], v[20:23]\n"
"ds_read_b64_tr_b16 v[140:141], v171 offset:35840\n"
"buffer_load_dwordx4 v177, s[44:47], s70 offen offset:3072 lds\n"
"v_mfma_f32_16x16x32_bf16 v[20:23], v[148:151], a[148:151], v[20:23]\n"
"ds_read_b64_tr_b16 v[142:143], v171 offset:36352\n"
"v_mfma_f32_16x16x32_bf16 v[24:27], v[144:147], a[136:139], v[24:27]\n"
"v_mfma_f32_16x16x32_bf16 v[24:27], v[148:151], a[152:155], v[24:27]\n"
"v_mfma_f32_16x16x32_bf16 v[28:31], v[144:147], a[140:143], v[28:31]\n"
"v_mfma_f32_16x16x32_bf16 v[28:31], v[148:151], a[156:159], v[28:31]\n"
"s_waitcnt lgkmcnt(4)\n"
"v_mfma_f32_16x16x32_bf16 v[32:35], v[128:131], a[128:131], v[32:35]\n"
"v_mfma_f32_16x16x32_bf16 v[32:35], v[132:135], a[144:147], v[32:35]\n"
"v_mfma_f32_16x16x32_bf16 v[36:39], v[128:131], a[132:135], v[36:39]\n"
"v_mfma_f32_16x16x32_bf16 v[36:39], v[132:135], a[148:151], v[36:39]\n"
"s_waitcnt lgkmcnt(0)\n"
"s_waitcnt vmcnt(9)\n"
"s_barrier\n"
"v_mfma_f32_16x16x32_bf16 v[40:43], v[128:131], a[136:139], v[40:43]\n"
"ds_read_b64_tr_b16 v[144:145], v168 offset:0\n"
"ds_read_b128 a[160:163], v179 offset:16384\n"
"s_add_u32 m0, s91, 0x22000\n"
"v_mfma_f32_16x16x32_bf16 v[40:43], v[132:135], a[152:155], v[40:43]\n"
"ds_read_b64_tr_b16 v[146:147], v168 offset:512\n"
"ds_read_b128 a[176:179], v180 offset:16384\n"
"buffer_load_dwordx4 v181, s[36:39], s80 offen offset:0 lds\n"
"v_mfma_f32_16x16x32_bf16 v[44:47], v[128:131], a[140:143], v[44:47]\n"
"ds_read_b64_tr_b16 v[148:149], v168 offset:2048\n"
"ds_read_b128 a[164:167], v179 offset:18432\n"
"v_mfma_f32_16x16x32_bf16 v[44:47], v[132:135], a[156:159], v[44:47]\n"
"ds_read_b64_tr_b16 v[150:151], v168 offset:2560\n"
"ds_read_b128 a[180:183], v180 offset:18432\n"
"v_mfma_f32_16x16x32_bf16 v[48:51], v[136:139], a[128:131], v[48:51]\n"
"ds_read_b64_tr_b16 v[128:129], v168 offset:1024\n"
"buffer_load_dwordx4 v182, s[36:39], s80 offen offset:1024 lds\n"
"v_mfma_f32_16x16x32_bf16 v[48:51], v[140:143], a[144:147], v[48:51]\n"
"ds_read_b64_tr_b16 v[130:131], v168 offset:1536\n"
"v_mfma_f32_16x16x32_bf16 v[52:55], v[136:139], a[132:135], v[52:55]\n"
"ds_read_b64_tr_b16 v[132:133], v168 offset:3072\n"
"v_mfma_f32_16x16x32_bf16 v[52:55], v[140:143], a[148:151], v[52:55]\n"
"ds_read_b64_tr_b16 v[134:135], v168 offset:3584\n"
"buffer_load_dwordx4 v183, s[36:39], s80 offen offset:2048 lds\n"
"v_mfma_f32_16x16x32_bf16 v[56:59], v[136:139], a[136:139], v[56:59]\n"
"ds_read_b128 a[168:171], v179 offset:20480\n"
"v_mfma_f32_16x16x32_bf16 v[56:59], v[140:143], a[152:155], v[56:59]\n"
"ds_read_b128 a[184:187], v180 offset:20480\n"
"v_mfma_f32_16x16x32_bf16 v[60:63], v[136:139], a[140:143], v[60:63]\n"
"ds_read_b128 a[172:175], v179 offset:22528\n"
"buffer_load_dwordx4 v184, s[36:39], s80 offen offset:3072 lds\n"
"v_mfma_f32_16x16x32_bf16 v[60:63], v[140:143], a[156:159], v[60:63]\n"
"s_waitcnt lgkmcnt(14)\n"
"ds_read_b128 a[188:191], v180 offset:22528\n"
"s_waitcnt lgkmcnt(9)\n"
"v_mfma_f32_16x16x32_bf16 v[0:3], v[144:147], a[160:163], v[0:3]\n"
"ds_read_b64_tr_b16 v[136:137], v170 offset:0\n"
"s_add_u32 m0, s91, 0x8000\n"
"v_mfma_f32_16x16x32_bf16 v[0:3], v[148:151], a[176:179], v[0:3]\n"
"ds_read_b64_tr_b16 v[138:139], v170 offset:512\n"
"buffer_load_dwordx4 v176, s[40:43], s71 offen offset:0 lds\n"
"v_mfma_f32_16x16x32_bf16 v[4:7], v[144:147], a[164:167], v[4:7]\n"
"ds_read_b64_tr_b16 v[140:141], v170 offset:2048\n"
"s_waitcnt lgkmcnt(11)\n"
"v_mfma_f32_16x16x32_bf16 v[4:7], v[148:151], a[180:183], v[4:7]\n"
"ds_read_b64_tr_b16 v[142:143], v170 offset:2560\n"
"s_waitcnt lgkmcnt(7)\n"
"v_mfma_f32_16x16x32_bf16 v[8:11], v[144:147], a[168:171], v[8:11]\n"
"buffer_load_dwordx4 v176, s[44:47], s71 offen offset:1024 lds\n"
"s_waitcnt lgkmcnt(6)\n"
"v_mfma_f32_16x16x32_bf16 v[8:11], v[148:151], a[184:187], v[8:11]\n"
"s_waitcnt lgkmcnt(5)\n"
"v_mfma_f32_16x16x32_bf16 v[12:15], v[144:147], a[172:175], v[12:15]\n"
"s_waitcnt lgkmcnt(4)\n"
"v_mfma_f32_16x16x32_bf16 v[12:15], v[148:151], a[188:191], v[12:15]\n"
"buffer_load_dwordx4 v177, s[40:43], s71 offen offset:2048 lds\n"
"v_mfma_f32_16x16x32_bf16 v[16:19], v[128:131], a[160:163], v[16:19]\n"
"ds_read_b64_tr_b16 v[144:145], v170 offset:1024\n"
"v_mfma_f32_16x16x32_bf16 v[16:19], v[132:135], a[176:179], v[16:19]\n"
"ds_read_b64_tr_b16 v[146:147], v170 offset:1536\n"
"v_mfma_f32_16x16x32_bf16 v[20:23], v[128:131], a[164:167], v[20:23]\n"
"ds_read_b64_tr_b16 v[148:149], v170 offset:3072\n"
"buffer_load_dwordx4 v177, s[44:47], s71 offen offset:3072 lds\n"
"v_mfma_f32_16x16x32_bf16 v[20:23], v[132:135], a[180:183], v[20:23]\n"
"ds_read_b64_tr_b16 v[150:151], v170 offset:3584\n"
"v_mfma_f32_16x16x32_bf16 v[24:27], v[128:131], a[168:171], v[24:27]\n"
"v_mfma_f32_16x16x32_bf16 v[24:27], v[132:135], a[184:187], v[24:27]\n"
"v_mfma_f32_16x16x32_bf16 v[28:31], v[128:131], a[172:175], v[28:31]\n"
"v_mfma_f32_16x16x32_bf16 v[28:31], v[132:135], a[188:191], v[28:31]\n"
"s_waitcnt lgkmcnt(4)\n"
"v_mfma_f32_16x16x32_bf16 v[32:35], v[136:139], a[160:163], v[32:35]\n"
"v_mfma_f32_16x16x32_bf16 v[32:35], v[140:143], a[176:179], v[32:35]\n"
"v_mfma_f32_16x16x32_bf16 v[36:39], v[136:139], a[164:167], v[36:39]\n"
"v_mfma_f32_16x16x32_bf16 v[36:39], v[140:143], a[180:183], v[36:39]\n"
"s_waitcnt lgkmcnt(0)\n"
"s_waitcnt vmcnt(8)\n"
"s_barrier\n"
"v_mfma_f32_16x16x32_bf16 v[40:43], v[136:139], a[168:171], v[40:43]\n"
"ds_read_b64_tr_b16 v[128:129], v168 offset:16384\n"
"ds_read_b128 a[192:195], v179 offset:32768\n"
"s_add_u32 m0, s91, 0x16000\n"
"v_mfma_f32_16x16x32_bf16 v[40:43], v[140:143], a[184:187], v[40:43]\n"
"ds_read_b64_tr_b16 v[130:131], v168 offset:16896\n"
"ds_read_b128 a[208:211], v180 offset:32768\n"
"buffer_load_dwordx4 v181, s[36:39], s81 offen offset:0 lds\n"
"v_mfma_f32_16x16x32_bf16 v[44:47], v[136:139], a[172:175], v[44:47]\n"
"ds_read_b64_tr_b16 v[132:133], v168 offset:18432\n"
"ds_read_b128 a[196:199], v179 offset:34816\n"
"v_mfma_f32_16x16x32_bf16 v[44:47], v[140:143], a[188:191], v[44:47]\n"
"ds_read_b64_tr_b16 v[134:135], v168 offset:18944\n"
"ds_read_b128 a[212:215], v180 offset:34816\n"
"v_mfma_f32_16x16x32_bf16 v[48:51], v[144:147], a[160:163], v[48:51]\n"
"ds_read_b64_tr_b16 v[136:137], v168 offset:17408\n"
"buffer_load_dwordx4 v182, s[36:39], s81 offen offset:1024 lds\n"
"v_mfma_f32_16x16x32_bf16 v[48:51], v[148:151], a[176:179], v[48:51]\n"
"ds_read_b64_tr_b16 v[138:139], v168 offset:17920\n"
"v_mfma_f32_16x16x32_bf16 v[52:55], v[144:147], a[164:167], v[52:55]\n"
"ds_read_b64_tr_b16 v[140:141], v168 offset:19456\n"
"v_mfma_f32_16x16x32_bf16 v[52:55], v[148:151], a[180:183], v[52:55]\n"
"ds_read_b64_tr_b16 v[142:143], v168 offset:19968\n"
"buffer_load_dwordx4 v183, s[36:39], s81 offen offset:2048 lds\n"
"v_mfma_f32_16x16x32_bf16 v[56:59], v[144:147], a[168:171], v[56:59]\n"
"ds_read_b128 a[200:203], v179 offset:36864\n"
"v_mfma_f32_16x16x32_bf16 v[56:59], v[148:151], a[184:187], v[56:59]\n"
"ds_read_b128 a[216:219], v180 offset:36864\n"
"v_mfma_f32_16x16x32_bf16 v[60:63], v[144:147], a[172:175], v[60:63]\n"
"ds_read_b128 a[204:207], v179 offset:38912\n"
"buffer_load_dwordx4 v184, s[36:39], s81 offen offset:3072 lds\n"
"v_mfma_f32_16x16x32_bf16 v[60:63], v[148:151], a[188:191], v[60:63]\n"
"s_waitcnt lgkmcnt(14)\n"
"ds_read_b128 a[220:223], v180 offset:38912\n"
"s_waitcnt lgkmcnt(9)\n"
"v_mfma_f32_16x16x32_bf16 v[0:3], v[128:131], a[192:195], v[0:3]\n"
"ds_read_b64_tr_b16 v[144:145], v170 offset:16384\n"
"s_add_u32 m0, s91, 0xc000\n"
"v_mfma_f32_16x16x32_bf16 v[0:3], v[132:135], a[208:211], v[0:3]\n"
"ds_read_b64_tr_b16 v[146:147], v170 offset:16896\n"
"buffer_load_dwordx4 v176, s[40:43], s72 offen offset:0 lds\n"
"v_mfma_f32_16x16x32_bf16 v[4:7], v[128:131], a[196:199], v[4:7]\n"
"ds_read_b64_tr_b16 v[148:149], v170 offset:18432\n"
"s_waitcnt lgkmcnt(11)\n"
"v_mfma_f32_16x16x32_bf16 v[4:7], v[132:135], a[212:215], v[4:7]\n"
"ds_read_b64_tr_b16 v[150:151], v170 offset:18944\n"
"s_waitcnt lgkmcnt(7)\n"
"v_mfma_f32_16x16x32_bf16 v[8:11], v[128:131], a[200:203], v[8:11]\n"
"buffer_load_dwordx4 v176, s[44:47], s72 offen offset:1024 lds\n"
"s_waitcnt lgkmcnt(6)\n"
"v_mfma_f32_16x16x32_bf16 v[8:11], v[132:135], a[216:219], v[8:11]\n"
"s_waitcnt lgkmcnt(5)\n"
"v_mfma_f32_16x16x32_bf16 v[12:15], v[128:131], a[204:207], v[12:15]\n"
"s_waitcnt lgkmcnt(4)\n"
"v_mfma_f32_16x16x32_bf16 v[12:15], v[132:135], a[220:223], v[12:15]\n"
"buffer_load_dwordx4 v177, s[40:43], s72 offen offset:2048 lds\n"
"v_mfma_f32_16x16x32_bf16 v[16:19], v[136:139], a[192:195], v[16:19]\n"
"ds_read_b64_tr_b16 v[128:129], v170 offset:17408\n"
"v_mfma_f32_16x16x32_bf16 v[16:19], v[140:143], a[208:211], v[16:19]\n"
"ds_read_b64_tr_b16 v[130:131], v170 offset:17920\n"
"v_mfma_f32_16x16x32_bf16 v[20:23], v[136:139], a[196:199], v[20:23]\n"
"ds_read_b64_tr_b16 v[132:133], v170 offset:19456\n"
"buffer_load_dwordx4 v177, s[44:47], s72 offen offset:3072 lds\n"
"v_mfma_f32_16x16x32_bf16 v[20:23], v[140:143], a[212:215], v[20:23]\n"
"ds_read_b64_tr_b16 v[134:135], v170 offset:19968\n"
"v_mfma_f32_16x16x32_bf16 v[24:27], v[136:139], a[200:203], v[24:27]\n"
"v_mfma_f32_16x16x32_bf16 v[24:27], v[140:143], a[216:219], v[24:27]\n"
"v_mfma_f32_16x16x32_bf16 v[28:31], v[136:139], a[204:207], v[28:31]\n"
"v_mfma_f32_16x16x32_bf16 v[28:31], v[140:143], a[220:223], v[28:31]\n"
"s_waitcnt lgkmcnt(4)\n"
"v_mfma_f32_16x16x32_bf16 v[32:35], v[144:147], a[192:195], v[32:35]\n"
"v_mfma_f32_16x16x32_bf16 v[32:35], v[148:151], a[208:211], v[32:35]\n"
"v_mfma_f32_16x16x32_bf16 v[36:39], v[144:147], a[196:199], v[36:39]\n"
"v_mfma_f32_16x16x32_bf16 v[36:39], v[148:151], a[212:215], v[36:39]\n"
"s_waitcnt lgkmcnt(0)\n"
"s_waitcnt vmcnt(8)\n"
"s_barrier\n"
"v_mfma_f32_16x16x32_bf16 v[40:43], v[144:147], a[200:203], v[40:43]\n"
"ds_read_b64_tr_b16 v[136:137], v169 offset:0\n"
"ds_read_b128 a[224:227], v179 offset:49152\n"
"v_mfma_f32_16x16x32_bf16 v[40:43], v[148:151], a[216:219], v[40:43]\n"
"ds_read_b64_tr_b16 v[138:139], v169 offset:512\n"
"ds_read_b128 a[240:243], v180 offset:49152\n"
"v_mfma_f32_16x16x32_bf16 v[44:47], v[144:147], a[204:207], v[44:47]\n"
"ds_read_b64_tr_b16 v[140:141], v169 offset:2048\n"
"ds_read_b128 a[228:231], v179 offset:51200\n"
"v_mfma_f32_16x16x32_bf16 v[44:47], v[148:151], a[220:223], v[44:47]\n"
"ds_read_b64_tr_b16 v[142:143], v169 offset:2560\n"
"ds_read_b128 a[244:247], v180 offset:51200\n"
"v_add_u32 v176, 0x80, v176\n"
"v_add_u32 v177, 0x80, v177\n"
"s_add_u32 m0, s91, 0x10000\n"
"v_mfma_f32_16x16x32_bf16 v[48:51], v[128:131], a[192:195], v[48:51]\n"
"ds_read_b64_tr_b16 v[144:145], v169 offset:1024\n"
"buffer_load_dwordx4 v176, s[40:43], s64 offen offset:0 lds\n"
"v_mfma_f32_16x16x32_bf16 v[48:51], v[132:135], a[208:211], v[48:51]\n"
"ds_read_b64_tr_b16 v[146:147], v169 offset:1536\n"
"v_mfma_f32_16x16x32_bf16 v[52:55], v[128:131], a[196:199], v[52:55]\n"
"ds_read_b64_tr_b16 v[148:149], v169 offset:3072\n"
"v_mfma_f32_16x16x32_bf16 v[52:55], v[132:135], a[212:215], v[52:55]\n"
"ds_read_b64_tr_b16 v[150:151], v169 offset:3584\n"
"v_mfma_f32_16x16x32_bf16 v[56:59], v[128:131], a[200:203], v[56:59]\n"
"ds_read_b128 a[232:235], v179 offset:53248\n"
"buffer_load_dwordx4 v176, s[44:47], s64 offen offset:1024 lds\n"
"v_mfma_f32_16x16x32_bf16 v[56:59], v[132:135], a[216:219], v[56:59]\n"
"ds_read_b128 a[248:251], v180 offset:53248\n"
"v_mfma_f32_16x16x32_bf16 v[60:63], v[128:131], a[204:207], v[60:63]\n"
"ds_read_b128 a[236:239], v179 offset:55296\n"
"v_mfma_f32_16x16x32_bf16 v[60:63], v[132:135], a[220:223], v[60:63]\n"
"s_waitcnt lgkmcnt(14)\n"
"ds_read_b128 a[252:255], v180 offset:55296\n"
"s_waitcnt lgkmcnt(9)\n"
"v_mfma_f32_16x16x32_bf16 v[0:3], v[136:139], a[224:227], v[0:3]\n"
"ds_read_b64_tr_b16 v[128:129], v171 offset:0\n"
"v_mfma_f32_16x16x32_bf16 v[0:3], v[140:143], a[240:243], v[0:3]\n"
"ds_read_b64_tr_b16 v[130:131], v171 offset:512\n"
"v_mfma_f32_16x16x32_bf16 v[4:7], v[136:139], a[228:231], v[4:7]\n"
"ds_read_b64_tr_b16 v[132:133], v171 offset:2048\n"
"s_waitcnt lgkmcnt(11)\n"
"v_mfma_f32_16x16x32_bf16 v[4:7], v[140:143], a[244:247], v[4:7]\n"
"ds_read_b64_tr_b16 v[134:135], v171 offset:2560\n"
"s_waitcnt lgkmcnt(7)\n"
"v_mfma_f32_16x16x32_bf16 v[8:11], v[136:139], a[232:235], v[8:11]\n"
"buffer_load_dwordx4 v177, s[40:43], s64 offen offset:2048 lds\n"
"s_waitcnt lgkmcnt(6)\n"
"v_mfma_f32_16x16x32_bf16 v[8:11], v[140:143], a[248:251], v[8:11]\n"
"s_waitcnt lgkmcnt(5)\n"
"v_mfma_f32_16x16x32_bf16 v[12:15], v[136:139], a[236:239], v[12:15]\n"
"s_waitcnt lgkmcnt(4)\n"
"v_mfma_f32_16x16x32_bf16 v[12:15], v[140:143], a[252:255], v[12:15]\n"
"v_mfma_f32_16x16x32_bf16 v[16:19], v[144:147], a[224:227], v[16:19]\n"
"ds_read_b64_tr_b16 v[136:137], v171 offset:1024\n"
"v_mfma_f32_16x16x32_bf16 v[16:19], v[148:151], a[240:243], v[16:19]\n"
"ds_read_b64_tr_b16 v[138:139], v171 offset:1536\n"
"v_mfma_f32_16x16x32_bf16 v[20:23], v[144:147], a[228:231], v[20:23]\n"
"ds_read_b64_tr_b16 v[140:141], v171 offset:3072\n"
"v_mfma_f32_16x16x32_bf16 v[20:23], v[148:151], a[244:247], v[20:23]\n"
"ds_read_b64_tr_b16 v[142:143], v171 offset:3584\n"
"v_mfma_f32_16x16x32_bf16 v[24:27], v[144:147], a[232:235], v[24:27]\n"
"buffer_load_dwordx4 v177, s[44:47], s64 offen offset:3072 lds\n"
"v_mfma_f32_16x16x32_bf16 v[24:27], v[148:151], a[248:251], v[24:27]\n"
"v_mfma_f32_16x16x32_bf16 v[28:31], v[144:147], a[236:239], v[28:31]\n"
"v_mfma_f32_16x16x32_bf16 v[28:31], v[148:151], a[252:255], v[28:31]\n"
"s_waitcnt lgkmcnt(4)\n"
"v_mfma_f32_16x16x32_bf16 v[32:35], v[128:131], a[224:227], v[32:35]\n"
"v_mfma_f32_16x16x32_bf16 v[32:35], v[132:135], a[240:243], v[32:35]\n"
"v_mfma_f32_16x16x32_bf16 v[36:39], v[128:131], a[228:231], v[36:39]\n"
"v_mfma_f32_16x16x32_bf16 v[36:39], v[132:135], a[244:247], v[36:39]\n"
"s_waitcnt lgkmcnt(0)\n"
"s_waitcnt vmcnt(4)\n"
"s_barrier\n"
"v_mfma_f32_16x16x32_bf16 v[40:43], v[128:131], a[232:235], v[40:43]\n"
"ds_read_b64_tr_b16 v[144:145], v169 offset:16384\n"
"ds_read_b128 v[224:227], v179 offset:0\n"
"v_mfma_f32_16x16x32_bf16 v[40:43], v[132:135], a[248:251], v[40:43]\n"
"ds_read_b64_tr_b16 v[146:147], v169 offset:16896\n"
"ds_read_b128 v[240:243], v180 offset:0\n"
"v_mfma_f32_16x16x32_bf16 v[44:47], v[128:131], a[236:239], v[44:47]\n"
"ds_read_b64_tr_b16 v[148:149], v169 offset:18432\n"
"ds_read_b128 v[228:231], v179 offset:2048\n"
"v_mfma_f32_16x16x32_bf16 v[44:47], v[132:135], a[252:255], v[44:47]\n"
"ds_read_b64_tr_b16 v[150:151], v169 offset:18944\n"
"ds_read_b128 v[244:247], v180 offset:2048\n"
"s_add_u32 m0, s91, 0\n"
"v_mfma_f32_16x16x32_bf16 v[48:51], v[136:139], a[224:227], v[48:51]\n"
"ds_read_b64_tr_b16 v[128:129], v169 offset:17408\n"
"buffer_load_dwordx4 v176, s[40:43], s65 offen offset:0 lds\n"
"v_mfma_f32_16x16x32_bf16 v[48:51], v[140:143], a[240:243], v[48:51]\n"
"ds_read_b64_tr_b16 v[130:131], v169 offset:17920\n"
"v_mfma_f32_16x16x32_bf16 v[52:55], v[136:139], a[228:231], v[52:55]\n"
"ds_read_b64_tr_b16 v[132:133], v169 offset:19456\n"
"v_mfma_f32_16x16x32_bf16 v[52:55], v[140:143], a[244:247], v[52:55]\n"
"ds_read_b64_tr_b16 v[134:135], v169 offset:19968\n"
"v_mfma_f32_16x16x32_bf16 v[56:59], v[136:139], a[232:235], v[56:59]\n"
"ds_read_b128 v[232:235], v179 offset:4096\n"
"buffer_load_dwordx4 v176, s[44:47], s65 offen offset:1024 lds\n"
"v_mfma_f32_16x16x32_bf16 v[56:59], v[140:143], a[248:251], v[56:59]\n"
"ds_read_b128 v[248:251], v180 offset:4096\n"
"v_mfma_f32_16x16x32_bf16 v[60:63], v[136:139], a[236:239], v[60:63]\n"
"ds_read_b128 v[236:239], v179 offset:6144\n"
"v_mfma_f32_16x16x32_bf16 v[60:63], v[140:143], a[252:255], v[60:63]\n"
"s_waitcnt lgkmcnt(14)\n"
"ds_read_b128 v[252:255], v180 offset:6144\n"
"s_waitcnt lgkmcnt(9)\n"
"v_mfma_f32_16x16x32_bf16 v[0:3], v[144:147], v[224:227], v[0:3]\n"
"ds_read_b64_tr_b16 v[136:137], v171 offset:16384\n"
"v_mfma_f32_16x16x32_bf16 v[0:3], v[148:151], v[240:243], v[0:3]\n"
"ds_read_b64_tr_b16 v[138:139], v171 offset:16896\n"
"v_mfma_f32_16x16x32_bf16 v[4:7], v[144:147], v[228:231], v[4:7]\n"
"ds_read_b64_tr_b16 v[140:141], v171 offset:18432\n"
"s_waitcnt lgkmcnt(11)\n"
"v_mfma_f32_16x16x32_bf16 v[4:7], v[148:151], v[244:247], v[4:7]\n"
"ds_read_b64_tr_b16 v[142:143], v171 offset:18944\n"
"s_waitcnt lgkmcnt(7)\n"
"v_mfma_f32_16x16x32_bf16 v[8:11], v[144:147], v[232:235], v[8:11]\n"
"buffer_load_dwordx4 v177, s[40:43], s65 offen offset:2048 lds\n"
"s_waitcnt lgkmcnt(6)\n"
"v_mfma_f32_16x16x32_bf16 v[8:11], v[148:151], v[248:251], v[8:11]\n"
"s_waitcnt lgkmcnt(5)\n"
"v_mfma_f32_16x16x32_bf16 v[12:15], v[144:147], v[236:239], v[12:15]\n"
"s_waitcnt lgkmcnt(4)\n"
"v_mfma_f32_16x16x32_bf16 v[12:15], v[148:151], v[252:255], v[12:15]\n"
"v_mfma_f32_16x16x32_bf16 v[16:19], v[128:131], v[224:227], v[16:19]\n"
"ds_read_b64_tr_b16 v[144:145], v171 offset:17408\n"
"v_mfma_f32_16x16x32_bf16 v[16:19], v[132:135], v[240:243], v[16:19]\n"
"ds_read_b64_tr_b16 v[146:147], v171 offset:17920\n"
"v_mfma_f32_16x16x32_bf16 v[20:23], v[128:131], v[228:231], v[20:23]\n"
"ds_read_b64_tr_b16 v[148:149], v171 offset:19456\n"
"v_mfma_f32_16x16x32_bf16 v[20:23], v[132:135], v[244:247], v[20:23]\n"
"ds_read_b64_tr_b16 v[150:151], v171 offset:19968\n"
"v_mfma_f32_16x16x32_bf16 v[24:27], v[128:131], v[232:235], v[24:27]\n"
"buffer_load_dwordx4 v177, s[44:47], s65 offen offset:3072 lds\n"
"v_mfma_f32_16x16x32_bf16 v[24:27], v[132:135], v[248:251], v[24:27]\n"
"v_mfma_f32_16x16x32_bf16 v[28:31], v[128:131], v[236:239], v[28:31]\n"
"v_mfma_f32_16x16x32_bf16 v[28:31], v[132:135], v[252:255], v[28:31]\n"
"s_waitcnt lgkmcnt(4)\n"
"v_mfma_f32_16x16x32_bf16 v[32:35], v[136:139], v[224:227], v[32:35]\n"
"v_mfma_f32_16x16x32_bf16 v[32:35], v[140:143], v[240:243], v[32:35]\n"
"v_mfma_f32_16x16x32_bf16 v[36:39], v[136:139], v[228:231], v[36:39]\n"
"v_mfma_f32_16x16x32_bf16 v[36:39], v[140:143], v[244:247], v[36:39]\n"
"s_waitcnt lgkmcnt(0)\n"
"s_waitcnt vmcnt(4)\n"
"s_barrier\n"
"v_mfma_f32_16x16x32_bf16 v[40:43], v[136:139], v[232:235], v[40:43]\n"
"ds_read_b64_tr_b16 v[128:129], v169 offset:32768\n"
"s_add_u32 m0, s91, 0x4000\n"
"v_mfma_f32_16x16x32_bf16 v[40:43], v[140:143], v[248:251], v[40:43]\n"
"ds_read_b64_tr_b16 v[130:131], v169 offset:33280\n"
"buffer_load_dwordx4 v176, s[40:43], s66 offen offset:0 lds\n"
"v_mfma_f32_16x16x32_bf16 v[44:47], v[136:139], v[236:239], v[44:47]\n"
"ds_read_b64_tr_b16 v[132:133], v169 offset:34816\n"
"v_mfma_f32_16x16x32_bf16 v[44:47], v[140:143], v[252:255], v[44:47]\n"
"ds_read_b64_tr_b16 v[134:135], v169 offset:35328\n"
"v_mfma_f32_16x16x32_bf16 v[48:51], v[144:147], v[224:227], v[48:51]\n"
"ds_read_b64_tr_b16 v[136:137], v169 offset:33792\n"
"v_mfma_f32_16x16x32_bf16 v[48:51], v[148:151], v[240:243], v[48:51]\n"
"ds_read_b64_tr_b16 v[138:139], v169 offset:34304\n"
"v_mfma_f32_16x16x32_bf16 v[52:55], v[144:147], v[228:231], v[52:55]\n"
"ds_read_b64_tr_b16 v[140:141], v169 offset:35840\n"
"v_mfma_f32_16x16x32_bf16 v[52:55], v[148:151], v[244:247], v[52:55]\n"
"ds_read_b64_tr_b16 v[142:143], v169 offset:36352\n"
"v_mfma_f32_16x16x32_bf16 v[56:59], v[144:147], v[232:235], v[56:59]\n"
"buffer_load_dwordx4 v176, s[44:47], s66 offen offset:1024 lds\n"
"v_mfma_f32_16x16x32_bf16 v[56:59], v[148:151], v[248:251], v[56:59]\n"
"v_mfma_f32_16x16x32_bf16 v[60:63], v[144:147], v[236:239], v[60:63]\n"
"v_mfma_f32_16x16x32_bf16 v[60:63], v[148:151], v[252:255], v[60:63]\n"
"s_waitcnt lgkmcnt(4)\n"
"v_mfma_f32_16x16x32_bf16 v[64:67], v[128:131], a[0:3], 0\n"
"ds_read_b64_tr_b16 v[144:145], v171 offset:32768\n"
"v_mfma_f32_16x16x32_bf16 v[64:67], v[132:135], a[16:19], v[64:67]\n"
"ds_read_b64_tr_b16 v[146:147], v171 offset:33280\n"
"v_mul_f32 v156, v0, v0\n"
"v_mul_f32 v157, v1, v1\n"
"v_mfma_f32_16x16x32_bf16 v[68:71], v[128:131], a[4:7], 0\n"
"ds_read_b64_tr_b16 v[148:149], v171 offset:34816\n"
"v_mul_f32 v158, v2, v2\n"
"v_mfma_f32_16x16x32_bf16 v[68:71], v[132:135], a[20:23], v[68:71]\n"
"ds_read_b64_tr_b16 v[150:151], v171 offset:35328\n"
"v_mul_f32 v159, v3, v3\n"
"v_mfma_f32_16x16x32_bf16 v[72:75], v[128:131], a[8:11], 0\n"
"buffer_load_dwordx4 v177, s[40:43], s66 offen offset:2048 lds\n"
"v_mfma_f32_16x16x32_bf16 v[72:75], v[132:135], a[24:27], v[72:75]\n"
"v_fma_f32 v156, v156, s94, v193\n"
"v_fma_f32 v157, v157, s94, v193\n"
"v_mfma_f32_16x16x32_bf16 v[76:79], v[128:131], a[12:15], 0\n"
"v_fma_f32 v158, v158, s94, v193\n"
"v_fma_f32 v159, v159, s94, v193\n"
"v_mfma_f32_16x16x32_bf16 v[76:79], v[132:135], a[28:31], v[76:79]\n"
"v_mul_f32 v156, v156, v0\n"
"v_mul_f32 v157, v157, v1\n"
"s_waitcnt lgkmcnt(4)\n"
"v_mfma_f32_16x16x32_bf16 v[80:83], v[136:139], a[0:3], 0\n"
"ds_read_b64_tr_b16 v[128:129], v171 offset:33792\n"
"v_mul_f32 v158, v158, v2\n"
"v_mfma_f32_16x16x32_bf16 v[80:83], v[140:143], a[16:19], v[80:83]\n"
"ds_read_b64_tr_b16 v[130:131], v171 offset:34304\n"
"v_mul_f32 v159, v159, v3\n"
"v_mfma_f32_16x16x32_bf16 v[84:87], v[136:139], a[4:7], 0\n"
"ds_read_b64_tr_b16 v[132:133], v171 offset:35840\n"
"v_mul_f32 v16, v0, v16\n"
"v_mfma_f32_16x16x32_bf16 v[84:87], v[140:143], a[20:23], v[84:87]\n"
"ds_read_b64_tr_b16 v[134:135], v171 offset:36352\n"
"v_mul_f32 v17, v1, v17\n"
"v_mfma_f32_16x16x32_bf16 v[88:91], v[136:139], a[8:11], 0\n"
"buffer_load_dwordx4 v177, s[44:47], s66 offen offset:3072 lds\n"
"v_mfma_f32_16x16x32_bf16 v[88:91], v[140:143], a[24:27], v[88:91]\n"
"v_exp_f32 v156, v156\n"
"v_mfma_f32_16x16x32_bf16 v[92:95], v[136:139], a[12:15], 0\n"
"v_exp_f32 v157, v157\n"
"v_mfma_f32_16x16x32_bf16 v[92:95], v[140:143], a[28:31], v[92:95]\n"
"v_exp_f32 v158, v158\n"
"s_waitcnt lgkmcnt(4)\n"
"v_mfma_f32_16x16x32_bf16 v[96:99], v[144:147], a[0:3], 0\n"
"v_exp_f32 v159, v159\n"
"v_mfma_f32_16x16x32_bf16 v[96:99], v[148:151], a[16:19], v[96:99]\n"
"v_mul_f32 v18, v2, v18\n"
"v_mul_f32 v19, v3, v19\n"
"v_mfma_f32_16x16x32_bf16 v[100:103], v[144:147], a[4:7], 0\n"
"v_add_f32 v156, 1.0, v156\n"
"v_add_f32 v157, 1.0, v157\n"
"v_mfma_f32_16x16x32_bf16 v[100:103], v[148:151], a[20:23], v[100:103]\n"
"s_waitcnt lgkmcnt(0)\n"
"s_waitcnt vmcnt(4)\n"
"s_barrier\n"
"v_add_f32 v158, 1.0, v158\n"
"v_add_f32 v159, 1.0, v159\n"
"v_mfma_f32_16x16x32_bf16 v[104:107], v[144:147], a[8:11], 0\n"
"ds_read_b64_tr_b16 v[136:137], v168 offset:0\n"
"s_add_u32 m0, s91, 0x8000\n"
"v_mul_f32 v216, v32, v32\n"
"v_mfma_f32_16x16x32_bf16 v[104:107], v[148:151], a[24:27], v[104:107]\n"
"ds_read_b64_tr_b16 v[138:139], v168 offset:512\n"
"buffer_load_dwordx4 v176, s[40:43], s67 offen offset:0 lds\n"
"v_mfma_f32_16x16x32_bf16 v[108:111], v[144:147], a[12:15], 0\n"
"ds_read_b64_tr_b16 v[140:141], v168 offset:2048\n"
"v_mul_f32 v217, v33, v33\n"
"v_mfma_f32_16x16x32_bf16 v[108:111], v[148:151], a[28:31], v[108:111]\n"
"ds_read_b64_tr_b16 v[142:143], v168 offset:2560\n"
"v_mul_f32 v218, v34, v34\n"
"v_mfma_f32_16x16x32_bf16 v[112:115], v[128:131], a[0:3], 0\n"
"ds_read_b64_tr_b16 v[144:145], v168 offset:1024\n"
"buffer_load_dwordx4 v176, s[44:47], s67 offen offset:1024 lds\n"
"v_mfma_f32_16x16x32_bf16 v[112:115], v[132:135], a[16:19], v[112:115]\n"
"ds_read_b64_tr_b16 v[146:147], v168 offset:1536\n"
"v_mul_f32 v219, v35, v35\n"
"v_mfma_f32_16x16x32_bf16 v[116:119], v[128:131], a[4:7], 0\n"
"ds_read_b64_tr_b16 v[148:149], v168 offset:3072\n"
"v_fma_f32 v216, v216, s94, v193\n"
"v_mfma_f32_16x16x32_bf16 v[116:119], v[132:135], a[20:23], v[116:119]\n"
"ds_read_b64_tr_b16 v[150:151], v168 offset:3584\n"
"v_fma_f32 v217, v217, s94, v193\n"
"v_mfma_f32_16x16x32_bf16 v[120:123], v[128:131], a[8:11], 0\n"
"buffer_load_dwordx4 v177, s[40:43], s67 offen offset:2048 lds\n"
"v_mfma_f32_16x16x32_bf16 v[120:123], v[132:135], a[24:27], v[120:123]\n"
"v_rcp_f32 v156, v156\n"
"v_mfma_f32_16x16x32_bf16 v[124:127], v[128:131], a[12:15], 0\n"
"buffer_load_dwordx4 v177, s[44:47], s67 offen offset:3072 lds\n"
"v_mfma_f32_16x16x32_bf16 v[124:127], v[132:135], a[28:31], v[124:127]\n"
"v_rcp_f32 v157, v157\n"
"s_waitcnt lgkmcnt(4)\n"
"v_mfma_f32_16x16x32_bf16 v[64:67], v[136:139], a[32:35], v[64:67]\n"
"ds_read_b64_tr_b16 v[128:129], v170 offset:0\n"
"s_add_u32 m0, s91, 0xc000\n"
"v_mul_f32 v16, v16, v156\n"
"v_mfma_f32_16x16x32_bf16 v[64:67], v[140:143], a[48:51], v[64:67]\n"
"ds_read_b64_tr_b16 v[130:131], v170 offset:512\n"
"buffer_load_dwordx4 v176, s[40:43], s68 offen offset:0 lds\n"
"v_mfma_f32_16x16x32_bf16 v[68:71], v[136:139], a[36:39], v[68:71]\n"
"ds_read_b64_tr_b16 v[132:133], v170 offset:2048\n"
"v_rcp_f32 v158, v158\n"
"v_mfma_f32_16x16x32_bf16 v[68:71], v[140:143], a[52:55], v[68:71]\n"
"ds_read_b64_tr_b16 v[134:135], v170 offset:2560\n"
"v_mul_f32 v17, v17, v157\n"
"v_mfma_f32_16x16x32_bf16 v[72:75], v[136:139], a[40:43], v[72:75]\n"
"buffer_load_dwordx4 v176, s[44:47], s68 offen offset:1024 lds\n"
"v_mfma_f32_16x16x32_bf16 v[72:75], v[140:143], a[56:59], v[72:75]\n"
"v_rcp_f32 v159, v159\n"
"v_mfma_f32_16x16x32_bf16 v[76:79], v[136:139], a[44:47], v[76:79]\n"
"v_mul_f32 v18, v18, v158\n"
"v_mul_f32 v19, v19, v159\n"
"v_mfma_f32_16x16x32_bf16 v[76:79], v[140:143], a[60:63], v[76:79]\n"
"buffer_load_dwordx4 v177, s[40:43], s68 offen offset:2048 lds\n"
"s_waitcnt lgkmcnt(4)\n"
"v_mfma_f32_16x16x32_bf16 v[80:83], v[144:147], a[32:35], v[80:83]\n"
"ds_read_b64_tr_b16 v[136:137], v170 offset:1024\n"
"v_cvt_pk_bf16_f32 v160, v16, v17\n"
"v_mfma_f32_16x16x32_bf16 v[80:83], v[148:151], a[48:51], v[80:83]\n"
"ds_read_b64_tr_b16 v[138:139], v170 offset:1536\n"
"v_cvt_pk_bf16_f32 v161, v18, v19\n"
"v_mfma_f32_16x16x32_bf16 v[84:87], v[144:147], a[36:39], v[84:87]\n"
"ds_read_b64_tr_b16 v[140:141], v170 offset:3072\n"
"buffer_load_dwordx4 v177, s[44:47], s68 offen offset:3072 lds\n"
"v_mfma_f32_16x16x32_bf16 v[84:87], v[148:151], a[52:55], v[84:87]\n"
"ds_read_b64_tr_b16 v[142:143], v170 offset:3584\n"
"v_fma_f32 v218, v218, s94, v193\n"
"v_mfma_f32_16x16x32_bf16 v[88:91], v[144:147], a[40:43], v[88:91]\n"
"v_fma_f32 v219, v219, s94, v193\n"
"v_mul_f32 v216, v216, v32\n"
"v_mfma_f32_16x16x32_bf16 v[88:91], v[148:151], a[56:59], v[88:91]\n"
"v_mul_f32 v217, v217, v33\n"
"v_mul_f32 v218, v218, v34\n"
"v_mfma_f32_16x16x32_bf16 v[92:95], v[144:147], a[44:47], v[92:95]\n"
"v_mul_f32 v219, v219, v35\n"
"v_mul_f32 v48, v32, v48\n"
"v_mfma_f32_16x16x32_bf16 v[92:95], v[148:151], a[60:63], v[92:95]\n"
"v_exp_f32 v216, v216\n"
"s_waitcnt lgkmcnt(4)\n"
"v_mfma_f32_16x16x32_bf16 v[96:99], v[128:131], a[32:35], v[96:99]\n"
"v_exp_f32 v217, v217\n"
"v_mfma_f32_16x16x32_bf16 v[96:99], v[132:135], a[48:51], v[96:99]\n"
"v_exp_f32 v218, v218\n"
"v_mfma_f32_16x16x32_bf16 v[100:103], v[128:131], a[36:39], v[100:103]\n"
"v_exp_f32 v219, v219\n"
"v_mfma_f32_16x16x32_bf16 v[100:103], v[132:135], a[52:55], v[100:103]\n"
"s_waitcnt lgkmcnt(0)\n"
"s_waitcnt vmcnt(8)\n"
"s_barrier\n"
"v_mul_f32 v49, v33, v49\n"
"v_mul_f32 v50, v34, v50\n"
"v_mfma_f32_16x16x32_bf16 v[104:107], v[128:131], a[40:43], v[104:107]\n"
"ds_read_b64_tr_b16 v[144:145], v168 offset:16384\n"
"s_add_u32 m0, s91, 0x10000\n"
"v_mul_f32 v51, v35, v51\n"
"v_mfma_f32_16x16x32_bf16 v[104:107], v[132:135], a[56:59], v[104:107]\n"
"ds_read_b64_tr_b16 v[146:147], v168 offset:16896\n"
"buffer_load_dwordx4 v176, s[40:43], s69 offen offset:0 lds\n"
"v_mfma_f32_16x16x32_bf16 v[108:111], v[128:131], a[44:47], v[108:111]\n"
"ds_read_b64_tr_b16 v[148:149], v168 offset:18432\n"
"v_add_f32 v216, 1.0, v216\n"
"v_mfma_f32_16x16x32_bf16 v[108:111], v[132:135], a[60:63], v[108:111]\n"
"ds_read_b64_tr_b16 v[150:151], v168 offset:18944\n"
"v_add_f32 v217, 1.0, v217\n"
"v_mfma_f32_16x16x32_bf16 v[112:115], v[136:139], a[32:35], v[112:115]\n"
"ds_read_b64_tr_b16 v[128:129], v168 offset:17408\n"
"buffer_load_dwordx4 v176, s[44:47], s69 offen offset:1024 lds\n"
"v_mfma_f32_16x16x32_bf16 v[112:115], v[140:143], a[48:51], v[112:115]\n"
"ds_read_b64_tr_b16 v[130:131], v168 offset:17920\n"
"v_add_f32 v218, 1.0, v218\n"
"v_mfma_f32_16x16x32_bf16 v[116:119], v[136:139], a[36:39], v[116:119]\n"
"ds_read_b64_tr_b16 v[132:133], v168 offset:19456\n"
"v_add_f32 v219, 1.0, v219\n"
"v_mfma_f32_16x16x32_bf16 v[116:119], v[140:143], a[52:55], v[116:119]\n"
"ds_read_b64_tr_b16 v[134:135], v168 offset:19968\n"
"v_mul_f32 v152, v4, v4\n"
"v_mfma_f32_16x16x32_bf16 v[120:123], v[136:139], a[40:43], v[120:123]\n"
"buffer_load_dwordx4 v177, s[40:43], s69 offen offset:2048 lds\n"
"v_mfma_f32_16x16x32_bf16 v[120:123], v[140:143], a[56:59], v[120:123]\n"
"v_rcp_f32 v216, v216\n"
"v_mfma_f32_16x16x32_bf16 v[124:127], v[136:139], a[44:47], v[124:127]\n"
"buffer_load_dwordx4 v177, s[44:47], s69 offen offset:3072 lds\n"
"v_mfma_f32_16x16x32_bf16 v[124:127], v[140:143], a[60:63], v[124:127]\n"
"v_rcp_f32 v217, v217\n"
"s_waitcnt lgkmcnt(4)\n"
"v_mfma_f32_16x16x32_bf16 v[64:67], v[144:147], a[64:67], v[64:67]\n"
"ds_read_b64_tr_b16 v[136:137], v170 offset:16384\n"
"s_add_u32 m0, s91, 0\n"
"v_mul_f32 v48, v48, v216\n"
"v_mfma_f32_16x16x32_bf16 v[64:67], v[148:151], a[80:83], v[64:67]\n"
"ds_read_b64_tr_b16 v[138:139], v170 offset:16896\n"
"buffer_load_dwordx4 v176, s[40:43], s70 offen offset:0 lds\n"
"v_mfma_f32_16x16x32_bf16 v[68:71], v[144:147], a[68:71], v[68:71]\n"
"ds_read_b64_tr_b16 v[140:141], v170 offset:18432\n"
"v_mul_f32 v49, v49, v217\n"
"v_mfma_f32_16x16x32_bf16 v[68:71], v[148:151], a[84:87], v[68:71]\n"
"ds_read_b64_tr_b16 v[142:143], v170 offset:18944\n"
"v_cvt_pk_bf16_f32 v162, v48, v49\n"
"v_mfma_f32_16x16x32_bf16 v[72:75], v[144:147], a[72:75], v[72:75]\n"
"buffer_load_dwordx4 v176, s[44:47], s70 offen offset:1024 lds\n"
"v_mfma_f32_16x16x32_bf16 v[72:75], v[148:151], a[88:91], v[72:75]\n"
"v_rcp_f32 v218, v218\n"
"v_mfma_f32_16x16x32_bf16 v[76:79], v[144:147], a[76:79], v[76:79]\n"
"v_rcp_f32 v219, v219\n"
"v_mfma_f32_16x16x32_bf16 v[76:79], v[148:151], a[92:95], v[76:79]\n"
"buffer_load_dwordx4 v177, s[40:43], s70 offen offset:2048 lds\n"
"s_waitcnt lgkmcnt(4)\n"
"v_mfma_f32_16x16x32_bf16 v[80:83], v[128:131], a[64:67], v[80:83]\n"
"ds_read_b64_tr_b16 v[144:145], v170 offset:17408\n"
"v_mul_f32 v50, v50, v218\n"
"v_mfma_f32_16x16x32_bf16 v[80:83], v[132:135], a[80:83], v[80:83]\n"
"ds_read_b64_tr_b16 v[146:147], v170 offset:17920\n"
"v_mul_f32 v51, v51, v219\n"
"v_mfma_f32_16x16x32_bf16 v[84:87], v[128:131], a[68:71], v[84:87]\n"
"ds_read_b64_tr_b16 v[148:149], v170 offset:19456\n"
"buffer_load_dwordx4 v177, s[44:47], s70 offen offset:3072 lds\n"
"v_mfma_f32_16x16x32_bf16 v[84:87], v[132:135], a[84:87], v[84:87]\n"
"ds_read_b64_tr_b16 v[150:151], v170 offset:19968\n"
"v_cvt_pk_bf16_f32 v163, v50, v51\n"
"v_mfma_f32_16x16x32_bf16 v[88:91], v[128:131], a[72:75], v[88:91]\n"
"s_mov_b32 s17, 0\n"
"ds_write_b128 v174, v[160:163] offset:0\n"
"v_mfma_f32_16x16x32_bf16 v[88:91], v[132:135], a[88:91], v[88:91]\n"
"buffer_load_dword v197, v194, s[60:63], s17 offen offset:256\n"
"v_mfma_f32_16x16x32_bf16 v[92:95], v[128:131], a[76:79], v[92:95]\n"
"v_mul_f32 v153, v5, v5\n"
"v_mul_f32 v154, v6, v6\n"
"v_mfma_f32_16x16x32_bf16 v[92:95], v[132:135], a[92:95], v[92:95]\n"
"v_mul_f32 v155, v7, v7\n"
"v_fma_f32 v152, v152, s94, v193\n"
"s_waitcnt lgkmcnt(5)\n"
"v_mfma_f32_16x16x32_bf16 v[96:99], v[136:139], a[64:67], v[96:99]\n"
"v_fma_f32 v153, v153, s94, v193\n"
"v_fma_f32 v154, v154, s94, v193\n"
"v_mfma_f32_16x16x32_bf16 v[96:99], v[140:143], a[80:83], v[96:99]\n"
"v_fma_f32 v155, v155, s94, v193\n"
"v_mul_f32 v152, v152, v4\n"
"v_mfma_f32_16x16x32_bf16 v[100:103], v[136:139], a[68:71], v[100:103]\n"
"v_mul_f32 v153, v153, v5\n"
"v_mul_f32 v154, v154, v6\n"
"v_mfma_f32_16x16x32_bf16 v[100:103], v[140:143], a[84:87], v[100:103]\n"
"s_waitcnt lgkmcnt(1)\n"
"s_waitcnt vmcnt(13)\n"
"s_barrier\n"
"v_mul_f32 v155, v155, v7\n"
"v_mul_f32 v20, v4, v20\n"
"v_mfma_f32_16x16x32_bf16 v[104:107], v[136:139], a[72:75], v[104:107]\n"
"ds_read_b64_tr_b16 v[128:129], v169 offset:0\n"
"s_add_u32 m0, s91, 0x4000\n"
"v_mul_f32 v21, v5, v21\n"
"v_mfma_f32_16x16x32_bf16 v[104:107], v[140:143], a[88:91], v[104:107]\n"
"ds_read_b64_tr_b16 v[130:131], v169 offset:512\n"
"buffer_load_dwordx4 v176, s[40:43], s71 offen offset:0 lds\n"
"v_mfma_f32_16x16x32_bf16 v[108:111], v[136:139], a[76:79], v[108:111]\n"
"ds_read_b64_tr_b16 v[132:133], v169 offset:2048\n"
"v_mul_f32 v22, v6, v22\n"
"v_mfma_f32_16x16x32_bf16 v[108:111], v[140:143], a[92:95], v[108:111]\n"
"ds_read_b64_tr_b16 v[134:135], v169 offset:2560\n"
"v_mul_f32 v23, v7, v23\n"
"v_mfma_f32_16x16x32_bf16 v[112:115], v[144:147], a[64:67], v[112:115]\n"
"ds_read_b64_tr_b16 v[136:137], v169 offset:1024\n"
"v_mul_f32 v156, v36, v36\n"
"v_mfma_f32_16x16x32_bf16 v[112:115], v[148:151], a[80:83], v[112:115]\n"
"ds_read_b64_tr_b16 v[138:139], v169 offset:1536\n"
"v_mul_f32 v157, v37, v37\n"
"v_mfma_f32_16x16x32_bf16 v[116:119], v[144:147], a[68:71], v[116:119]\n"
"ds_read_b64_tr_b16 v[140:141], v169 offset:3072\n"
"v_mul_f32 v158, v38, v38\n"
"v_mfma_f32_16x16x32_bf16 v[116:119], v[148:151], a[84:87], v[116:119]\n"
"ds_read_b64_tr_b16 v[142:143], v169 offset:3584\n"
"v_mul_f32 v159, v39, v39\n"
"v_mfma_f32_16x16x32_bf16 v[120:123], v[144:147], a[72:75], v[120:123]\n"
"buffer_load_dwordx4 v176, s[44:47], s71 offen offset:1024 lds\n"
"v_mfma_f32_16x16x32_bf16 v[120:123], v[148:151], a[88:91], v[120:123]\n"
"v_exp_f32 v152, v152\n"
"v_mfma_f32_16x16x32_bf16 v[124:127], v[144:147], a[76:79], v[124:127]\n"
"v_exp_f32 v153, v153\n"
"v_mfma_f32_16x16x32_bf16 v[124:127], v[148:151], a[92:95], v[124:127]\n"
"v_exp_f32 v154, v154\n"
"s_waitcnt lgkmcnt(4)\n"
"v_mfma_f32_16x16x32_bf16 v[64:67], v[128:131], a[96:99], v[64:67]\n"
"ds_read_b64_tr_b16 v[144:145], v171 offset:0\n"
"v_add_f32 v152, 1.0, v152\n"
"v_mfma_f32_16x16x32_bf16 v[64:67], v[132:135], a[112:115], v[64:67]\n"
"ds_read_b64_tr_b16 v[146:147], v171 offset:512\n"
"v_add_f32 v153, 1.0, v153\n"
"v_mfma_f32_16x16x32_bf16 v[68:71], v[128:131], a[100:103], v[68:71]\n"
"ds_read_b64_tr_b16 v[148:149], v171 offset:2048\n"
"v_add_f32 v154, 1.0, v154\n"
"v_mfma_f32_16x16x32_bf16 v[68:71], v[132:135], a[116:119], v[68:71]\n"
"ds_read_b64_tr_b16 v[150:151], v171 offset:2560\n"
"v_fma_f32 v156, v156, s94, v193\n"
"v_mfma_f32_16x16x32_bf16 v[72:75], v[128:131], a[104:107], v[72:75]\n"
"buffer_load_dwordx4 v177, s[40:43], s71 offen offset:2048 lds\n"
"v_mfma_f32_16x16x32_bf16 v[72:75], v[132:135], a[120:123], v[72:75]\n"
"v_exp_f32 v155, v155\n"
"v_mfma_f32_16x16x32_bf16 v[76:79], v[128:131], a[108:111], v[76:79]\n"
"v_add_f32 v155, 1.0, v155\n"
"v_fma_f32 v157, v157, s94, v193\n"
"v_mfma_f32_16x16x32_bf16 v[76:79], v[132:135], a[124:127], v[76:79]\n"
"v_rcp_f32 v152, v152\n"
"s_waitcnt lgkmcnt(4)\n"
"v_mfma_f32_16x16x32_bf16 v[80:83], v[136:139], a[96:99], v[80:83]\n"
"ds_read_b64_tr_b16 v[128:129], v171 offset:1024\n"
"v_mul_f32 v20, v20, v152\n"
"v_mfma_f32_16x16x32_bf16 v[80:83], v[140:143], a[112:115], v[80:83]\n"
"ds_read_b64_tr_b16 v[130:131], v171 offset:1536\n"
"v_fma_f32 v158, v158, s94, v193\n"
"v_mfma_f32_16x16x32_bf16 v[84:87], v[136:139], a[100:103], v[84:87]\n"
"ds_read_b64_tr_b16 v[132:133], v171 offset:3072\n"
"v_fma_f32 v159, v159, s94, v193\n"
"v_mfma_f32_16x16x32_bf16 v[84:87], v[140:143], a[116:119], v[84:87]\n"
"ds_read_b64_tr_b16 v[134:135], v171 offset:3584\n"
"v_mul_f32 v156, v156, v36\n"
"v_mfma_f32_16x16x32_bf16 v[88:91], v[136:139], a[104:107], v[88:91]\n"
"buffer_load_dwordx4 v177, s[44:47], s71 offen offset:3072 lds\n"
"v_mfma_f32_16x16x32_bf16 v[88:91], v[140:143], a[120:123], v[88:91]\n"
"v_rcp_f32 v153, v153\n"
"v_mfma_f32_16x16x32_bf16 v[92:95], v[136:139], a[108:111], v[92:95]\n"
"v_rcp_f32 v154, v154\n"
"v_mfma_f32_16x16x32_bf16 v[92:95], v[140:143], a[124:127], v[92:95]\n"
"v_rcp_f32 v155, v155\n"
"s_waitcnt lgkmcnt(4)\n"
"v_mfma_f32_16x16x32_bf16 v[96:99], v[144:147], a[96:99], v[96:99]\n"
"v_mul_f32 v21, v21, v153\n"
"v_mul_f32 v22, v22, v154\n"
"v_mfma_f32_16x16x32_bf16 v[96:99], v[148:151], a[112:115], v[96:99]\n"
"v_mul_f32 v23, v23, v155\n"
"v_mfma_f32_16x16x32_bf16 v[100:103], v[144:147], a[100:103], v[100:103]\n"
"v_cvt_pk_bf16_f32 v164, v20, v21\n"
"v_cvt_pk_bf16_f32 v165, v22, v23\n"
"v_mfma_f32_16x16x32_bf16 v[100:103], v[148:151], a[116:119], v[100:103]\n"
"s_waitcnt lgkmcnt(0)\n"
"s_waitcnt vmcnt(13)\n"
"s_barrier\n"
"v_mul_f32 v157, v157, v37\n"
"v_mul_f32 v158, v158, v38\n"
"v_mfma_f32_16x16x32_bf16 v[104:107], v[144:147], a[104:107], v[104:107]\n"
"ds_read_b64_tr_b16 v[136:137], v169 offset:16384\n"
"s_add_u32 m0, s91, 0x8000\n"
"v_mul_f32 v159, v159, v39\n"
"v_mfma_f32_16x16x32_bf16 v[104:107], v[148:151], a[120:123], v[104:107]\n"
"ds_read_b64_tr_b16 v[138:139], v169 offset:16896\n"
"buffer_load_dwordx4 v176, s[40:43], s72 offen offset:0 lds\n"
"v_mfma_f32_16x16x32_bf16 v[108:111], v[144:147], a[108:111], v[108:111]\n"
"ds_read_b64_tr_b16 v[140:141], v169 offset:18432\n"
"v_mul_f32 v52, v36, v52\n"
"v_mfma_f32_16x16x32_bf16 v[108:111], v[148:151], a[124:127], v[108:111]\n"
"ds_read_b64_tr_b16 v[142:143], v169 offset:18944\n"
"v_mul_f32 v53, v37, v53\n"
"v_mfma_f32_16x16x32_bf16 v[112:115], v[128:131], a[96:99], v[112:115]\n"
"ds_read_b64_tr_b16 v[144:145], v169 offset:17408\n"
"v_mul_f32 v54, v38, v54\n"
"v_mfma_f32_16x16x32_bf16 v[112:115], v[132:135], a[112:115], v[112:115]\n"
"ds_read_b64_tr_b16 v[146:147], v169 offset:17920\n"
"v_mul_f32 v55, v39, v55\n"
"v_mfma_f32_16x16x32_bf16 v[116:119], v[128:131], a[100:103], v[116:119]\n"
"ds_read_b64_tr_b16 v[148:149], v169 offset:19456\n"
"v_mul_f32 v216, v8, v8\n"
"v_mfma_f32_16x16x32_bf16 v[116:119], v[132:135], a[116:119], v[116:119]\n"
"ds_read_b64_tr_b16 v[150:151], v169 offset:19968\n"
"v_mul_f32 v217, v9, v9\n"
"v_mfma_f32_16x16x32_bf16 v[120:123], v[128:131], a[104:107], v[120:123]\n"
"buffer_load_dwordx4 v176, s[44:47], s72 offen offset:1024 lds\n"
"v_mfma_f32_16x16x32_bf16 v[120:123], v[132:135], a[120:123], v[120:123]\n"
"v_exp_f32 v156, v156\n"
"v_mfma_f32_16x16x32_bf16 v[124:127], v[128:131], a[108:111], v[124:127]\n"
"v_exp_f32 v157, v157\n"
"v_mfma_f32_16x16x32_bf16 v[124:127], v[132:135], a[124:127], v[124:127]\n"
"v_exp_f32 v158, v158\n"
"s_waitcnt lgkmcnt(4)\n"
"v_mfma_f32_16x16x32_bf16 v[64:67], v[136:139], a[128:131], v[64:67]\n"
"ds_read_b64_tr_b16 v[128:129], v171 offset:16384\n"
"v_add_f32 v156, 1.0, v156\n"
"v_mfma_f32_16x16x32_bf16 v[64:67], v[140:143], a[144:147], v[64:67]\n"
"ds_read_b64_tr_b16 v[130:131], v171 offset:16896\n"
"v_add_f32 v157, 1.0, v157\n"
"v_mfma_f32_16x16x32_bf16 v[68:71], v[136:139], a[132:135], v[68:71]\n"
"ds_read_b64_tr_b16 v[132:133], v171 offset:18432\n"
"v_add_f32 v158, 1.0, v158\n"
"v_mfma_f32_16x16x32_bf16 v[68:71], v[140:143], a[148:151], v[68:71]\n"
"ds_read_b64_tr_b16 v[134:135], v171 offset:18944\n"
"v_mul_f32 v218, v10, v10\n"
"v_mfma_f32_16x16x32_bf16 v[72:75], v[136:139], a[136:139], v[72:75]\n"
"buffer_load_dwordx4 v177, s[40:43], s72 offen offset:2048 lds\n"
"v_mfma_f32_16x16x32_bf16 v[72:75], v[140:143], a[152:155], v[72:75]\n"
"v_exp_f32 v159, v159\n"
"v_mfma_f32_16x16x32_bf16 v[76:79], v[136:139], a[140:143], v[76:79]\n"
"v_add_f32 v159, 1.0, v159\n"
"v_mul_f32 v219, v11, v11\n"
"v_mfma_f32_16x16x32_bf16 v[76:79], v[140:143], a[156:159], v[76:79]\n"
"v_rcp_f32 v156, v156\n"
"s_waitcnt lgkmcnt(4)\n"
"v_mfma_f32_16x16x32_bf16 v[80:83], v[144:147], a[128:131], v[80:83]\n"
"ds_read_b64_tr_b16 v[136:137], v171 offset:17408\n"
"v_mul_f32 v52, v52, v156\n"
"v_mfma_f32_16x16x32_bf16 v[80:83], v[148:151], a[144:147], v[80:83]\n"
"ds_read_b64_tr_b16 v[138:139], v171 offset:17920\n"
"v_fma_f32 v216, v216, s94, v193\n"
"v_mfma_f32_16x16x32_bf16 v[84:87], v[144:147], a[132:135], v[84:87]\n"
"ds_read_b64_tr_b16 v[140:141], v171 offset:19456\n"
"v_fma_f32 v217, v217, s94, v193\n"
"v_mfma_f32_16x16x32_bf16 v[84:87], v[148:151], a[148:151], v[84:87]\n"
"ds_read_b64_tr_b16 v[142:143], v171 offset:19968\n"
"v_fma_f32 v218, v218, s94, v193\n"
"v_mfma_f32_16x16x32_bf16 v[88:91], v[144:147], a[136:139], v[88:91]\n"
"buffer_load_dwordx4 v177, s[44:47], s72 offen offset:3072 lds\n"
"v_mfma_f32_16x16x32_bf16 v[88:91], v[148:151], a[152:155], v[88:91]\n"
"v_rcp_f32 v157, v157\n"
"v_mfma_f32_16x16x32_bf16 v[92:95], v[144:147], a[140:143], v[92:95]\n"
"v_rcp_f32 v158, v158\n"
"v_mfma_f32_16x16x32_bf16 v[92:95], v[148:151], a[156:159], v[92:95]\n"
"v_rcp_f32 v159, v159\n"
"s_waitcnt lgkmcnt(4)\n"
"v_mfma_f32_16x16x32_bf16 v[96:99], v[128:131], a[128:131], v[96:99]\n"
"v_mul_f32 v53, v53, v157\n"
"v_mul_f32 v54, v54, v158\n"
"v_mfma_f32_16x16x32_bf16 v[96:99], v[132:135], a[144:147], v[96:99]\n"
"v_mul_f32 v55, v55, v159\n"
"v_cvt_pk_bf16_f32 v166, v52, v53\n"
"v_mfma_f32_16x16x32_bf16 v[100:103], v[128:131], a[132:135], v[100:103]\n"
"v_cvt_pk_bf16_f32 v167, v54, v55\n"
"v_mfma_f32_16x16x32_bf16 v[100:103], v[132:135], a[148:151], v[100:103]\n"
"s_waitcnt lgkmcnt(0)\n"
"s_waitcnt vmcnt(13)\n"
"s_barrier\n"
"v_fma_f32 v219, v219, s94, v193\n"
"v_mul_f32 v216, v216, v8\n"
"v_mfma_f32_16x16x32_bf16 v[104:107], v[128:131], a[136:139], v[104:107]\n"
"ds_read_b64_tr_b16 v[144:145], v169 offset:32768\n"
"v_add_u32 v176, 0x80, v176\n"
"v_add_u32 v177, 0x80, v177\n"
"s_add_u32 m0, s91, 0xc000\n"
"v_mfma_f32_16x16x32_bf16 v[104:107], v[132:135], a[152:155], v[104:107]\n"
"ds_read_b64_tr_b16 v[146:147], v169 offset:33280\n"
"buffer_load_dwordx4 v176, s[40:43], s64 offen offset:0 lds\n"
"v_mfma_f32_16x16x32_bf16 v[108:111], v[128:131], a[140:143], v[108:111]\n"
"ds_read_b64_tr_b16 v[148:149], v169 offset:34816\n"
"v_mul_f32 v217, v217, v9\n"
"v_mfma_f32_16x16x32_bf16 v[108:111], v[132:135], a[156:159], v[108:111]\n"
"ds_read_b64_tr_b16 v[150:151], v169 offset:35328\n"
"v_mul_f32 v218, v218, v10\n"
"v_mfma_f32_16x16x32_bf16 v[112:115], v[136:139], a[128:131], v[112:115]\n"
"ds_read_b64_tr_b16 v[128:129], v169 offset:33792\n"
"v_mul_f32 v219, v219, v11\n"
"v_mfma_f32_16x16x32_bf16 v[112:115], v[140:143], a[144:147], v[112:115]\n"
"ds_read_b64_tr_b16 v[130:131], v169 offset:34304\n"
"v_mul_f32 v24, v8, v24\n"
"v_mfma_f32_16x16x32_bf16 v[116:119], v[136:139], a[132:135], v[116:119]\n"
"ds_read_b64_tr_b16 v[132:133], v169 offset:35840\n"
"v_mul_f32 v25, v9, v25\n"
"v_mfma_f32_16x16x32_bf16 v[116:119], v[140:143], a[148:151], v[116:119]\n"
"ds_read_b64_tr_b16 v[134:135], v169 offset:36352\n"
"v_mul_f32 v26, v10, v26\n"
"v_mfma_f32_16x16x32_bf16 v[120:123], v[136:139], a[136:139], v[120:123]\n"
"buffer_load_dwordx4 v176, s[44:47], s64 offen offset:1024 lds\n"
"v_mfma_f32_16x16x32_bf16 v[120:123], v[140:143], a[152:155], v[120:123]\n"
"ds_write_b128 v174, v[164:167] offset:1024\n"
"v_mfma_f32_16x16x32_bf16 v[124:127], v[136:139], a[140:143], v[124:127]\n"
"v_exp_f32 v216, v216\n"
"v_mfma_f32_16x16x32_bf16 v[124:127], v[140:143], a[156:159], v[124:127]\n"
"v_exp_f32 v217, v217\n"
"s_waitcnt lgkmcnt(5)\n"
"v_mfma_f32_16x16x32_bf16 v[64:67], v[144:147], a[160:163], v[64:67]\n"
"ds_read_b64_tr_b16 v[136:137], v171 offset:32768\n"
"v_mul_f32 v27, v11, v27\n"
"v_mfma_f32_16x16x32_bf16 v[64:67], v[148:151], a[176:179], v[64:67]\n"
"ds_read_b64_tr_b16 v[138:139], v171 offset:33280\n"
"v_add_f32 v216, 1.0, v216\n"
"v_mfma_f32_16x16x32_bf16 v[68:71], v[144:147], a[164:167], v[68:71]\n"
"ds_read_b64_tr_b16 v[140:141], v171 offset:34816\n"
"v_add_f32 v217, 1.0, v217\n"
"v_mfma_f32_16x16x32_bf16 v[68:71], v[148:151], a[180:183], v[68:71]\n"
"ds_read_b64_tr_b16 v[142:143], v171 offset:35328\n"
"v_mul_f32 v152, v40, v40\n"
"v_mfma_f32_16x16x32_bf16 v[72:75], v[144:147], a[168:171], v[72:75]\n"
"buffer_load_dwordx4 v177, s[40:43], s64 offen offset:2048 lds\n"
"v_mfma_f32_16x16x32_bf16 v[72:75], v[148:151], a[184:187], v[72:75]\n"
"v_exp_f32 v218, v218\n"
"v_mfma_f32_16x16x32_bf16 v[76:79], v[144:147], a[172:175], v[76:79]\n"
"v_exp_f32 v219, v219\n"
"v_mfma_f32_16x16x32_bf16 v[76:79], v[148:151], a[188:191], v[76:79]\n"
"buffer_load_dwordx4 v177, s[44:47], s64 offen offset:3072 lds\n"
"s_waitcnt lgkmcnt(5)\n"
"v_mfma_f32_16x16x32_bf16 v[80:83], v[128:131], a[160:163], v[80:83]\n"
"ds_read_b64_tr_b16 v[144:145], v171 offset:33792\n"
"v_add_f32 v218, 1.0, v218\n"
"v_mfma_f32_16x16x32_bf16 v[80:83], v[132:135], a[176:179], v[80:83]\n"
"ds_read_b64_tr_b16 v[146:147], v171 offset:34304\n"
"v_add_f32 v219, 1.0, v219\n"
"v_mfma_f32_16x16x32_bf16 v[84:87], v[128:131], a[164:167], v[84:87]\n"
"ds_read_b64_tr_b16 v[148:149], v171 offset:35840\n"
"v_mul_f32 v153, v41, v41\n"
"v_mfma_f32_16x16x32_bf16 v[84:87], v[132:135], a[180:183], v[84:87]\n"
"ds_read_b64_tr_b16 v[150:151], v171 offset:36352\n"
"v_mul_f32 v154, v42, v42\n"
"v_mfma_f32_16x16x32_bf16 v[88:91], v[128:131], a[168:171], v[88:91]\n"
"s_mov_b32 s17, 0\n"
"v_rcp_f32 v216, v216\n"
"v_mfma_f32_16x16x32_bf16 v[88:91], v[132:135], a[184:187], v[88:91]\n"
"buffer_load_dword v197, v196, s[48:51], s17 offen offset:0\n"
"v_mfma_f32_16x16x32_bf16 v[92:95], v[128:131], a[172:175], v[92:95]\n"
"v_rcp_f32 v217, v217\n"
"v_mfma_f32_16x16x32_bf16 v[92:95], v[132:135], a[188:191], v[92:95]\n"
"v_rcp_f32 v218, v218\n"
"s_waitcnt lgkmcnt(4)\n"
"v_mfma_f32_16x16x32_bf16 v[96:99], v[136:139], a[160:163], v[96:99]\n"
"v_rcp_f32 v219, v219\n"
"v_mfma_f32_16x16x32_bf16 v[96:99], v[140:143], a[176:179], v[96:99]\n"
"v_mul_f32 v24, v24, v216\n"
"v_mul_f32 v25, v25, v217\n"
"v_mfma_f32_16x16x32_bf16 v[100:103], v[136:139], a[164:167], v[100:103]\n"
"v_mul_f32 v26, v26, v218\n"
"v_mfma_f32_16x16x32_bf16 v[100:103], v[140:143], a[180:183], v[100:103]\n"
"s_waitcnt lgkmcnt(0)\n"
"s_waitcnt vmcnt(14)\n"
"s_barrier\n"
"v_mul_f32 v27, v27, v219\n"
"v_cvt_pk_bf16_f32 v160, v24, v25\n"
"v_mfma_f32_16x16x32_bf16 v[104:107], v[136:139], a[168:171], v[104:107]\n"
"ds_read_b64_tr_b16 v[128:129], v168 offset:0\n"
"s_add_u32 m0, s91, 0x10000\n"
"v_cvt_pk_bf16_f32 v161, v26, v27\n"
"v_mfma_f32_16x16x32_bf16 v[104:107], v[140:143], a[184:187], v[104:107]\n"
"ds_read_b64_tr_b16 v[130:131], v168 offset:512\n"
"buffer_load_dwordx4 v176, s[40:43], s65 offen offset:0 lds\n"
"v_mfma_f32_16x16x32_bf16 v[108:111], v[136:139], a[172:175], v[108:111]\n"
"ds_read_b64_tr_b16 v[132:133], v168 offset:2048\n"
"v_mul_f32 v155, v43, v43\n"
"v_mfma_f32_16x16x32_bf16 v[108:111], v[140:143], a[188:191], v[108:111]\n"
"ds_read_b64_tr_b16 v[134:135], v168 offset:2560\n"
"v_fma_f32 v152, v152, s94, v193\n"
"v_mfma_f32_16x16x32_bf16 v[112:115], v[144:147], a[160:163], v[112:115]\n"
"ds_read_b64_tr_b16 v[136:137], v168 offset:1024\n"
"v_fma_f32 v153, v153, s94, v193\n"
"v_mfma_f32_16x16x32_bf16 v[112:115], v[148:151], a[176:179], v[112:115]\n"
"ds_read_b64_tr_b16 v[138:139], v168 offset:1536\n"
"v_fma_f32 v154, v154, s94, v193\n"
"v_mfma_f32_16x16x32_bf16 v[116:119], v[144:147], a[164:167], v[116:119]\n"
"ds_read_b64_tr_b16 v[140:141], v168 offset:3072\n"
"v_fma_f32 v155, v155, s94, v193\n"
"v_mfma_f32_16x16x32_bf16 v[116:119], v[148:151], a[180:183], v[116:119]\n"
"ds_read_b64_tr_b16 v[142:143], v168 offset:3584\n"
"v_mul_f32 v152, v152, v40\n"
"v_mfma_f32_16x16x32_bf16 v[120:123], v[144:147], a[168:171], v[120:123]\n"
"buffer_load_dwordx4 v176, s[44:47], s65 offen offset:1024 lds\n"
"v_mfma_f32_16x16x32_bf16 v[120:123], v[148:151], a[184:187], v[120:123]\n"
"v_mul_f32 v153, v153, v41\n"
"v_mul_f32 v154, v154, v42\n"
"v_mfma_f32_16x16x32_bf16 v[124:127], v[144:147], a[172:175], v[124:127]\n"
"v_mul_f32 v155, v155, v43\n"
"v_mul_f32 v56, v40, v56\n"
"v_mfma_f32_16x16x32_bf16 v[124:127], v[148:151], a[188:191], v[124:127]\n"
"v_exp_f32 v152, v152\n"
"s_waitcnt lgkmcnt(4)\n"
"v_mfma_f32_16x16x32_bf16 v[64:67], v[128:131], a[192:195], v[64:67]\n"
"ds_read_b64_tr_b16 v[144:145], v170 offset:0\n"
"v_mul_f32 v57, v41, v57\n"
"v_mfma_f32_16x16x32_bf16 v[64:67], v[132:135], a[208:211], v[64:67]\n"
"ds_read_b64_tr_b16 v[146:147], v170 offset:512\n"
"v_mul_f32 v58, v42, v58\n"
"v_mfma_f32_16x16x32_bf16 v[68:71], v[128:131], a[196:199], v[68:71]\n"
"ds_read_b64_tr_b16 v[148:149], v170 offset:2048\n"
"v_mul_f32 v59, v43, v59\n"
"v_mfma_f32_16x16x32_bf16 v[68:71], v[132:135], a[212:215], v[68:71]\n"
"ds_read_b64_tr_b16 v[150:151], v170 offset:2560\n"
"v_add_f32 v152, 1.0, v152\n"
"v_mfma_f32_16x16x32_bf16 v[72:75], v[128:131], a[200:203], v[72:75]\n"
"buffer_load_dwordx4 v177, s[40:43], s65 offen offset:2048 lds\n"
"v_mfma_f32_16x16x32_bf16 v[72:75], v[132:135], a[216:219], v[72:75]\n"
"v_exp_f32 v153, v153\n"
"v_mfma_f32_16x16x32_bf16 v[76:79], v[128:131], a[204:207], v[76:79]\n"
"v_exp_f32 v154, v154\n"
"v_mfma_f32_16x16x32_bf16 v[76:79], v[132:135], a[220:223], v[76:79]\n"
"v_exp_f32 v155, v155\n"
"s_waitcnt lgkmcnt(4)\n"
"v_mfma_f32_16x16x32_bf16 v[80:83], v[136:139], a[192:195], v[80:83]\n"
"ds_read_b64_tr_b16 v[128:129], v170 offset:1024\n"
"v_add_f32 v153, 1.0, v153\n"
"v_mfma_f32_16x16x32_bf16 v[80:83], v[140:143], a[208:211], v[80:83]\n"
"ds_read_b64_tr_b16 v[130:131], v170 offset:1536\n"
"v_add_f32 v154, 1.0, v154\n"
"v_mfma_f32_16x16x32_bf16 v[84:87], v[136:139], a[196:199], v[84:87]\n"
"ds_read_b64_tr_b16 v[132:133], v170 offset:3072\n"
"v_add_f32 v155, 1.0, v155\n"
"v_mfma_f32_16x16x32_bf16 v[84:87], v[140:143], a[212:215], v[84:87]\n"
"ds_read_b64_tr_b16 v[134:135], v170 offset:3584\n"
"v_mul_f32 v156, v12, v12\n"
"v_mfma_f32_16x16x32_bf16 v[88:91], v[136:139], a[200:203], v[88:91]\n"
"buffer_load_dwordx4 v177, s[44:47], s65 offen offset:3072 lds\n"
"v_mfma_f32_16x16x32_bf16 v[88:91], v[140:143], a[216:219], v[88:91]\n"
"v_rcp_f32 v152, v152\n"
"v_mfma_f32_16x16x32_bf16 v[92:95], v[136:139], a[204:207], v[92:95]\n"
"v_rcp_f32 v153, v153\n"
"v_mfma_f32_16x16x32_bf16 v[92:95], v[140:143], a[220:223], v[92:95]\n"
"v_rcp_f32 v154, v154\n"
"s_waitcnt lgkmcnt(4)\n"
"v_mfma_f32_16x16x32_bf16 v[96:99], v[144:147], a[192:195], v[96:99]\n"
"v_rcp_f32 v155, v155\n"
"v_mfma_f32_16x16x32_bf16 v[96:99], v[148:151], a[208:211], v[96:99]\n"
"v_mul_f32 v56, v56, v152\n"
"v_mul_f32 v57, v57, v153\n"
"v_mfma_f32_16x16x32_bf16 v[100:103], v[144:147], a[196:199], v[100:103]\n"
"v_mul_f32 v58, v58, v154\n"
"v_mul_f32 v59, v59, v155\n"
"v_mfma_f32_16x16x32_bf16 v[100:103], v[148:151], a[212:215], v[100:103]\n"
"s_waitcnt lgkmcnt(0)\n"
"s_waitcnt vmcnt(13)\n"
"s_barrier\n"
"v_cvt_pk_bf16_f32 v162, v56, v57\n"
"v_cvt_pk_bf16_f32 v163, v58, v59\n"
"v_mfma_f32_16x16x32_bf16 v[104:107], v[144:147], a[200:203], v[104:107]\n"
"ds_read_b64_tr_b16 v[136:137], v168 offset:16384\n"
"s_add_u32 m0, s91, 0\n"
"v_mul_f32 v157, v13, v13\n"
"v_mfma_f32_16x16x32_bf16 v[104:107], v[148:151], a[216:219], v[104:107]\n"
"ds_read_b64_tr_b16 v[138:139], v168 offset:16896\n"
"buffer_load_dwordx4 v176, s[40:43], s66 offen offset:0 lds\n"
"v_mfma_f32_16x16x32_bf16 v[108:111], v[144:147], a[204:207], v[108:111]\n"
"ds_read_b64_tr_b16 v[140:141], v168 offset:18432\n"
"v_mul_f32 v158, v14, v14\n"
"v_mfma_f32_16x16x32_bf16 v[108:111], v[148:151], a[220:223], v[108:111]\n"
"ds_read_b64_tr_b16 v[142:143], v168 offset:18944\n"
"v_mul_f32 v159, v15, v15\n"
"v_mfma_f32_16x16x32_bf16 v[112:115], v[128:131], a[192:195], v[112:115]\n"
"ds_read_b64_tr_b16 v[144:145], v168 offset:17408\n"
"v_fma_f32 v156, v156, s94, v193\n"
"v_mfma_f32_16x16x32_bf16 v[112:115], v[132:135], a[208:211], v[112:115]\n"
"ds_read_b64_tr_b16 v[146:147], v168 offset:17920\n"
"v_fma_f32 v157, v157, s94, v193\n"
"v_mfma_f32_16x16x32_bf16 v[116:119], v[128:131], a[196:199], v[116:119]\n"
"ds_read_b64_tr_b16 v[148:149], v168 offset:19456\n"
"v_fma_f32 v158, v158, s94, v193\n"
"v_mfma_f32_16x16x32_bf16 v[116:119], v[132:135], a[212:215], v[116:119]\n"
"ds_read_b64_tr_b16 v[150:151], v168 offset:19968\n"
"v_fma_f32 v159, v159, s94, v193\n"
"v_mfma_f32_16x16x32_bf16 v[120:123], v[128:131], a[200:203], v[120:123]\n"
"buffer_load_dwordx4 v176, s[44:47], s66 offen offset:1024 lds\n"
"v_mfma_f32_16x16x32_bf16 v[120:123], v[132:135], a[216:219], v[120:123]\n"
"v_mul_f32 v156, v156, v12\n"
"v_mul_f32 v157, v157, v13\n"
"v_mfma_f32_16x16x32_bf16 v[124:127], v[128:131], a[204:207], v[124:127]\n"
"v_mul_f32 v158, v158, v14\n"
"v_mul_f32 v159, v159, v15\n"
"v_mfma_f32_16x16x32_bf16 v[124:127], v[132:135], a[220:223], v[124:127]\n"
"v_exp_f32 v156, v156\n"
"s_waitcnt lgkmcnt(4)\n"
"v_mfma_f32_16x16x32_bf16 v[64:67], v[136:139], a[224:227], v[64:67]\n"
"ds_read_b64_tr_b16 v[128:129], v170 offset:16384\n"
"v_mul_f32 v28, v12, v28\n"
"v_mfma_f32_16x16x32_bf16 v[64:67], v[140:143], a[240:243], v[64:67]\n"
"ds_read_b64_tr_b16 v[130:131], v170 offset:16896\n"
"v_mul_f32 v29, v13, v29\n"
"v_mfma_f32_16x16x32_bf16 v[68:71], v[136:139], a[228:231], v[68:71]\n"
"ds_read_b64_tr_b16 v[132:133], v170 offset:18432\n"
"v_mul_f32 v30, v14, v30\n"
"v_mfma_f32_16x16x32_bf16 v[68:71], v[140:143], a[244:247], v[68:71]\n"
"ds_read_b64_tr_b16 v[134:135], v170 offset:18944\n"
"v_mul_f32 v31, v15, v31\n"
"v_mfma_f32_16x16x32_bf16 v[72:75], v[136:139], a[232:235], v[72:75]\n"
"buffer_load_dwordx4 v177, s[40:43], s66 offen offset:2048 lds\n"
"v_mfma_f32_16x16x32_bf16 v[72:75], v[140:143], a[248:251], v[72:75]\n"
"v_exp_f32 v157, v157\n"
"v_mfma_f32_16x16x32_bf16 v[76:79], v[136:139], a[236:239], v[76:79]\n"
"v_exp_f32 v158, v158\n"
"v_mfma_f32_16x16x32_bf16 v[76:79], v[140:143], a[252:255], v[76:79]\n"
"v_exp_f32 v159, v159\n"
"s_waitcnt lgkmcnt(4)\n"
"v_mfma_f32_16x16x32_bf16 v[80:83], v[144:147], a[224:227], v[80:83]\n"
"ds_read_b64_tr_b16 v[136:137], v170 offset:17408\n"
"v_add_f32 v156, 1.0, v156\n"
"v_mfma_f32_16x16x32_bf16 v[80:83], v[148:151], a[240:243], v[80:83]\n"
"ds_read_b64_tr_b16 v[138:139], v170 offset:17920\n"
"v_add_f32 v157, 1.0, v157\n"
"v_mfma_f32_16x16x32_bf16 v[84:87], v[144:147], a[228:231], v[84:87]\n"
"ds_read_b64_tr_b16 v[140:141], v170 offset:19456\n"
"v_add_f32 v158, 1.0, v158\n"
"v_mfma_f32_16x16x32_bf16 v[84:87], v[148:151], a[244:247], v[84:87]\n"
"ds_read_b64_tr_b16 v[142:143], v170 offset:19968\n"
"v_add_f32 v159, 1.0, v159\n"
"v_mfma_f32_16x16x32_bf16 v[88:91], v[144:147], a[232:235], v[88:91]\n"
"buffer_load_dwordx4 v177, s[44:47], s66 offen offset:3072 lds\n"
"v_mfma_f32_16x16x32_bf16 v[88:91], v[148:151], a[248:251], v[88:91]\n"
"v_rcp_f32 v156, v156\n"
"v_mfma_f32_16x16x32_bf16 v[92:95], v[144:147], a[236:239], v[92:95]\n"
"v_rcp_f32 v157, v157\n"
"v_mfma_f32_16x16x32_bf16 v[92:95], v[148:151], a[252:255], v[92:95]\n"
"v_rcp_f32 v158, v158\n"
"s_waitcnt lgkmcnt(4)\n"
"v_mfma_f32_16x16x32_bf16 v[96:99], v[128:131], a[224:227], v[96:99]\n"
"v_mul_f32 v28, v28, v156\n"
"v_mfma_f32_16x16x32_bf16 v[96:99], v[132:135], a[240:243], v[96:99]\n"
"v_rcp_f32 v159, v159\n"
"v_mfma_f32_16x16x32_bf16 v[100:103], v[128:131], a[228:231], v[100:103]\n"
"v_mul_f32 v29, v29, v157\n"
"v_mul_f32 v30, v30, v158\n"
"v_mfma_f32_16x16x32_bf16 v[100:103], v[132:135], a[244:247], v[100:103]\n"
"s_waitcnt lgkmcnt(0)\n"
"s_waitcnt vmcnt(13)\n"
"s_barrier\n"
"v_mul_f32 v31, v31, v159\n"
"v_cvt_pk_bf16_f32 v164, v28, v29\n"
"v_mfma_f32_16x16x32_bf16 v[104:107], v[128:131], a[232:235], v[104:107]\n"
"ds_read_b64_tr_b16 v[144:145], v169 offset:0\n"
"s_add_u32 m0, s91, 0x4000\n"
"v_cvt_pk_bf16_f32 v165, v30, v31\n"
"v_mfma_f32_16x16x32_bf16 v[104:107], v[132:135], a[248:251], v[104:107]\n"
"ds_read_b64_tr_b16 v[146:147], v169 offset:512\n"
"buffer_load_dwordx4 v176, s[40:43], s67 offen offset:0 lds\n"
"v_mfma_f32_16x16x32_bf16 v[108:111], v[128:131], a[236:239], v[108:111]\n"
"ds_read_b64_tr_b16 v[148:149], v169 offset:2048\n"
"v_mul_f32 v216, v44, v44\n"
"v_mfma_f32_16x16x32_bf16 v[108:111], v[132:135], a[252:255], v[108:111]\n"
"ds_read_b64_tr_b16 v[150:151], v169 offset:2560\n"
"v_mul_f32 v217, v45, v45\n"
"v_mfma_f32_16x16x32_bf16 v[112:115], v[136:139], a[224:227], v[112:115]\n"
"ds_read_b64_tr_b16 v[128:129], v169 offset:1024\n"
"v_mul_f32 v218, v46, v46\n"
"v_mfma_f32_16x16x32_bf16 v[112:115], v[140:143], a[240:243], v[112:115]\n"
"ds_read_b64_tr_b16 v[130:131], v169 offset:1536\n"
"v_mul_f32 v219, v47, v47\n"
"v_mfma_f32_16x16x32_bf16 v[116:119], v[136:139], a[228:231], v[116:119]\n"
"ds_read_b64_tr_b16 v[132:133], v169 offset:3072\n"
"v_fma_f32 v216, v216, s94, v193\n"
"v_mfma_f32_16x16x32_bf16 v[116:119], v[140:143], a[244:247], v[116:119]\n"
"ds_read_b64_tr_b16 v[134:135], v169 offset:3584\n"
"v_fma_f32 v217, v217, s94, v193\n"
"v_mfma_f32_16x16x32_bf16 v[120:123], v[136:139], a[232:235], v[120:123]\n"
"buffer_load_dwordx4 v176, s[44:47], s67 offen offset:1024 lds\n"
"v_mfma_f32_16x16x32_bf16 v[120:123], v[140:143], a[248:251], v[120:123]\n"
"v_fma_f32 v218, v218, s94, v193\n"
"v_fma_f32 v219, v219, s94, v193\n"
"v_mfma_f32_16x16x32_bf16 v[124:127], v[136:139], a[236:239], v[124:127]\n"
"v_mul_f32 v216, v216, v44\n"
"v_mul_f32 v217, v217, v45\n"
"v_mfma_f32_16x16x32_bf16 v[124:127], v[140:143], a[252:255], v[124:127]\n"
"v_mul_f32 v218, v218, v46\n"
"v_mul_f32 v219, v219, v47\n"
"s_waitcnt lgkmcnt(4)\n"
"v_mfma_f32_16x16x32_bf16 v[64:67], v[144:147], v[224:227], v[64:67]\n"
"ds_read_b64_tr_b16 v[136:137], v171 offset:0\n"
"v_mul_f32 v60, v44, v60\n"
"v_mfma_f32_16x16x32_bf16 v[64:67], v[148:151], v[240:243], v[64:67]\n"
"ds_read_b64_tr_b16 v[138:139], v171 offset:512\n"
"v_mul_f32 v61, v45, v61\n"
"v_mfma_f32_16x16x32_bf16 v[68:71], v[144:147], v[228:231], v[68:71]\n"
"ds_read_b64_tr_b16 v[140:141], v171 offset:2048\n"
"v_mul_f32 v62, v46, v62\n"
"v_mfma_f32_16x16x32_bf16 v[68:71], v[148:151], v[244:247], v[68:71]\n"
"ds_read_b64_tr_b16 v[142:143], v171 offset:2560\n"
"v_mul_f32 v63, v47, v63\n"
"v_mfma_f32_16x16x32_bf16 v[72:75], v[144:147], v[232:235], v[72:75]\n"
"buffer_load_dwordx4 v177, s[40:43], s67 offen offset:2048 lds\n"
"v_mfma_f32_16x16x32_bf16 v[72:75], v[148:151], v[248:251], v[72:75]\n"
"v_exp_f32 v216, v216\n"
"v_mfma_f32_16x16x32_bf16 v[76:79], v[144:147], v[236:239], v[76:79]\n"
"v_exp_f32 v217, v217\n"
"v_mfma_f32_16x16x32_bf16 v[76:79], v[148:151], v[252:255], v[76:79]\n"
"v_exp_f32 v218, v218\n"
"s_waitcnt lgkmcnt(4)\n"
"v_mfma_f32_16x16x32_bf16 v[80:83], v[128:131], v[224:227], v[80:83]\n"
"ds_read_b64_tr_b16 v[144:145], v171 offset:1024\n"
"v_add_f32 v216, 1.0, v216\n"
"v_mfma_f32_16x16x32_bf16 v[80:83], v[132:135], v[240:243], v[80:83]\n"
"ds_read_b64_tr_b16 v[146:147], v171 offset:1536\n"
"v_add_f32 v217, 1.0, v217\n"
"v_mfma_f32_16x16x32_bf16 v[84:87], v[128:131], v[228:231], v[84:87]\n"
"ds_read_b64_tr_b16 v[148:149], v171 offset:3072\n"
"v_add_f32 v218, 1.0, v218\n"
"v_mfma_f32_16x16x32_bf16 v[84:87], v[132:135], v[244:247], v[84:87]\n"
"ds_read_b64_tr_b16 v[150:151], v171 offset:3584\n"
"v_mfma_f32_16x16x32_bf16 v[88:91], v[128:131], v[232:235], v[88:91]\n"
"buffer_load_dwordx4 v177, s[44:47], s67 offen offset:3072 lds\n"
"v_mfma_f32_16x16x32_bf16 v[88:91], v[132:135], v[248:251], v[88:91]\n"
"ds_write_b128 v174, v[160:163] offset:2048\n"
"v_mfma_f32_16x16x32_bf16 v[92:95], v[128:131], v[236:239], v[92:95]\n"
"v_exp_f32 v219, v219\n"
"v_mfma_f32_16x16x32_bf16 v[92:95], v[132:135], v[252:255], v[92:95]\n"
"v_add_f32 v219, 1.0, v219\n"
"s_waitcnt lgkmcnt(5)\n"
"v_mfma_f32_16x16x32_bf16 v[96:99], v[136:139], v[224:227], v[96:99]\n"
"v_rcp_f32 v216, v216\n"
"v_mfma_f32_16x16x32_bf16 v[96:99], v[140:143], v[240:243], v[96:99]\n"
"v_rcp_f32 v217, v217\n"
"v_mfma_f32_16x16x32_bf16 v[100:103], v[136:139], v[228:231], v[100:103]\n"
"v_rcp_f32 v218, v218\n"
"v_mfma_f32_16x16x32_bf16 v[100:103], v[140:143], v[244:247], v[100:103]\n"
"s_waitcnt lgkmcnt(1)\n"
"s_waitcnt vmcnt(13)\n"
"s_barrier\n"
"v_rcp_f32 v219, v219\n"
"v_mfma_f32_16x16x32_bf16 v[104:107], v[136:139], v[232:235], v[104:107]\n"
"ds_read_b64_tr_b16 v[128:129], v169 offset:16384\n"
"s_add_u32 m0, s91, 0x8000\n"
"v_mul_f32 v60, v60, v216\n"
"v_mfma_f32_16x16x32_bf16 v[104:107], v[140:143], v[248:251], v[104:107]\n"
"ds_read_b64_tr_b16 v[130:131], v169 offset:16896\n"
"buffer_load_dwordx4 v176, s[40:43], s68 offen offset:0 lds\n"
"v_mfma_f32_16x16x32_bf16 v[108:111], v[136:139], v[236:239], v[108:111]\n"
"ds_read_b64_tr_b16 v[132:133], v169 offset:18432\n"
"v_mul_f32 v61, v61, v217\n"
"v_mfma_f32_16x16x32_bf16 v[108:111], v[140:143], v[252:255], v[108:111]\n"
"ds_read_b64_tr_b16 v[134:135], v169 offset:18944\n"
"v_mul_f32 v62, v62, v218\n"
"v_mfma_f32_16x16x32_bf16 v[112:115], v[144:147], v[224:227], v[112:115]\n"
"ds_read_b64_tr_b16 v[136:137], v169 offset:17408\n"
"v_mul_f32 v63, v63, v219\n"
"v_mfma_f32_16x16x32_bf16 v[112:115], v[148:151], v[240:243], v[112:115]\n"
"ds_read_b64_tr_b16 v[138:139], v169 offset:17920\n"
"v_cvt_pk_bf16_f32 v166, v60, v61\n"
"v_mfma_f32_16x16x32_bf16 v[116:119], v[144:147], v[228:231], v[116:119]\n"
"ds_read_b64_tr_b16 v[140:141], v169 offset:19456\n"
"v_cvt_pk_bf16_f32 v167, v62, v63\n"
"v_mfma_f32_16x16x32_bf16 v[116:119], v[148:151], v[244:247], v[116:119]\n"
"ds_read_b64_tr_b16 v[142:143], v169 offset:19968\n"
"v_mfma_f32_16x16x32_bf16 v[120:123], v[144:147], v[232:235], v[120:123]\n"
"buffer_load_dwordx4 v176, s[44:47], s68 offen offset:1024 lds\n"
"v_mfma_f32_16x16x32_bf16 v[120:123], v[148:151], v[248:251], v[120:123]\n"
"ds_write_b128 v174, v[164:167] offset:3072\n"
"v_mfma_f32_16x16x32_bf16 v[124:127], v[144:147], v[236:239], v[124:127]\n"
"v_mfma_f32_16x16x32_bf16 v[124:127], v[148:151], v[252:255], v[124:127]\n"
"s_waitcnt lgkmcnt(5)\n"
"v_mfma_f32_16x16x32_bf16 v[0:3], v[128:131], a[0:3], 0\n"
"ds_read_b64_tr_b16 v[144:145], v171 offset:16384\n"
"v_mfma_f32_16x16x32_bf16 v[0:3], v[132:135], a[16:19], v[0:3]\n"
"ds_read_b64_tr_b16 v[146:147], v171 offset:16896\n"
"v_mul_f32 v152, v64, v64\n"
"v_mul_f32 v153, v65, v65\n"
"v_mfma_f32_16x16x32_bf16 v[4:7], v[128:131], a[4:7], 0\n"
"ds_read_b64_tr_b16 v[148:149], v171 offset:18432\n"
"v_mul_f32 v154, v66, v66\n"
"v_mfma_f32_16x16x32_bf16 v[4:7], v[132:135], a[20:23], v[4:7]\n"
"ds_read_b64_tr_b16 v[150:151], v171 offset:18944\n"
"v_mul_f32 v155, v67, v67\n"
"v_mfma_f32_16x16x32_bf16 v[8:11], v[128:131], a[8:11], 0\n"
"buffer_load_dwordx4 v177, s[40:43], s68 offen offset:2048 lds\n"
"v_mfma_f32_16x16x32_bf16 v[8:11], v[132:135], a[24:27], v[8:11]\n"
"v_fma_f32 v152, v152, s94, v193\n"
"v_fma_f32 v153, v153, s94, v193\n"
"v_mfma_f32_16x16x32_bf16 v[12:15], v[128:131], a[12:15], 0\n"
"v_fma_f32 v154, v154, s94, v193\n"
"v_fma_f32 v155, v155, s94, v193\n"
"v_mfma_f32_16x16x32_bf16 v[12:15], v[132:135], a[28:31], v[12:15]\n"
"v_mul_f32 v152, v152, v64\n"
"v_mul_f32 v153, v153, v65\n"
"s_waitcnt lgkmcnt(5)\n"
"v_mfma_f32_16x16x32_bf16 v[16:19], v[136:139], a[0:3], 0\n"
"ds_read_b64_tr_b16 v[128:129], v171 offset:17408\n"
"v_mul_f32 v154, v154, v66\n"
"v_mfma_f32_16x16x32_bf16 v[16:19], v[140:143], a[16:19], v[16:19]\n"
"ds_read_b64_tr_b16 v[130:131], v171 offset:17920\n"
"v_mul_f32 v155, v155, v67\n"
"v_mfma_f32_16x16x32_bf16 v[20:23], v[136:139], a[4:7], 0\n"
"ds_read_b64_tr_b16 v[132:133], v171 offset:19456\n"
"v_mul_f32 v80, v64, v80\n"
"v_mfma_f32_16x16x32_bf16 v[20:23], v[140:143], a[20:23], v[20:23]\n"
"ds_read_b64_tr_b16 v[134:135], v171 offset:19968\n"
"v_mul_f32 v81, v65, v81\n"
"v_mfma_f32_16x16x32_bf16 v[24:27], v[136:139], a[8:11], 0\n"
"buffer_load_dwordx4 v177, s[44:47], s68 offen offset:3072 lds\n"
"v_mfma_f32_16x16x32_bf16 v[24:27], v[140:143], a[24:27], v[24:27]\n"
"v_exp_f32 v152, v152\n"
"v_mfma_f32_16x16x32_bf16 v[28:31], v[136:139], a[12:15], 0\n"
"v_exp_f32 v153, v153\n"
"v_mfma_f32_16x16x32_bf16 v[28:31], v[140:143], a[28:31], v[28:31]\n"
"v_exp_f32 v154, v154\n"
"s_waitcnt lgkmcnt(4)\n"
"v_mfma_f32_16x16x32_bf16 v[32:35], v[144:147], a[0:3], 0\n"
"v_exp_f32 v155, v155\n"
"v_mfma_f32_16x16x32_bf16 v[32:35], v[148:151], a[16:19], v[32:35]\n"
"v_mul_f32 v82, v66, v82\n"
"v_mul_f32 v83, v67, v83\n"
"v_mfma_f32_16x16x32_bf16 v[36:39], v[144:147], a[4:7], 0\n"
"v_add_f32 v152, 1.0, v152\n"
"v_add_f32 v153, 1.0, v153\n"
"v_mfma_f32_16x16x32_bf16 v[36:39], v[148:151], a[20:23], v[36:39]\n"
"s_waitcnt lgkmcnt(0)\n"
"s_waitcnt vmcnt(12)\n"
"s_barrier\n"
"v_add_f32 v154, 1.0, v154\n"
"v_add_f32 v155, 1.0, v155\n"
"v_mfma_f32_16x16x32_bf16 v[40:43], v[144:147], a[8:11], 0\n"
"ds_read_b64_tr_b16 v[136:137], v169 offset:32768\n"
"s_add_u32 m0, s91, 0xc000\n"
"v_mul_f32 v156, v96, v96\n"
"v_mfma_f32_16x16x32_bf16 v[40:43], v[148:151], a[24:27], v[40:43]\n"
"ds_read_b64_tr_b16 v[138:139], v169 offset:33280\n"
"buffer_load_dwordx4 v176, s[40:43], s69 offen offset:0 lds\n"
"v_mfma_f32_16x16x32_bf16 v[44:47], v[144:147], a[12:15], 0\n"
"ds_read_b64_tr_b16 v[140:141], v169 offset:34816\n"
"v_mul_f32 v157, v97, v97\n"
"v_mfma_f32_16x16x32_bf16 v[44:47], v[148:151], a[28:31], v[44:47]\n"
"ds_read_b64_tr_b16 v[142:143], v169 offset:35328\n"
"v_mul_f32 v158, v98, v98\n"
"v_mfma_f32_16x16x32_bf16 v[48:51], v[128:131], a[0:3], 0\n"
"ds_read_b64_tr_b16 v[144:145], v169 offset:33792\n"
"v_mul_f32 v159, v99, v99\n"
"v_mfma_f32_16x16x32_bf16 v[48:51], v[132:135], a[16:19], v[48:51]\n"
"ds_read_b64_tr_b16 v[146:147], v169 offset:34304\n"
"v_fma_f32 v156, v156, s94, v193\n"
"v_mfma_f32_16x16x32_bf16 v[52:55], v[128:131], a[4:7], 0\n"
"ds_read_b64_tr_b16 v[148:149], v169 offset:35840\n"
"v_fma_f32 v157, v157, s94, v193\n"
"v_mfma_f32_16x16x32_bf16 v[52:55], v[132:135], a[20:23], v[52:55]\n"
"ds_read_b64_tr_b16 v[150:151], v169 offset:36352\n"
"v_fma_f32 v158, v158, s94, v193\n"
"v_mfma_f32_16x16x32_bf16 v[56:59], v[128:131], a[8:11], 0\n"
"buffer_load_dwordx4 v176, s[44:47], s69 offen offset:1024 lds\n"
"v_mfma_f32_16x16x32_bf16 v[56:59], v[132:135], a[24:27], v[56:59]\n"
"v_rcp_f32 v152, v152\n"
"v_mfma_f32_16x16x32_bf16 v[60:63], v[128:131], a[12:15], 0\n"
"v_rcp_f32 v153, v153\n"
"v_mfma_f32_16x16x32_bf16 v[60:63], v[132:135], a[28:31], v[60:63]\n"
"v_rcp_f32 v154, v154\n"
"s_waitcnt lgkmcnt(4)\n"
"v_mfma_f32_16x16x32_bf16 v[0:3], v[136:139], a[32:35], v[0:3]\n"
"ds_read_b64_tr_b16 v[128:129], v171 offset:32768\n"
"v_mul_f32 v80, v80, v152\n"
"v_mfma_f32_16x16x32_bf16 v[0:3], v[140:143], a[48:51], v[0:3]\n"
"ds_read_b64_tr_b16 v[130:131], v171 offset:33280\n"
"v_mul_f32 v81, v81, v153\n"
"v_mfma_f32_16x16x32_bf16 v[4:7], v[136:139], a[36:39], v[4:7]\n"
"ds_read_b64_tr_b16 v[132:133], v171 offset:34816\n"
"v_mul_f32 v82, v82, v154\n"
"v_mfma_f32_16x16x32_bf16 v[4:7], v[140:143], a[52:55], v[4:7]\n"
"ds_read_b64_tr_b16 v[134:135], v171 offset:35328\n"
"v_cvt_pk_bf16_f32 v160, v80, v81\n"
"v_mfma_f32_16x16x32_bf16 v[8:11], v[136:139], a[40:43], v[8:11]\n"
"buffer_load_dwordx4 v177, s[40:43], s69 offen offset:2048 lds\n"
"v_mfma_f32_16x16x32_bf16 v[8:11], v[140:143], a[56:59], v[8:11]\n"
"v_rcp_f32 v155, v155\n"
"v_mfma_f32_16x16x32_bf16 v[12:15], v[136:139], a[44:47], v[12:15]\n"
"v_mul_f32 v83, v83, v155\n"
"v_cvt_pk_bf16_f32 v161, v82, v83\n"
"v_mfma_f32_16x16x32_bf16 v[12:15], v[140:143], a[60:63], v[12:15]\n"
"v_fma_f32 v159, v159, s94, v193\n"
"v_mul_f32 v156, v156, v96\n"
"s_waitcnt lgkmcnt(4)\n"
"v_mfma_f32_16x16x32_bf16 v[16:19], v[144:147], a[32:35], v[16:19]\n"
"ds_read_b64_tr_b16 v[136:137], v171 offset:33792\n"
"v_mul_f32 v157, v157, v97\n"
"v_mfma_f32_16x16x32_bf16 v[16:19], v[148:151], a[48:51], v[16:19]\n"
"ds_read_b64_tr_b16 v[138:139], v171 offset:34304\n"
"v_mul_f32 v158, v158, v98\n"
"v_mfma_f32_16x16x32_bf16 v[20:23], v[144:147], a[36:39], v[20:23]\n"
"ds_read_b64_tr_b16 v[140:141], v171 offset:35840\n"
"v_mul_f32 v159, v159, v99\n"
"v_mfma_f32_16x16x32_bf16 v[20:23], v[148:151], a[52:55], v[20:23]\n"
"ds_read_b64_tr_b16 v[142:143], v171 offset:36352\n"
"v_mul_f32 v112, v96, v112\n"
"v_mfma_f32_16x16x32_bf16 v[24:27], v[144:147], a[40:43], v[24:27]\n"
"buffer_load_dwordx4 v177, s[44:47], s69 offen offset:3072 lds\n"
"v_mfma_f32_16x16x32_bf16 v[24:27], v[148:151], a[56:59], v[24:27]\n"
"v_exp_f32 v156, v156\n"
"v_mfma_f32_16x16x32_bf16 v[28:31], v[144:147], a[44:47], v[28:31]\n"
"v_mul_f32 v113, v97, v113\n"
"v_mfma_f32_16x16x32_bf16 v[28:31], v[148:151], a[60:63], v[28:31]\n"
"v_exp_f32 v157, v157\n"
"s_waitcnt lgkmcnt(4)\n"
"v_mfma_f32_16x16x32_bf16 v[32:35], v[128:131], a[32:35], v[32:35]\n"
"v_exp_f32 v158, v158\n"
"v_mfma_f32_16x16x32_bf16 v[32:35], v[132:135], a[48:51], v[32:35]\n"
"v_exp_f32 v159, v159\n"
"v_mfma_f32_16x16x32_bf16 v[36:39], v[128:131], a[36:39], v[36:39]\n"
"v_mul_f32 v114, v98, v114\n"
"v_mul_f32 v115, v99, v115\n"
"v_mfma_f32_16x16x32_bf16 v[36:39], v[132:135], a[52:55], v[36:39]\n"
"s_waitcnt lgkmcnt(0)\n"
"s_waitcnt vmcnt(12)\n"
"s_barrier\n"
"v_add_f32 v156, 1.0, v156\n"
"v_mfma_f32_16x16x32_bf16 v[40:43], v[128:131], a[40:43], v[40:43]\n"
"ds_read_b64_tr_b16 v[144:145], v168 offset:0\n"
"s_add_u32 m0, s91, 0x10000\n"
"v_add_f32 v157, 1.0, v157\n"
"v_mfma_f32_16x16x32_bf16 v[40:43], v[132:135], a[56:59], v[40:43]\n"
"ds_read_b64_tr_b16 v[146:147], v168 offset:512\n"
"buffer_load_dwordx4 v176, s[40:43], s70 offen offset:0 lds\n"
"v_mfma_f32_16x16x32_bf16 v[44:47], v[128:131], a[44:47], v[44:47]\n"
"ds_read_b64_tr_b16 v[148:149], v168 offset:2048\n"
"v_add_f32 v158, 1.0, v158\n"
"v_mfma_f32_16x16x32_bf16 v[44:47], v[132:135], a[60:63], v[44:47]\n"
"ds_read_b64_tr_b16 v[150:151], v168 offset:2560\n"
"v_add_f32 v159, 1.0, v159\n"
"v_mfma_f32_16x16x32_bf16 v[48:51], v[136:139], a[32:35], v[48:51]\n"
"ds_read_b64_tr_b16 v[128:129], v168 offset:1024\n"
"v_mul_f32 v216, v68, v68\n"
"v_mfma_f32_16x16x32_bf16 v[48:51], v[140:143], a[48:51], v[48:51]\n"
"ds_read_b64_tr_b16 v[130:131], v168 offset:1536\n"
"v_mul_f32 v217, v69, v69\n"
"v_mfma_f32_16x16x32_bf16 v[52:55], v[136:139], a[36:39], v[52:55]\n"
"ds_read_b64_tr_b16 v[132:133], v168 offset:3072\n"
"v_mul_f32 v218, v70, v70\n"
"v_mfma_f32_16x16x32_bf16 v[52:55], v[140:143], a[52:55], v[52:55]\n"
"ds_read_b64_tr_b16 v[134:135], v168 offset:3584\n"
"v_mul_f32 v219, v71, v71\n"
"v_mfma_f32_16x16x32_bf16 v[56:59], v[136:139], a[40:43], v[56:59]\n"
"buffer_load_dwordx4 v176, s[44:47], s70 offen offset:1024 lds\n"
"v_mfma_f32_16x16x32_bf16 v[56:59], v[140:143], a[56:59], v[56:59]\n"
"v_rcp_f32 v156, v156\n"
"v_mfma_f32_16x16x32_bf16 v[60:63], v[136:139], a[44:47], v[60:63]\n"
"v_rcp_f32 v157, v157\n"
"v_mfma_f32_16x16x32_bf16 v[60:63], v[140:143], a[60:63], v[60:63]\n"
"v_rcp_f32 v158, v158\n"
"s_waitcnt lgkmcnt(4)\n"
"v_mfma_f32_16x16x32_bf16 v[0:3], v[144:147], a[64:67], v[0:3]\n"
"ds_read_b64_tr_b16 v[136:137], v170 offset:0\n"
"v_mul_f32 v112, v112, v156\n"
"v_mfma_f32_16x16x32_bf16 v[0:3], v[148:151], a[80:83], v[0:3]\n"
"ds_read_b64_tr_b16 v[138:139], v170 offset:512\n"
"v_mul_f32 v113, v113, v157\n"
"v_mfma_f32_16x16x32_bf16 v[4:7], v[144:147], a[68:71], v[4:7]\n"
"ds_read_b64_tr_b16 v[140:141], v170 offset:2048\n"
"v_mul_f32 v114, v114, v158\n"
"v_mfma_f32_16x16x32_bf16 v[4:7], v[148:151], a[84:87], v[4:7]\n"
"ds_read_b64_tr_b16 v[142:143], v170 offset:2560\n"
"v_cvt_pk_bf16_f32 v162, v112, v113\n"
"v_mfma_f32_16x16x32_bf16 v[8:11], v[144:147], a[72:75], v[8:11]\n"
"buffer_load_dwordx4 v177, s[40:43], s70 offen offset:2048 lds\n"
"v_mfma_f32_16x16x32_bf16 v[8:11], v[148:151], a[88:91], v[8:11]\n"
"v_rcp_f32 v159, v159\n"
"v_mfma_f32_16x16x32_bf16 v[12:15], v[144:147], a[76:79], v[12:15]\n"
"v_mul_f32 v115, v115, v159\n"
"v_cvt_pk_bf16_f32 v163, v114, v115\n"
"v_mfma_f32_16x16x32_bf16 v[12:15], v[148:151], a[92:95], v[12:15]\n"
"buffer_load_dwordx4 v177, s[44:47], s70 offen offset:3072 lds\n"
"s_waitcnt lgkmcnt(4)\n"
"v_mfma_f32_16x16x32_bf16 v[16:19], v[128:131], a[64:67], v[16:19]\n"
"ds_read_b64_tr_b16 v[144:145], v170 offset:1024\n"
"v_fma_f32 v216, v216, s94, v193\n"
"v_mfma_f32_16x16x32_bf16 v[16:19], v[132:135], a[80:83], v[16:19]\n"
"ds_read_b64_tr_b16 v[146:147], v170 offset:1536\n"
"v_fma_f32 v217, v217, s94, v193\n"
"v_mfma_f32_16x16x32_bf16 v[20:23], v[128:131], a[68:71], v[20:23]\n"
"ds_read_b64_tr_b16 v[148:149], v170 offset:3072\n"
"v_fma_f32 v218, v218, s94, v193\n"
"v_mfma_f32_16x16x32_bf16 v[20:23], v[132:135], a[84:87], v[20:23]\n"
"ds_read_b64_tr_b16 v[150:151], v170 offset:3584\n"
"v_fma_f32 v219, v219, s94, v193\n"
"v_mfma_f32_16x16x32_bf16 v[24:27], v[128:131], a[72:75], v[24:27]\n"
"s_mov_b32 s17, 0\n"
"v_mul_f32 v216, v216, v68\n"
"v_mul_f32 v217, v217, v69\n"
"v_mfma_f32_16x16x32_bf16 v[24:27], v[132:135], a[88:91], v[24:27]\n"
"buffer_load_dword v197, v194, s[60:63], s17 offen offset:384\n"
"v_mfma_f32_16x16x32_bf16 v[28:31], v[128:131], a[76:79], v[28:31]\n"
"v_mul_f32 v218, v218, v70\n"
"v_mul_f32 v219, v219, v71\n"
"v_mfma_f32_16x16x32_bf16 v[28:31], v[132:135], a[92:95], v[28:31]\n"
"v_exp_f32 v216, v216\n"
"s_waitcnt lgkmcnt(4)\n"
"v_mfma_f32_16x16x32_bf16 v[32:35], v[136:139], a[64:67], v[32:35]\n"
"v_exp_f32 v217, v217\n"
"v_mfma_f32_16x16x32_bf16 v[32:35], v[140:143], a[80:83], v[32:35]\n"
"v_exp_f32 v218, v218\n"
"v_mfma_f32_16x16x32_bf16 v[36:39], v[136:139], a[68:71], v[36:39]\n"
"v_mul_f32 v84, v68, v84\n"
"v_mfma_f32_16x16x32_bf16 v[36:39], v[140:143], a[84:87], v[36:39]\n"
"s_waitcnt lgkmcnt(0)\n"
"s_waitcnt vmcnt(13)\n"
"s_barrier\n"
"v_exp_f32 v219, v219\n"
"v_mfma_f32_16x16x32_bf16 v[40:43], v[136:139], a[72:75], v[40:43]\n"
"ds_read_b64_tr_b16 v[128:129], v168 offset:16384\n"
"s_add_u32 m0, s91, 0\n"
"v_mul_f32 v85, v69, v85\n"
"v_mfma_f32_16x16x32_bf16 v[40:43], v[140:143], a[88:91], v[40:43]\n"
"ds_read_b64_tr_b16 v[130:131], v168 offset:16896\n"
"buffer_load_dwordx4 v176, s[40:43], s71 offen offset:0 lds\n"
"v_mfma_f32_16x16x32_bf16 v[44:47], v[136:139], a[76:79], v[44:47]\n"
"ds_read_b64_tr_b16 v[132:133], v168 offset:18432\n"
"v_mul_f32 v86, v70, v86\n"
"v_mfma_f32_16x16x32_bf16 v[44:47], v[140:143], a[92:95], v[44:47]\n"
"ds_read_b64_tr_b16 v[134:135], v168 offset:18944\n"
"v_mul_f32 v87, v71, v87\n"
"v_mfma_f32_16x16x32_bf16 v[48:51], v[144:147], a[64:67], v[48:51]\n"
"ds_read_b64_tr_b16 v[136:137], v168 offset:17408\n"
"v_add_f32 v216, 1.0, v216\n"
"v_mfma_f32_16x16x32_bf16 v[48:51], v[148:151], a[80:83], v[48:51]\n"
"ds_read_b64_tr_b16 v[138:139], v168 offset:17920\n"
"v_add_f32 v217, 1.0, v217\n"
"v_mfma_f32_16x16x32_bf16 v[52:55], v[144:147], a[68:71], v[52:55]\n"
"ds_read_b64_tr_b16 v[140:141], v168 offset:19456\n"
"v_add_f32 v218, 1.0, v218\n"
"v_mfma_f32_16x16x32_bf16 v[52:55], v[148:151], a[84:87], v[52:55]\n"
"ds_read_b64_tr_b16 v[142:143], v168 offset:19968\n"
"v_add_f32 v219, 1.0, v219\n"
"v_mfma_f32_16x16x32_bf16 v[56:59], v[144:147], a[72:75], v[56:59]\n"
"buffer_load_dwordx4 v176, s[44:47], s71 offen offset:1024 lds\n"
"v_mfma_f32_16x16x32_bf16 v[56:59], v[148:151], a[88:91], v[56:59]\n"
"ds_write_b128 v174, v[160:163] offset:16384\n"
"v_mfma_f32_16x16x32_bf16 v[60:63], v[144:147], a[76:79], v[60:63]\n"
"v_mul_f32 v152, v100, v100\n"
"v_mfma_f32_16x16x32_bf16 v[60:63], v[148:151], a[92:95], v[60:63]\n"
"v_rcp_f32 v216, v216\n"
"s_waitcnt lgkmcnt(5)\n"
"v_mfma_f32_16x16x32_bf16 v[0:3], v[128:131], a[96:99], v[0:3]\n"
"ds_read_b64_tr_b16 v[144:145], v170 offset:16384\n"
"v_mul_f32 v84, v84, v216\n"
"v_mfma_f32_16x16x32_bf16 v[0:3], v[132:135], a[112:115], v[0:3]\n"
"ds_read_b64_tr_b16 v[146:147], v170 offset:16896\n"
"v_mul_f32 v153, v101, v101\n"
"v_mfma_f32_16x16x32_bf16 v[4:7], v[128:131], a[100:103], v[4:7]\n"
"ds_read_b64_tr_b16 v[148:149], v170 offset:18432\n"
"v_mul_f32 v154, v102, v102\n"
"v_mfma_f32_16x16x32_bf16 v[4:7], v[132:135], a[116:119], v[4:7]\n"
"ds_read_b64_tr_b16 v[150:151], v170 offset:18944\n"
"v_mul_f32 v155, v103, v103\n"
"v_mfma_f32_16x16x32_bf16 v[8:11], v[128:131], a[104:107], v[8:11]\n"
"buffer_load_dwordx4 v177, s[40:43], s71 offen offset:2048 lds\n"
"v_mfma_f32_16x16x32_bf16 v[8:11], v[132:135], a[120:123], v[8:11]\n"
"v_rcp_f32 v217, v217\n"
"v_mfma_f32_16x16x32_bf16 v[12:15], v[128:131], a[108:111], v[12:15]\n"
"v_rcp_f32 v218, v218\n"
"v_mfma_f32_16x16x32_bf16 v[12:15], v[132:135], a[124:127], v[12:15]\n"
"v_rcp_f32 v219, v219\n"
"s_waitcnt lgkmcnt(5)\n"
"v_mfma_f32_16x16x32_bf16 v[16:19], v[136:139], a[96:99], v[16:19]\n"
"ds_read_b64_tr_b16 v[128:129], v170 offset:17408\n"
"v_mul_f32 v85, v85, v217\n"
"v_mfma_f32_16x16x32_bf16 v[16:19], v[140:143], a[112:115], v[16:19]\n"
"ds_read_b64_tr_b16 v[130:131], v170 offset:17920\n"
"v_mul_f32 v86, v86, v218\n"
"v_mfma_f32_16x16x32_bf16 v[20:23], v[136:139], a[100:103], v[20:23]\n"
"ds_read_b64_tr_b16 v[132:133], v170 offset:19456\n"
"v_mul_f32 v87, v87, v219\n"
"v_mfma_f32_16x16x32_bf16 v[20:23], v[140:143], a[116:119], v[20:23]\n"
"ds_read_b64_tr_b16 v[134:135], v170 offset:19968\n"
"v_cvt_pk_bf16_f32 v164, v84, v85\n"
"v_mfma_f32_16x16x32_bf16 v[24:27], v[136:139], a[104:107], v[24:27]\n"
"buffer_load_dwordx4 v177, s[44:47], s71 offen offset:3072 lds\n"
"v_mfma_f32_16x16x32_bf16 v[24:27], v[140:143], a[120:123], v[24:27]\n"
"v_cvt_pk_bf16_f32 v165, v86, v87\n"
"v_fma_f32 v152, v152, s94, v193\n"
"v_mfma_f32_16x16x32_bf16 v[28:31], v[136:139], a[108:111], v[28:31]\n"
"v_fma_f32 v153, v153, s94, v193\n"
"v_fma_f32 v154, v154, s94, v193\n"
"v_mfma_f32_16x16x32_bf16 v[28:31], v[140:143], a[124:127], v[28:31]\n"
"v_fma_f32 v155, v155, s94, v193\n"
"s_waitcnt lgkmcnt(4)\n"
"v_mfma_f32_16x16x32_bf16 v[32:35], v[144:147], a[96:99], v[32:35]\n"
"v_mul_f32 v152, v152, v100\n"
"v_mul_f32 v153, v153, v101\n"
"v_mfma_f32_16x16x32_bf16 v[32:35], v[148:151], a[112:115], v[32:35]\n"
"v_mul_f32 v154, v154, v102\n"
"v_mul_f32 v155, v155, v103\n"
"v_mfma_f32_16x16x32_bf16 v[36:39], v[144:147], a[100:103], v[36:39]\n"
"v_exp_f32 v152, v152\n"
"v_mfma_f32_16x16x32_bf16 v[36:39], v[148:151], a[116:119], v[36:39]\n"
"s_waitcnt lgkmcnt(0)\n"
"s_waitcnt vmcnt(13)\n"
"s_barrier\n"
"v_exp_f32 v153, v153\n"
"v_mfma_f32_16x16x32_bf16 v[40:43], v[144:147], a[104:107], v[40:43]\n"
"ds_read_b64_tr_b16 v[136:137], v169 offset:0\n"
"s_add_u32 m0, s91, 0x4000\n"
"v_mul_f32 v116, v100, v116\n"
"v_mfma_f32_16x16x32_bf16 v[40:43], v[148:151], a[120:123], v[40:43]\n"
"ds_read_b64_tr_b16 v[138:139], v169 offset:512\n"
"buffer_load_dwordx4 v176, s[40:43], s72 offen offset:0 lds\n"
"v_mfma_f32_16x16x32_bf16 v[44:47], v[144:147], a[108:111], v[44:47]\n"
"ds_read_b64_tr_b16 v[140:141], v169 offset:2048\n"
"v_mul_f32 v117, v101, v117\n"
"v_mfma_f32_16x16x32_bf16 v[44:47], v[148:151], a[124:127], v[44:47]\n"
"ds_read_b64_tr_b16 v[142:143], v169 offset:2560\n"
"v_mul_f32 v118, v102, v118\n"
"v_mfma_f32_16x16x32_bf16 v[48:51], v[128:131], a[96:99], v[48:51]\n"
"ds_read_b64_tr_b16 v[144:145], v169 offset:1024\n"
"v_mul_f32 v119, v103, v119\n"
"v_mfma_f32_16x16x32_bf16 v[48:51], v[132:135], a[112:115], v[48:51]\n"
"ds_read_b64_tr_b16 v[146:147], v169 offset:1536\n"
"v_add_f32 v152, 1.0, v152\n"
"v_mfma_f32_16x16x32_bf16 v[52:55], v[128:131], a[100:103], v[52:55]\n"
"ds_read_b64_tr_b16 v[148:149], v169 offset:3072\n"
"v_add_f32 v153, 1.0, v153\n"
"v_mfma_f32_16x16x32_bf16 v[52:55], v[132:135], a[116:119], v[52:55]\n"
"ds_read_b64_tr_b16 v[150:151], v169 offset:3584\n"
"v_mul_f32 v156, v72, v72\n"
"v_mfma_f32_16x16x32_bf16 v[56:59], v[128:131], a[104:107], v[56:59]\n"
"buffer_load_dwordx4 v176, s[44:47], s72 offen offset:1024 lds\n"
"v_mfma_f32_16x16x32_bf16 v[56:59], v[132:135], a[120:123], v[56:59]\n"
"v_exp_f32 v154, v154\n"
"v_mfma_f32_16x16x32_bf16 v[60:63], v[128:131], a[108:111], v[60:63]\n"
"v_exp_f32 v155, v155\n"
"v_mfma_f32_16x16x32_bf16 v[60:63], v[132:135], a[124:127], v[60:63]\n"
"v_add_f32 v154, 1.0, v154\n"
"v_add_f32 v155, 1.0, v155\n"
"s_waitcnt lgkmcnt(4)\n"
"v_mfma_f32_16x16x32_bf16 v[0:3], v[136:139], a[128:131], v[0:3]\n"
"ds_read_b64_tr_b16 v[128:129], v171 offset:0\n"
"v_mul_f32 v157, v73, v73\n"
"v_mfma_f32_16x16x32_bf16 v[0:3], v[140:143], a[144:147], v[0:3]\n"
"ds_read_b64_tr_b16 v[130:131], v171 offset:512\n"
"v_mul_f32 v158, v74, v74\n"
"v_mfma_f32_16x16x32_bf16 v[4:7], v[136:139], a[132:135], v[4:7]\n"
"ds_read_b64_tr_b16 v[132:133], v171 offset:2048\n"
"v_mul_f32 v159, v75, v75\n"
"v_mfma_f32_16x16x32_bf16 v[4:7], v[140:143], a[148:151], v[4:7]\n"
"ds_read_b64_tr_b16 v[134:135], v171 offset:2560\n"
"v_fma_f32 v156, v156, s94, v193\n"
"v_mfma_f32_16x16x32_bf16 v[8:11], v[136:139], a[136:139], v[8:11]\n"
"buffer_load_dwordx4 v177, s[40:43], s72 offen offset:2048 lds\n"
"v_mfma_f32_16x16x32_bf16 v[8:11], v[140:143], a[152:155], v[8:11]\n"
"v_rcp_f32 v152, v152\n"
"v_mfma_f32_16x16x32_bf16 v[12:15], v[136:139], a[140:143], v[12:15]\n"
"v_mul_f32 v116, v116, v152\n"
"v_mfma_f32_16x16x32_bf16 v[12:15], v[140:143], a[156:159], v[12:15]\n"
"v_rcp_f32 v153, v153\n"
"s_waitcnt lgkmcnt(4)\n"
"v_mfma_f32_16x16x32_bf16 v[16:19], v[144:147], a[128:131], v[16:19]\n"
"ds_read_b64_tr_b16 v[136:137], v171 offset:1024\n"
"v_mul_f32 v117, v117, v153\n"
"v_mfma_f32_16x16x32_bf16 v[16:19], v[148:151], a[144:147], v[16:19]\n"
"ds_read_b64_tr_b16 v[138:139], v171 offset:1536\n"
"v_cvt_pk_bf16_f32 v166, v116, v117\n"
"v_mfma_f32_16x16x32_bf16 v[20:23], v[144:147], a[132:135], v[20:23]\n"
"ds_read_b64_tr_b16 v[140:141], v171 offset:3072\n"
"v_fma_f32 v157, v157, s94, v193\n"
"v_mfma_f32_16x16x32_bf16 v[20:23], v[148:151], a[148:151], v[20:23]\n"
"ds_read_b64_tr_b16 v[142:143], v171 offset:3584\n"
"v_fma_f32 v158, v158, s94, v193\n"
"v_mfma_f32_16x16x32_bf16 v[24:27], v[144:147], a[136:139], v[24:27]\n"
"buffer_load_dwordx4 v177, s[44:47], s72 offen offset:3072 lds\n"
"v_mfma_f32_16x16x32_bf16 v[24:27], v[148:151], a[152:155], v[24:27]\n"
"v_rcp_f32 v154, v154\n"
"v_mfma_f32_16x16x32_bf16 v[28:31], v[144:147], a[140:143], v[28:31]\n"
"v_rcp_f32 v155, v155\n"
"v_mfma_f32_16x16x32_bf16 v[28:31], v[148:151], a[156:159], v[28:31]\n"
"v_mul_f32 v118, v118, v154\n"
"v_mul_f32 v119, v119, v155\n"
"s_waitcnt lgkmcnt(4)\n"
"v_mfma_f32_16x16x32_bf16 v[32:35], v[128:131], a[128:131], v[32:35]\n"
"v_cvt_pk_bf16_f32 v167, v118, v119\n"
"v_fma_f32 v159, v159, s94, v193\n"
"v_mfma_f32_16x16x32_bf16 v[32:35], v[132:135], a[144:147], v[32:35]\n"
"v_mul_f32 v156, v156, v72\n"
"v_mfma_f32_16x16x32_bf16 v[36:39], v[128:131], a[132:135], v[36:39]\n"
"v_mul_f32 v157, v157, v73\n"
"v_mul_f32 v158, v158, v74\n"
"v_mfma_f32_16x16x32_bf16 v[36:39], v[132:135], a[148:151], v[36:39]\n"
"s_waitcnt lgkmcnt(0)\n"
"s_waitcnt vmcnt(13)\n"
"s_barrier\n"
"v_mul_f32 v159, v159, v75\n"
"v_mul_f32 v88, v72, v88\n"
"v_mfma_f32_16x16x32_bf16 v[40:43], v[128:131], a[136:139], v[40:43]\n"
"ds_read_b64_tr_b16 v[144:145], v169 offset:16384\n"
"v_add_u32 v176, 0x80, v176\n"
"v_add_u32 v177, 0x80, v177\n"
"s_add_u32 m0, s91, 0x8000\n"
"v_mfma_f32_16x16x32_bf16 v[40:43], v[132:135], a[152:155], v[40:43]\n"
"ds_read_b64_tr_b16 v[146:147], v169 offset:16896\n"
"buffer_load_dwordx4 v176, s[40:43], s64 offen offset:0 lds\n"
"v_mfma_f32_16x16x32_bf16 v[44:47], v[128:131], a[140:143], v[44:47]\n"
"ds_read_b64_tr_b16 v[148:149], v169 offset:18432\n"
"v_mul_f32 v89, v73, v89\n"
"v_mfma_f32_16x16x32_bf16 v[44:47], v[132:135], a[156:159], v[44:47]\n"
"ds_read_b64_tr_b16 v[150:151], v169 offset:18944\n"
"v_mul_f32 v90, v74, v90\n"
"v_mfma_f32_16x16x32_bf16 v[48:51], v[136:139], a[128:131], v[48:51]\n"
"ds_read_b64_tr_b16 v[128:129], v169 offset:17408\n"
"v_mul_f32 v91, v75, v91\n"
"v_mfma_f32_16x16x32_bf16 v[48:51], v[140:143], a[144:147], v[48:51]\n"
"ds_read_b64_tr_b16 v[130:131], v169 offset:17920\n"
"v_mul_f32 v216, v104, v104\n"
"v_mfma_f32_16x16x32_bf16 v[52:55], v[136:139], a[132:135], v[52:55]\n"
"ds_read_b64_tr_b16 v[132:133], v169 offset:19456\n"
"v_mul_f32 v217, v105, v105\n"
"v_mfma_f32_16x16x32_bf16 v[52:55], v[140:143], a[148:151], v[52:55]\n"
"ds_read_b64_tr_b16 v[134:135], v169 offset:19968\n"
"v_mul_f32 v218, v106, v106\n"
"v_mfma_f32_16x16x32_bf16 v[56:59], v[136:139], a[136:139], v[56:59]\n"
"buffer_load_dwordx4 v176, s[44:47], s64 offen offset:1024 lds\n"
"v_mfma_f32_16x16x32_bf16 v[56:59], v[140:143], a[152:155], v[56:59]\n"
"ds_write_b128 v174, v[164:167] offset:17408\n"
"v_mfma_f32_16x16x32_bf16 v[60:63], v[136:139], a[140:143], v[60:63]\n"
"v_mul_f32 v219, v107, v107\n"
"v_mfma_f32_16x16x32_bf16 v[60:63], v[140:143], a[156:159], v[60:63]\n"
"v_exp_f32 v156, v156\n"
"s_waitcnt lgkmcnt(5)\n"
"v_mfma_f32_16x16x32_bf16 v[0:3], v[144:147], a[160:163], v[0:3]\n"
"ds_read_b64_tr_b16 v[136:137], v171 offset:16384\n"
"v_add_f32 v156, 1.0, v156\n"
"v_mfma_f32_16x16x32_bf16 v[0:3], v[148:151], a[176:179], v[0:3]\n"
"ds_read_b64_tr_b16 v[138:139], v171 offset:16896\n"
"v_fma_f32 v216, v216, s94, v193\n"
"v_mfma_f32_16x16x32_bf16 v[4:7], v[144:147], a[164:167], v[4:7]\n"
"ds_read_b64_tr_b16 v[140:141], v171 offset:18432\n"
"v_fma_f32 v217, v217, s94, v193\n"
"v_mfma_f32_16x16x32_bf16 v[4:7], v[148:151], a[180:183], v[4:7]\n"
"ds_read_b64_tr_b16 v[142:143], v171 offset:18944\n"
"v_fma_f32 v218, v218, s94, v193\n"
"v_mfma_f32_16x16x32_bf16 v[8:11], v[144:147], a[168:171], v[8:11]\n"
"buffer_load_dwordx4 v177, s[40:43], s64 offen offset:2048 lds\n"
"v_mfma_f32_16x16x32_bf16 v[8:11], v[148:151], a[184:187], v[8:11]\n"
"v_exp_f32 v157, v157\n"
"v_mfma_f32_16x16x32_bf16 v[12:15], v[144:147], a[172:175], v[12:15]\n"
"v_exp_f32 v158, v158\n"
"v_mfma_f32_16x16x32_bf16 v[12:15], v[148:151], a[188:191], v[12:15]\n"
"buffer_load_dwordx4 v177, s[44:47], s64 offen offset:3072 lds\n"
"s_waitcnt lgkmcnt(5)\n"
"v_mfma_f32_16x16x32_bf16 v[16:19], v[128:131], a[160:163], v[16:19]\n"
"ds_read_b64_tr_b16 v[144:145], v171 offset:17408\n"
"v_add_f32 v157, 1.0, v157\n"
"v_mfma_f32_16x16x32_bf16 v[16:19], v[132:135], a[176:179], v[16:19]\n"
"ds_read_b64_tr_b16 v[146:147], v171 offset:17920\n"
"v_add_f32 v158, 1.0, v158\n"
"v_mfma_f32_16x16x32_bf16 v[20:23], v[128:131], a[164:167], v[20:23]\n"
"ds_read_b64_tr_b16 v[148:149], v171 offset:19456\n"
"v_fma_f32 v219, v219, s94, v193\n"
"v_mfma_f32_16x16x32_bf16 v[20:23], v[132:135], a[180:183], v[20:23]\n"
"ds_read_b64_tr_b16 v[150:151], v171 offset:19968\n"
"v_mul_f32 v216, v216, v104\n"
"v_mfma_f32_16x16x32_bf16 v[24:27], v[128:131], a[168:171], v[24:27]\n"
"s_mov_b32 s17, 0\n"
"v_exp_f32 v159, v159\n"
"v_mfma_f32_16x16x32_bf16 v[24:27], v[132:135], a[184:187], v[24:27]\n"
"buffer_load_dword v197, v195, s[60:63], s17 offen offset:0\n"
"v_mfma_f32_16x16x32_bf16 v[28:31], v[128:131], a[172:175], v[28:31]\n"
"v_add_f32 v159, 1.0, v159\n"
"v_mul_f32 v217, v217, v105\n"
"v_mfma_f32_16x16x32_bf16 v[28:31], v[132:135], a[188:191], v[28:31]\n"
"v_rcp_f32 v156, v156\n"
"s_waitcnt lgkmcnt(4)\n"
"v_mfma_f32_16x16x32_bf16 v[32:35], v[136:139], a[160:163], v[32:35]\n"
"v_rcp_f32 v157, v157\n"
"v_mfma_f32_16x16x32_bf16 v[32:35], v[140:143], a[176:179], v[32:35]\n"
"v_rcp_f32 v158, v158\n"
"v_mfma_f32_16x16x32_bf16 v[36:39], v[136:139], a[164:167], v[36:39]\n"
"v_mul_f32 v88, v88, v156\n"
"v_mfma_f32_16x16x32_bf16 v[36:39], v[140:143], a[180:183], v[36:39]\n"
"s_waitcnt lgkmcnt(0)\n"
"s_waitcnt vmcnt(14)\n"
"s_barrier\n"
"v_rcp_f32 v159, v159\n"
"v_mfma_f32_16x16x32_bf16 v[40:43], v[136:139], a[168:171], v[40:43]\n"
"ds_read_b64_tr_b16 v[128:129], v169 offset:32768\n"
"s_add_u32 m0, s91, 0xc000\n"
"v_mul_f32 v89, v89, v157\n"
"v_mfma_f32_16x16x32_bf16 v[40:43], v[140:143], a[184:187], v[40:43]\n"
"ds_read_b64_tr_b16 v[130:131], v169 offset:33280\n"
"buffer_load_dwordx4 v176, s[40:43], s65 offen offset:0 lds\n"
"v_mfma_f32_16x16x32_bf16 v[44:47], v[136:139], a[172:175], v[44:47]\n"
"ds_read_b64_tr_b16 v[132:133], v169 offset:34816\n"
"v_mul_f32 v90, v90, v158\n"
"v_mfma_f32_16x16x32_bf16 v[44:47], v[140:143], a[188:191], v[44:47]\n"
"ds_read_b64_tr_b16 v[134:135], v169 offset:35328\n"
"v_mul_f32 v91, v91, v159\n"
"v_mfma_f32_16x16x32_bf16 v[48:51], v[144:147], a[160:163], v[48:51]\n"
"ds_read_b64_tr_b16 v[136:137], v169 offset:33792\n"
"v_cvt_pk_bf16_f32 v160, v88, v89\n"
"v_mfma_f32_16x16x32_bf16 v[48:51], v[148:151], a[176:179], v[48:51]\n"
"ds_read_b64_tr_b16 v[138:139], v169 offset:34304\n"
"v_cvt_pk_bf16_f32 v161, v90, v91\n"
"v_mfma_f32_16x16x32_bf16 v[52:55], v[144:147], a[164:167], v[52:55]\n"
"ds_read_b64_tr_b16 v[140:141], v169 offset:35840\n"
"v_mul_f32 v218, v218, v106\n"
"v_mfma_f32_16x16x32_bf16 v[52:55], v[148:151], a[180:183], v[52:55]\n"
"ds_read_b64_tr_b16 v[142:143], v169 offset:36352\n"
"v_mul_f32 v219, v219, v107\n"
"v_mfma_f32_16x16x32_bf16 v[56:59], v[144:147], a[168:171], v[56:59]\n"
"buffer_load_dwordx4 v176, s[44:47], s65 offen offset:1024 lds\n"
"v_mfma_f32_16x16x32_bf16 v[56:59], v[148:151], a[184:187], v[56:59]\n"
"v_exp_f32 v216, v216\n"
"v_mfma_f32_16x16x32_bf16 v[60:63], v[144:147], a[172:175], v[60:63]\n"
"v_exp_f32 v217, v217\n"
"v_mfma_f32_16x16x32_bf16 v[60:63], v[148:151], a[188:191], v[60:63]\n"
"v_exp_f32 v218, v218\n"
"s_waitcnt lgkmcnt(4)\n"
"v_mfma_f32_16x16x32_bf16 v[0:3], v[128:131], a[192:195], v[0:3]\n"
"ds_read_b64_tr_b16 v[144:145], v171 offset:32768\n"
"v_mul_f32 v120, v104, v120\n"
"v_mfma_f32_16x16x32_bf16 v[0:3], v[132:135], a[208:211], v[0:3]\n"
"ds_read_b64_tr_b16 v[146:147], v171 offset:33280\n"
"v_mul_f32 v121, v105, v121\n"
"v_mfma_f32_16x16x32_bf16 v[4:7], v[128:131], a[196:199], v[4:7]\n"
"ds_read_b64_tr_b16 v[148:149], v171 offset:34816\n"
"v_mul_f32 v122, v106, v122\n"
"v_mfma_f32_16x16x32_bf16 v[4:7], v[132:135], a[212:215], v[4:7]\n"
"ds_read_b64_tr_b16 v[150:151], v171 offset:35328\n"
"v_mul_f32 v123, v107, v123\n"
"v_mfma_f32_16x16x32_bf16 v[8:11], v[128:131], a[200:203], v[8:11]\n"
"buffer_load_dwordx4 v177, s[40:43], s65 offen offset:2048 lds\n"
"v_mfma_f32_16x16x32_bf16 v[8:11], v[132:135], a[216:219], v[8:11]\n"
"v_exp_f32 v219, v219\n"
"v_mfma_f32_16x16x32_bf16 v[12:15], v[128:131], a[204:207], v[12:15]\n"
"v_add_f32 v216, 1.0, v216\n"
"v_add_f32 v217, 1.0, v217\n"
"v_mfma_f32_16x16x32_bf16 v[12:15], v[132:135], a[220:223], v[12:15]\n"
"v_add_f32 v218, 1.0, v218\n"
"v_add_f32 v219, 1.0, v219\n"
"s_waitcnt lgkmcnt(4)\n"
"v_mfma_f32_16x16x32_bf16 v[16:19], v[136:139], a[192:195], v[16:19]\n"
"ds_read_b64_tr_b16 v[128:129], v171 offset:33792\n"
"v_mul_f32 v152, v76, v76\n"
"v_mfma_f32_16x16x32_bf16 v[16:19], v[140:143], a[208:211], v[16:19]\n"
"ds_read_b64_tr_b16 v[130:131], v171 offset:34304\n"
"v_mul_f32 v153, v77, v77\n"
"v_mfma_f32_16x16x32_bf16 v[20:23], v[136:139], a[196:199], v[20:23]\n"
"ds_read_b64_tr_b16 v[132:133], v171 offset:35840\n"
"v_mul_f32 v154, v78, v78\n"
"v_mfma_f32_16x16x32_bf16 v[20:23], v[140:143], a[212:215], v[20:23]\n"
"ds_read_b64_tr_b16 v[134:135], v171 offset:36352\n"
"v_mul_f32 v155, v79, v79\n"
"v_mfma_f32_16x16x32_bf16 v[24:27], v[136:139], a[200:203], v[24:27]\n"
"buffer_load_dwordx4 v177, s[44:47], s65 offen offset:3072 lds\n"
"v_mfma_f32_16x16x32_bf16 v[24:27], v[140:143], a[216:219], v[24:27]\n"
"v_rcp_f32 v216, v216\n"
"v_mfma_f32_16x16x32_bf16 v[28:31], v[136:139], a[204:207], v[28:31]\n"
"v_rcp_f32 v217, v217\n"
"v_mfma_f32_16x16x32_bf16 v[28:31], v[140:143], a[220:223], v[28:31]\n"
"v_mul_f32 v120, v120, v216\n"
"s_waitcnt lgkmcnt(4)\n"
"v_mfma_f32_16x16x32_bf16 v[32:35], v[144:147], a[192:195], v[32:35]\n"
"v_rcp_f32 v218, v218\n"
"v_mfma_f32_16x16x32_bf16 v[32:35], v[148:151], a[208:211], v[32:35]\n"
"v_rcp_f32 v219, v219\n"
"v_mfma_f32_16x16x32_bf16 v[36:39], v[144:147], a[196:199], v[36:39]\n"
"v_mul_f32 v121, v121, v217\n"
"v_mul_f32 v122, v122, v218\n"
"v_mfma_f32_16x16x32_bf16 v[36:39], v[148:151], a[212:215], v[36:39]\n"
"s_waitcnt lgkmcnt(0)\n"
"s_waitcnt vmcnt(13)\n"
"s_barrier\n"
"v_mul_f32 v123, v123, v219\n"
"v_mfma_f32_16x16x32_bf16 v[40:43], v[144:147], a[200:203], v[40:43]\n"
"ds_read_b64_tr_b16 v[136:137], v168 offset:0\n"
"s_add_u32 m0, s91, 0x10000\n"
"v_cvt_pk_bf16_f32 v162, v120, v121\n"
"v_mfma_f32_16x16x32_bf16 v[40:43], v[148:151], a[216:219], v[40:43]\n"
"ds_read_b64_tr_b16 v[138:139], v168 offset:512\n"
"buffer_load_dwordx4 v176, s[40:43], s66 offen offset:0 lds\n"
"v_mfma_f32_16x16x32_bf16 v[44:47], v[144:147], a[204:207], v[44:47]\n"
"ds_read_b64_tr_b16 v[140:141], v168 offset:2048\n"
"v_cvt_pk_bf16_f32 v163, v122, v123\n"
"v_mfma_f32_16x16x32_bf16 v[44:47], v[148:151], a[220:223], v[44:47]\n"
"ds_read_b64_tr_b16 v[142:143], v168 offset:2560\n"
"v_fma_f32 v152, v152, s94, v193\n"
"v_mfma_f32_16x16x32_bf16 v[48:51], v[128:131], a[192:195], v[48:51]\n"
"ds_read_b64_tr_b16 v[144:145], v168 offset:1024\n"
"v_fma_f32 v153, v153, s94, v193\n"
"v_mfma_f32_16x16x32_bf16 v[48:51], v[132:135], a[208:211], v[48:51]\n"
"ds_read_b64_tr_b16 v[146:147], v168 offset:1536\n"
"v_fma_f32 v154, v154, s94, v193\n"
"v_mfma_f32_16x16x32_bf16 v[52:55], v[128:131], a[196:199], v[52:55]\n"
"ds_read_b64_tr_b16 v[148:149], v168 offset:3072\n"
"v_fma_f32 v155, v155, s94, v193\n"
"v_mfma_f32_16x16x32_bf16 v[52:55], v[132:135], a[212:215], v[52:55]\n"
"ds_read_b64_tr_b16 v[150:151], v168 offset:3584\n"
"v_mul_f32 v152, v152, v76\n"
"v_mfma_f32_16x16x32_bf16 v[56:59], v[128:131], a[200:203], v[56:59]\n"
"buffer_load_dwordx4 v176, s[44:47], s66 offen offset:1024 lds\n"
"v_mfma_f32_16x16x32_bf16 v[56:59], v[132:135], a[216:219], v[56:59]\n"
"ds_write_b128 v174, v[160:163] offset:18432\n"
"v_mfma_f32_16x16x32_bf16 v[60:63], v[128:131], a[204:207], v[60:63]\n"
"v_mul_f32 v153, v153, v77\n"
"v_mul_f32 v154, v154, v78\n"
"v_mfma_f32_16x16x32_bf16 v[60:63], v[132:135], a[220:223], v[60:63]\n"
"v_mul_f32 v155, v155, v79\n"
"s_waitcnt lgkmcnt(5)\n"
"v_mfma_f32_16x16x32_bf16 v[0:3], v[136:139], a[224:227], v[0:3]\n"
"ds_read_b64_tr_b16 v[128:129], v170 offset:0\n"
"v_mul_f32 v92, v76, v92\n"
"v_mfma_f32_16x16x32_bf16 v[0:3], v[140:143], a[240:243], v[0:3]\n"
"ds_read_b64_tr_b16 v[130:131], v170 offset:512\n"
"v_mul_f32 v93, v77, v93\n"
"v_mfma_f32_16x16x32_bf16 v[4:7], v[136:139], a[228:231], v[4:7]\n"
"ds_read_b64_tr_b16 v[132:133], v170 offset:2048\n"
"v_mul_f32 v94, v78, v94\n"
"v_mfma_f32_16x16x32_bf16 v[4:7], v[140:143], a[244:247], v[4:7]\n"
"ds_read_b64_tr_b16 v[134:135], v170 offset:2560\n"
"v_mul_f32 v95, v79, v95\n"
"v_mfma_f32_16x16x32_bf16 v[8:11], v[136:139], a[232:235], v[8:11]\n"
"buffer_load_dwordx4 v177, s[40:43], s66 offen offset:2048 lds\n"
"v_mfma_f32_16x16x32_bf16 v[8:11], v[140:143], a[248:251], v[8:11]\n"
"v_exp_f32 v152, v152\n"
"v_mfma_f32_16x16x32_bf16 v[12:15], v[136:139], a[236:239], v[12:15]\n"
"v_exp_f32 v153, v153\n"
"v_mfma_f32_16x16x32_bf16 v[12:15], v[140:143], a[252:255], v[12:15]\n"
"v_exp_f32 v154, v154\n"
"s_waitcnt lgkmcnt(5)\n"
"v_mfma_f32_16x16x32_bf16 v[16:19], v[144:147], a[224:227], v[16:19]\n"
"ds_read_b64_tr_b16 v[136:137], v170 offset:1024\n"
"v_add_f32 v152, 1.0, v152\n"
"v_mfma_f32_16x16x32_bf16 v[16:19], v[148:151], a[240:243], v[16:19]\n"
"ds_read_b64_tr_b16 v[138:139], v170 offset:1536\n"
"v_add_f32 v153, 1.0, v153\n"
"v_mfma_f32_16x16x32_bf16 v[20:23], v[144:147], a[228:231], v[20:23]\n"
"ds_read_b64_tr_b16 v[140:141], v170 offset:3072\n"
"v_add_f32 v154, 1.0, v154\n"
"v_mfma_f32_16x16x32_bf16 v[20:23], v[148:151], a[244:247], v[20:23]\n"
"ds_read_b64_tr_b16 v[142:143], v170 offset:3584\n"
"v_mul_f32 v156, v108, v108\n"
"v_mfma_f32_16x16x32_bf16 v[24:27], v[144:147], a[232:235], v[24:27]\n"
"buffer_load_dwordx4 v177, s[44:47], s66 offen offset:3072 lds\n"
"v_mfma_f32_16x16x32_bf16 v[24:27], v[148:151], a[248:251], v[24:27]\n"
"v_exp_f32 v155, v155\n"
"v_mfma_f32_16x16x32_bf16 v[28:31], v[144:147], a[236:239], v[28:31]\n"
"v_add_f32 v155, 1.0, v155\n"
"v_mul_f32 v157, v109, v109\n"
"v_mfma_f32_16x16x32_bf16 v[28:31], v[148:151], a[252:255], v[28:31]\n"
"v_rcp_f32 v152, v152\n"
"s_waitcnt lgkmcnt(4)\n"
"v_mfma_f32_16x16x32_bf16 v[32:35], v[128:131], a[224:227], v[32:35]\n"
"v_rcp_f32 v153, v153\n"
"v_mfma_f32_16x16x32_bf16 v[32:35], v[132:135], a[240:243], v[32:35]\n"
"v_mul_f32 v92, v92, v152\n"
"v_mfma_f32_16x16x32_bf16 v[36:39], v[128:131], a[228:231], v[36:39]\n"
"v_rcp_f32 v154, v154\n"
"v_mfma_f32_16x16x32_bf16 v[36:39], v[132:135], a[244:247], v[36:39]\n"
"s_waitcnt lgkmcnt(0)\n"
"s_waitcnt vmcnt(13)\n"
"s_barrier\n"
"v_rcp_f32 v155, v155\n"
"v_mfma_f32_16x16x32_bf16 v[40:43], v[128:131], a[232:235], v[40:43]\n"
"ds_read_b64_tr_b16 v[144:145], v168 offset:16384\n"
"s_add_u32 m0, s91, 0\n"
"v_mul_f32 v93, v93, v153\n"
"v_mfma_f32_16x16x32_bf16 v[40:43], v[132:135], a[248:251], v[40:43]\n"
"ds_read_b64_tr_b16 v[146:147], v168 offset:16896\n"
"buffer_load_dwordx4 v176, s[40:43], s67 offen offset:0 lds\n"
"v_mfma_f32_16x16x32_bf16 v[44:47], v[128:131], a[236:239], v[44:47]\n"
"ds_read_b64_tr_b16 v[148:149], v168 offset:18432\n"
"v_mul_f32 v94, v94, v154\n"
"v_mfma_f32_16x16x32_bf16 v[44:47], v[132:135], a[252:255], v[44:47]\n"
"ds_read_b64_tr_b16 v[150:151], v168 offset:18944\n"
"v_mul_f32 v95, v95, v155\n"
"v_mfma_f32_16x16x32_bf16 v[48:51], v[136:139], a[224:227], v[48:51]\n"
"ds_read_b64_tr_b16 v[128:129], v168 offset:17408\n"
"v_cvt_pk_bf16_f32 v164, v92, v93\n"
"v_mfma_f32_16x16x32_bf16 v[48:51], v[140:143], a[240:243], v[48:51]\n"
"ds_read_b64_tr_b16 v[130:131], v168 offset:17920\n"
"v_cvt_pk_bf16_f32 v165, v94, v95\n"
"v_mfma_f32_16x16x32_bf16 v[52:55], v[136:139], a[228:231], v[52:55]\n"
"ds_read_b64_tr_b16 v[132:133], v168 offset:19456\n"
"v_mul_f32 v158, v110, v110\n"
"v_mfma_f32_16x16x32_bf16 v[52:55], v[140:143], a[244:247], v[52:55]\n"
"ds_read_b64_tr_b16 v[134:135], v168 offset:19968\n"
"v_mul_f32 v159, v111, v111\n"
"v_mfma_f32_16x16x32_bf16 v[56:59], v[136:139], a[232:235], v[56:59]\n"
"buffer_load_dwordx4 v176, s[44:47], s67 offen offset:1024 lds\n"
"v_mfma_f32_16x16x32_bf16 v[56:59], v[140:143], a[248:251], v[56:59]\n"
"v_fma_f32 v156, v156, s94, v193\n"
"v_fma_f32 v157, v157, s94, v193\n"
"v_mfma_f32_16x16x32_bf16 v[60:63], v[136:139], a[236:239], v[60:63]\n"
"v_fma_f32 v158, v158, s94, v193\n"
"v_fma_f32 v159, v159, s94, v193\n"
"v_mfma_f32_16x16x32_bf16 v[60:63], v[140:143], a[252:255], v[60:63]\n"
"v_mul_f32 v156, v156, v108\n"
"v_mul_f32 v157, v157, v109\n"
"s_waitcnt lgkmcnt(4)\n"
"v_mfma_f32_16x16x32_bf16 v[0:3], v[144:147], v[224:227], v[0:3]\n"
"ds_read_b64_tr_b16 v[136:137], v170 offset:16384\n"
"v_mul_f32 v158, v158, v110\n"
"v_mfma_f32_16x16x32_bf16 v[0:3], v[148:151], v[240:243], v[0:3]\n"
"ds_read_b64_tr_b16 v[138:139], v170 offset:16896\n"
"v_mul_f32 v159, v159, v111\n"
"v_mfma_f32_16x16x32_bf16 v[4:7], v[144:147], v[228:231], v[4:7]\n"
"ds_read_b64_tr_b16 v[140:141], v170 offset:18432\n"
"v_mul_f32 v124, v108, v124\n"
"v_mfma_f32_16x16x32_bf16 v[4:7], v[148:151], v[244:247], v[4:7]\n"
"ds_read_b64_tr_b16 v[142:143], v170 offset:18944\n"
"v_mul_f32 v125, v109, v125\n"
"v_mfma_f32_16x16x32_bf16 v[8:11], v[144:147], v[232:235], v[8:11]\n"
"buffer_load_dwordx4 v177, s[40:43], s67 offen offset:2048 lds\n"
"v_mfma_f32_16x16x32_bf16 v[8:11], v[148:151], v[248:251], v[8:11]\n"
"v_exp_f32 v156, v156\n"
"v_mfma_f32_16x16x32_bf16 v[12:15], v[144:147], v[236:239], v[12:15]\n"
"v_exp_f32 v157, v157\n"
"v_mfma_f32_16x16x32_bf16 v[12:15], v[148:151], v[252:255], v[12:15]\n"
"v_exp_f32 v158, v158\n"
"s_waitcnt lgkmcnt(4)\n"
"v_mfma_f32_16x16x32_bf16 v[16:19], v[128:131], v[224:227], v[16:19]\n"
"ds_read_b64_tr_b16 v[144:145], v170 offset:17408\n"
"v_mul_f32 v126, v110, v126\n"
"v_mfma_f32_16x16x32_bf16 v[16:19], v[132:135], v[240:243], v[16:19]\n"
"ds_read_b64_tr_b16 v[146:147], v170 offset:17920\n"
"v_mul_f32 v127, v111, v127\n"
"v_mfma_f32_16x16x32_bf16 v[20:23], v[128:131], v[228:231], v[20:23]\n"
"ds_read_b64_tr_b16 v[148:149], v170 offset:19456\n"
"v_add_f32 v156, 1.0, v156\n"
"v_mfma_f32_16x16x32_bf16 v[20:23], v[132:135], v[244:247], v[20:23]\n"
"ds_read_b64_tr_b16 v[150:151], v170 offset:19968\n"
"v_add_f32 v157, 1.0, v157\n"
"v_mfma_f32_16x16x32_bf16 v[24:27], v[128:131], v[232:235], v[24:27]\n"
"buffer_load_dwordx4 v177, s[44:47], s67 offen offset:3072 lds\n"
"v_mfma_f32_16x16x32_bf16 v[24:27], v[132:135], v[248:251], v[24:27]\n"
"v_exp_f32 v159, v159\n"
"v_mfma_f32_16x16x32_bf16 v[28:31], v[128:131], v[236:239], v[28:31]\n"
"v_add_f32 v158, 1.0, v158\n"
"v_mfma_f32_16x16x32_bf16 v[28:31], v[132:135], v[252:255], v[28:31]\n"
"v_add_f32 v159, 1.0, v159\n"
"s_waitcnt lgkmcnt(4)\n"
"v_mfma_f32_16x16x32_bf16 v[32:35], v[136:139], v[224:227], v[32:35]\n"
"v_rcp_f32 v156, v156\n"
"v_mfma_f32_16x16x32_bf16 v[32:35], v[140:143], v[240:243], v[32:35]\n"
"v_rcp_f32 v157, v157\n"
"v_mfma_f32_16x16x32_bf16 v[36:39], v[136:139], v[228:231], v[36:39]\n"
"v_rcp_f32 v158, v158\n"
"v_mfma_f32_16x16x32_bf16 v[36:39], v[140:143], v[244:247], v[36:39]\n"
"s_waitcnt lgkmcnt(0)\n"
"s_waitcnt vmcnt(13)\n"
"s_barrier\n"
"v_rcp_f32 v159, v159\n"
"v_mfma_f32_16x16x32_bf16 v[40:43], v[136:139], v[232:235], v[40:43]\n"
"ds_read_b64_tr_b16 v[128:129], v169 offset:0\n"
"s_add_u32 m0, s91, 0x4000\n"
"v_mul_f32 v124, v124, v156\n"
"v_mfma_f32_16x16x32_bf16 v[40:43], v[140:143], v[248:251], v[40:43]\n"
"ds_read_b64_tr_b16 v[130:131], v169 offset:512\n"
"buffer_load_dwordx4 v176, s[40:43], s68 offen offset:0 lds\n"
"v_mfma_f32_16x16x32_bf16 v[44:47], v[136:139], v[236:239], v[44:47]\n"
"ds_read_b64_tr_b16 v[132:133], v169 offset:2048\n"
"v_mul_f32 v125, v125, v157\n"
"v_mfma_f32_16x16x32_bf16 v[44:47], v[140:143], v[252:255], v[44:47]\n"
"ds_read_b64_tr_b16 v[134:135], v169 offset:2560\n"
"v_mul_f32 v126, v126, v158\n"
"v_mfma_f32_16x16x32_bf16 v[48:51], v[144:147], v[224:227], v[48:51]\n"
"ds_read_b64_tr_b16 v[136:137], v169 offset:1024\n"
"v_mul_f32 v127, v127, v159\n"
"v_mfma_f32_16x16x32_bf16 v[48:51], v[148:151], v[240:243], v[48:51]\n"
"ds_read_b64_tr_b16 v[138:139], v169 offset:1536\n"
"v_cvt_pk_bf16_f32 v166, v124, v125\n"
"v_mfma_f32_16x16x32_bf16 v[52:55], v[144:147], v[228:231], v[52:55]\n"
"ds_read_b64_tr_b16 v[140:141], v169 offset:3072\n"
"v_cvt_pk_bf16_f32 v167, v126, v127\n"
"v_mfma_f32_16x16x32_bf16 v[52:55], v[148:151], v[244:247], v[52:55]\n"
"ds_read_b64_tr_b16 v[142:143], v169 offset:3584\n"
"v_mfma_f32_16x16x32_bf16 v[56:59], v[144:147], v[232:235], v[56:59]\n"
"buffer_load_dwordx4 v176, s[44:47], s68 offen offset:1024 lds\n"
"v_mfma_f32_16x16x32_bf16 v[56:59], v[148:151], v[248:251], v[56:59]\n"
"ds_write_b128 v174, v[164:167] offset:19456\n"
"v_mfma_f32_16x16x32_bf16 v[60:63], v[144:147], v[236:239], v[60:63]\n"
"v_mfma_f32_16x16x32_bf16 v[60:63], v[148:151], v[252:255], v[60:63]\n"
"s_waitcnt lgkmcnt(5)\n"
"v_mfma_f32_16x16x32_bf16 v[64:67], v[128:131], a[0:3], 0\n"
"ds_read_b64_tr_b16 v[144:145], v171 offset:0\n"
"v_mfma_f32_16x16x32_bf16 v[64:67], v[132:135], a[16:19], v[64:67]\n"
"ds_read_b64_tr_b16 v[146:147], v171 offset:512\n"
"v_mul_f32 v216, v0, v0\n"
"v_mul_f32 v217, v1, v1\n"
"v_mfma_f32_16x16x32_bf16 v[68:71], v[128:131], a[4:7], 0\n"
"ds_read_b64_tr_b16 v[148:149], v171 offset:2048\n"
"v_mul_f32 v218, v2, v2\n"
"v_mfma_f32_16x16x32_bf16 v[68:71], v[132:135], a[20:23], v[68:71]\n"
"ds_read_b64_tr_b16 v[150:151], v171 offset:2560\n"
"v_mul_f32 v219, v3, v3\n"
"v_mfma_f32_16x16x32_bf16 v[72:75], v[128:131], a[8:11], 0\n"
"buffer_load_dwordx4 v177, s[40:43], s68 offen offset:2048 lds\n"
"v_mfma_f32_16x16x32_bf16 v[72:75], v[132:135], a[24:27], v[72:75]\n"
"v_fma_f32 v216, v216, s94, v193\n"
"v_fma_f32 v217, v217, s94, v193\n"
"v_mfma_f32_16x16x32_bf16 v[76:79], v[128:131], a[12:15], 0\n"
"v_fma_f32 v218, v218, s94, v193\n"
"v_fma_f32 v219, v219, s94, v193\n"
"v_mfma_f32_16x16x32_bf16 v[76:79], v[132:135], a[28:31], v[76:79]\n"
"v_mul_f32 v216, v216, v0\n"
"v_mul_f32 v217, v217, v1\n"
"s_waitcnt lgkmcnt(5)\n"
"v_mfma_f32_16x16x32_bf16 v[80:83], v[136:139], a[0:3], 0\n"
"ds_read_b64_tr_b16 v[128:129], v171 offset:1024\n"
"v_mul_f32 v218, v218, v2\n"
"v_mfma_f32_16x16x32_bf16 v[80:83], v[140:143], a[16:19], v[80:83]\n"
"ds_read_b64_tr_b16 v[130:131], v171 offset:1536\n"
"v_mul_f32 v219, v219, v3\n"
"v_mfma_f32_16x16x32_bf16 v[84:87], v[136:139], a[4:7], 0\n"
"ds_read_b64_tr_b16 v[132:133], v171 offset:3072\n"
"v_mul_f32 v16, v0, v16\n"
"v_mfma_f32_16x16x32_bf16 v[84:87], v[140:143], a[20:23], v[84:87]\n"
"ds_read_b64_tr_b16 v[134:135], v171 offset:3584\n"
"v_mul_f32 v17, v1, v17\n"
"v_mfma_f32_16x16x32_bf16 v[88:91], v[136:139], a[8:11], 0\n"
"buffer_load_dwordx4 v177, s[44:47], s68 offen offset:3072 lds\n"
"v_mfma_f32_16x16x32_bf16 v[88:91], v[140:143], a[24:27], v[88:91]\n"
"v_exp_f32 v216, v216\n"
"v_mfma_f32_16x16x32_bf16 v[92:95], v[136:139], a[12:15], 0\n"
"v_exp_f32 v217, v217\n"
"v_mfma_f32_16x16x32_bf16 v[92:95], v[140:143], a[28:31], v[92:95]\n"
"v_exp_f32 v218, v218\n"
"s_waitcnt lgkmcnt(4)\n"
"v_mfma_f32_16x16x32_bf16 v[96:99], v[144:147], a[0:3], 0\n"
"v_exp_f32 v219, v219\n"
"v_mfma_f32_16x16x32_bf16 v[96:99], v[148:151], a[16:19], v[96:99]\n"
"v_mul_f32 v18, v2, v18\n"
"v_mul_f32 v19, v3, v19\n"
"v_mfma_f32_16x16x32_bf16 v[100:103], v[144:147], a[4:7], 0\n"
"v_add_f32 v216, 1.0, v216\n"
"v_add_f32 v217, 1.0, v217\n"
"v_mfma_f32_16x16x32_bf16 v[100:103], v[148:151], a[20:23], v[100:103]\n"
"s_waitcnt lgkmcnt(0)\n"
"s_waitcnt vmcnt(12)\n"
"s_barrier\n"
"v_add_f32 v218, 1.0, v218\n"
"v_add_f32 v219, 1.0, v219\n"
"v_mfma_f32_16x16x32_bf16 v[104:107], v[144:147], a[8:11], 0\n"
"ds_read_b64_tr_b16 v[136:137], v169 offset:16384\n"
"s_add_u32 m0, s91, 0x8000\n"
"v_mul_f32 v152, v32, v32\n"
"v_mfma_f32_16x16x32_bf16 v[104:107], v[148:151], a[24:27], v[104:107]\n"
"ds_read_b64_tr_b16 v[138:139], v169 offset:16896\n"
"buffer_load_dwordx4 v176, s[40:43], s69 offen offset:0 lds\n"
"v_mfma_f32_16x16x32_bf16 v[108:111], v[144:147], a[12:15], 0\n"
"ds_read_b64_tr_b16 v[140:141], v169 offset:18432\n"
"v_mul_f32 v153, v33, v33\n"
"v_mfma_f32_16x16x32_bf16 v[108:111], v[148:151], a[28:31], v[108:111]\n"
"ds_read_b64_tr_b16 v[142:143], v169 offset:18944\n"
"v_mul_f32 v154, v34, v34\n"
"v_mfma_f32_16x16x32_bf16 v[112:115], v[128:131], a[0:3], 0\n"
"ds_read_b64_tr_b16 v[144:145], v169 offset:17408\n"
"v_mul_f32 v155, v35, v35\n"
"v_mfma_f32_16x16x32_bf16 v[112:115], v[132:135], a[16:19], v[112:115]\n"
"ds_read_b64_tr_b16 v[146:147], v169 offset:17920\n"
"v_fma_f32 v152, v152, s94, v193\n"
"v_mfma_f32_16x16x32_bf16 v[116:119], v[128:131], a[4:7], 0\n"
"ds_read_b64_tr_b16 v[148:149], v169 offset:19456\n"
"v_fma_f32 v153, v153, s94, v193\n"
"v_mfma_f32_16x16x32_bf16 v[116:119], v[132:135], a[20:23], v[116:119]\n"
"ds_read_b64_tr_b16 v[150:151], v169 offset:19968\n"
"v_fma_f32 v154, v154, s94, v193\n"
"v_mfma_f32_16x16x32_bf16 v[120:123], v[128:131], a[8:11], 0\n"
"buffer_load_dwordx4 v176, s[44:47], s69 offen offset:1024 lds\n"
"v_mfma_f32_16x16x32_bf16 v[120:123], v[132:135], a[24:27], v[120:123]\n"
"v_rcp_f32 v216, v216\n"
"v_mfma_f32_16x16x32_bf16 v[124:127], v[128:131], a[12:15], 0\n"
"v_rcp_f32 v217, v217\n"
"v_mfma_f32_16x16x32_bf16 v[124:127], v[132:135], a[28:31], v[124:127]\n"
"v_rcp_f32 v218, v218\n"
"s_waitcnt lgkmcnt(4)\n"
"v_mfma_f32_16x16x32_bf16 v[64:67], v[136:139], a[32:35], v[64:67]\n"
"ds_read_b64_tr_b16 v[128:129], v171 offset:16384\n"
"v_mul_f32 v16, v16, v216\n"
"v_mfma_f32_16x16x32_bf16 v[64:67], v[140:143], a[48:51], v[64:67]\n"
"ds_read_b64_tr_b16 v[130:131], v171 offset:16896\n"
"v_mul_f32 v17, v17, v217\n"
"v_mfma_f32_16x16x32_bf16 v[68:71], v[136:139], a[36:39], v[68:71]\n"
"ds_read_b64_tr_b16 v[132:133], v171 offset:18432\n"
"v_mul_f32 v18, v18, v218\n"
"v_mfma_f32_16x16x32_bf16 v[68:71], v[140:143], a[52:55], v[68:71]\n"
"ds_read_b64_tr_b16 v[134:135], v171 offset:18944\n"
"v_cvt_pk_bf16_f32 v160, v16, v17\n"
"v_mfma_f32_16x16x32_bf16 v[72:75], v[136:139], a[40:43], v[72:75]\n"
"buffer_load_dwordx4 v177, s[40:43], s69 offen offset:2048 lds\n"
"v_mfma_f32_16x16x32_bf16 v[72:75], v[140:143], a[56:59], v[72:75]\n"
"v_rcp_f32 v219, v219\n"
"v_mfma_f32_16x16x32_bf16 v[76:79], v[136:139], a[44:47], v[76:79]\n"
"v_mul_f32 v19, v19, v219\n"
"v_mfma_f32_16x16x32_bf16 v[76:79], v[140:143], a[60:63], v[76:79]\n"
"v_cvt_pk_bf16_f32 v161, v18, v19\n"
"v_fma_f32 v155, v155, s94, v193\n"
"s_waitcnt lgkmcnt(4)\n"
"v_mfma_f32_16x16x32_bf16 v[80:83], v[144:147], a[32:35], v[80:83]\n"
"ds_read_b64_tr_b16 v[136:137], v171 offset:17408\n"
"v_mul_f32 v152, v152, v32\n"
"v_mfma_f32_16x16x32_bf16 v[80:83], v[148:151], a[48:51], v[80:83]\n"
"ds_read_b64_tr_b16 v[138:139], v171 offset:17920\n"
"v_mul_f32 v153, v153, v33\n"
"v_mfma_f32_16x16x32_bf16 v[84:87], v[144:147], a[36:39], v[84:87]\n"
"ds_read_b64_tr_b16 v[140:141], v171 offset:19456\n"
"v_mul_f32 v154, v154, v34\n"
"v_mfma_f32_16x16x32_bf16 v[84:87], v[148:151], a[52:55], v[84:87]\n"
"ds_read_b64_tr_b16 v[142:143], v171 offset:19968\n"
"v_mul_f32 v155, v155, v35\n"
"v_mfma_f32_16x16x32_bf16 v[88:91], v[144:147], a[40:43], v[88:91]\n"
"buffer_load_dwordx4 v177, s[44:47], s69 offen offset:3072 lds\n"
"v_mfma_f32_16x16x32_bf16 v[88:91], v[148:151], a[56:59], v[88:91]\n"
"v_exp_f32 v152, v152\n"
"v_mfma_f32_16x16x32_bf16 v[92:95], v[144:147], a[44:47], v[92:95]\n"
"v_exp_f32 v153, v153\n"
"v_mfma_f32_16x16x32_bf16 v[92:95], v[148:151], a[60:63], v[92:95]\n"
"v_exp_f32 v154, v154\n"
"s_waitcnt lgkmcnt(4)\n"
"v_mfma_f32_16x16x32_bf16 v[96:99], v[128:131], a[32:35], v[96:99]\n"
"v_mul_f32 v48, v32, v48\n"
"v_mfma_f32_16x16x32_bf16 v[96:99], v[132:135], a[48:51], v[96:99]\n"
"v_exp_f32 v155, v155\n"
"v_mfma_f32_16x16x32_bf16 v[100:103], v[128:131], a[36:39], v[100:103]\n"
"v_mul_f32 v49, v33, v49\n"
"v_mul_f32 v50, v34, v50\n"
"v_mfma_f32_16x16x32_bf16 v[100:103], v[132:135], a[52:55], v[100:103]\n"
"s_waitcnt lgkmcnt(0)\n"
"s_waitcnt vmcnt(12)\n"
"s_barrier\n"
"v_mul_f32 v51, v35, v51\n"
"v_add_f32 v152, 1.0, v152\n"
"v_mfma_f32_16x16x32_bf16 v[104:107], v[128:131], a[40:43], v[104:107]\n"
"ds_read_b64_tr_b16 v[144:145], v169 offset:32768\n"
"s_add_u32 m0, s91, 0xc000\n"
"v_add_f32 v153, 1.0, v153\n"
"v_mfma_f32_16x16x32_bf16 v[104:107], v[132:135], a[56:59], v[104:107]\n"
"ds_read_b64_tr_b16 v[146:147], v169 offset:33280\n"
"buffer_load_dwordx4 v176, s[40:43], s70 offen offset:0 lds\n"
"v_mfma_f32_16x16x32_bf16 v[108:111], v[128:131], a[44:47], v[108:111]\n"
"ds_read_b64_tr_b16 v[148:149], v169 offset:34816\n"
"v_add_f32 v154, 1.0, v154\n"
"v_mfma_f32_16x16x32_bf16 v[108:111], v[132:135], a[60:63], v[108:111]\n"
"ds_read_b64_tr_b16 v[150:151], v169 offset:35328\n"
"v_add_f32 v155, 1.0, v155\n"
"v_mfma_f32_16x16x32_bf16 v[112:115], v[136:139], a[32:35], v[112:115]\n"
"ds_read_b64_tr_b16 v[128:129], v169 offset:33792\n"
"v_mul_f32 v156, v4, v4\n"
"v_mfma_f32_16x16x32_bf16 v[112:115], v[140:143], a[48:51], v[112:115]\n"
"ds_read_b64_tr_b16 v[130:131], v169 offset:34304\n"
"v_mul_f32 v157, v5, v5\n"
"v_mfma_f32_16x16x32_bf16 v[116:119], v[136:139], a[36:39], v[116:119]\n"
"ds_read_b64_tr_b16 v[132:133], v169 offset:35840\n"
"v_mul_f32 v158, v6, v6\n"
"v_mfma_f32_16x16x32_bf16 v[116:119], v[140:143], a[52:55], v[116:119]\n"
"ds_read_b64_tr_b16 v[134:135], v169 offset:36352\n"
"v_mul_f32 v159, v7, v7\n"
"v_mfma_f32_16x16x32_bf16 v[120:123], v[136:139], a[40:43], v[120:123]\n"
"buffer_load_dwordx4 v176, s[44:47], s70 offen offset:1024 lds\n"
"v_mfma_f32_16x16x32_bf16 v[120:123], v[140:143], a[56:59], v[120:123]\n"
"v_rcp_f32 v152, v152\n"
"v_mfma_f32_16x16x32_bf16 v[124:127], v[136:139], a[44:47], v[124:127]\n"
"v_rcp_f32 v153, v153\n"
"v_mfma_f32_16x16x32_bf16 v[124:127], v[140:143], a[60:63], v[124:127]\n"
"v_rcp_f32 v154, v154\n"
"s_waitcnt lgkmcnt(4)\n"
"v_mfma_f32_16x16x32_bf16 v[64:67], v[144:147], a[64:67], v[64:67]\n"
"ds_read_b64_tr_b16 v[136:137], v171 offset:32768\n"
"v_mul_f32 v48, v48, v152\n"
"v_mfma_f32_16x16x32_bf16 v[64:67], v[148:151], a[80:83], v[64:67]\n"
"ds_read_b64_tr_b16 v[138:139], v171 offset:33280\n"
"v_mul_f32 v49, v49, v153\n"
"v_mfma_f32_16x16x32_bf16 v[68:71], v[144:147], a[68:71], v[68:71]\n"
"ds_read_b64_tr_b16 v[140:141], v171 offset:34816\n"
"v_mul_f32 v50, v50, v154\n"
"v_mfma_f32_16x16x32_bf16 v[68:71], v[148:151], a[84:87], v[68:71]\n"
"ds_read_b64_tr_b16 v[142:143], v171 offset:35328\n"
"v_cvt_pk_bf16_f32 v162, v48, v49\n"
"v_mfma_f32_16x16x32_bf16 v[72:75], v[144:147], a[72:75], v[72:75]\n"
"buffer_load_dwordx4 v177, s[40:43], s70 offen offset:2048 lds\n"
"v_mfma_f32_16x16x32_bf16 v[72:75], v[148:151], a[88:91], v[72:75]\n"
"v_rcp_f32 v155, v155\n"
"v_mfma_f32_16x16x32_bf16 v[76:79], v[144:147], a[76:79], v[76:79]\n"
"v_mul_f32 v51, v51, v155\n"
"v_mfma_f32_16x16x32_bf16 v[76:79], v[148:151], a[92:95], v[76:79]\n"
"v_cvt_pk_bf16_f32 v163, v50, v51\n"
"v_fma_f32 v156, v156, s94, v193\n"
"s_waitcnt lgkmcnt(4)\n"
"v_mfma_f32_16x16x32_bf16 v[80:83], v[128:131], a[64:67], v[80:83]\n"
"ds_read_b64_tr_b16 v[144:145], v171 offset:33792\n"
"v_fma_f32 v157, v157, s94, v193\n"
"v_mfma_f32_16x16x32_bf16 v[80:83], v[132:135], a[80:83], v[80:83]\n"
"ds_read_b64_tr_b16 v[146:147], v171 offset:34304\n"
"v_fma_f32 v158, v158, s94, v193\n"
"v_mfma_f32_16x16x32_bf16 v[84:87], v[128:131], a[68:71], v[84:87]\n"
"ds_read_b64_tr_b16 v[148:149], v171 offset:35840\n"
"v_fma_f32 v159, v159, s94, v193\n"
"v_mfma_f32_16x16x32_bf16 v[84:87], v[132:135], a[84:87], v[84:87]\n"
"ds_read_b64_tr_b16 v[150:151], v171 offset:36352\n"
"v_mul_f32 v156, v156, v4\n"
"v_mfma_f32_16x16x32_bf16 v[88:91], v[128:131], a[72:75], v[88:91]\n"
"buffer_load_dwordx4 v177, s[44:47], s70 offen offset:3072 lds\n"
"v_mfma_f32_16x16x32_bf16 v[88:91], v[132:135], a[88:91], v[88:91]\n"
"v_mul_f32 v157, v157, v5\n"
"v_mul_f32 v158, v158, v6\n"
"v_mfma_f32_16x16x32_bf16 v[92:95], v[128:131], a[76:79], v[92:95]\n"
"v_mul_f32 v159, v159, v7\n"
"v_mul_f32 v20, v4, v20\n"
"v_mfma_f32_16x16x32_bf16 v[92:95], v[132:135], a[92:95], v[92:95]\n"
"v_exp_f32 v156, v156\n"
"s_waitcnt lgkmcnt(4)\n"
"v_mfma_f32_16x16x32_bf16 v[96:99], v[136:139], a[64:67], v[96:99]\n"
"v_exp_f32 v157, v157\n"
"v_mfma_f32_16x16x32_bf16 v[96:99], v[140:143], a[80:83], v[96:99]\n"
"v_mul_f32 v21, v5, v21\n"
"v_mfma_f32_16x16x32_bf16 v[100:103], v[136:139], a[68:71], v[100:103]\n"
"v_exp_f32 v158, v158\n"
"v_mfma_f32_16x16x32_bf16 v[100:103], v[140:143], a[84:87], v[100:103]\n"
"s_waitcnt lgkmcnt(0)\n"
"s_waitcnt vmcnt(12)\n"
"s_barrier\n"
"v_exp_f32 v159, v159\n"
"v_mfma_f32_16x16x32_bf16 v[104:107], v[136:139], a[72:75], v[104:107]\n"
"ds_read_b64_tr_b16 v[128:129], v168 offset:0\n"
"s_add_u32 m0, s91, 0x10000\n"
"v_mul_f32 v22, v6, v22\n"
"v_mfma_f32_16x16x32_bf16 v[104:107], v[140:143], a[88:91], v[104:107]\n"
"ds_read_b64_tr_b16 v[130:131], v168 offset:512\n"
"buffer_load_dwordx4 v176, s[40:43], s71 offen offset:0 lds\n"
"v_mfma_f32_16x16x32_bf16 v[108:111], v[136:139], a[76:79], v[108:111]\n"
"ds_read_b64_tr_b16 v[132:133], v168 offset:2048\n"
"v_mul_f32 v23, v7, v23\n"
"v_mfma_f32_16x16x32_bf16 v[108:111], v[140:143], a[92:95], v[108:111]\n"
"ds_read_b64_tr_b16 v[134:135], v168 offset:2560\n"
"v_add_f32 v156, 1.0, v156\n"
"v_mfma_f32_16x16x32_bf16 v[112:115], v[144:147], a[64:67], v[112:115]\n"
"ds_read_b64_tr_b16 v[136:137], v168 offset:1024\n"
"v_add_f32 v157, 1.0, v157\n"
"v_mfma_f32_16x16x32_bf16 v[112:115], v[148:151], a[80:83], v[112:115]\n"
"ds_read_b64_tr_b16 v[138:139], v168 offset:1536\n"
"v_add_f32 v158, 1.0, v158\n"
"v_mfma_f32_16x16x32_bf16 v[116:119], v[144:147], a[68:71], v[116:119]\n"
"ds_read_b64_tr_b16 v[140:141], v168 offset:3072\n"
"v_add_f32 v159, 1.0, v159\n"
"v_mfma_f32_16x16x32_bf16 v[116:119], v[148:151], a[84:87], v[116:119]\n"
"ds_read_b64_tr_b16 v[142:143], v168 offset:3584\n"
"v_mul_f32 v216, v36, v36\n"
"v_mfma_f32_16x16x32_bf16 v[120:123], v[144:147], a[72:75], v[120:123]\n"
"buffer_load_dwordx4 v176, s[44:47], s71 offen offset:1024 lds\n"
"v_mfma_f32_16x16x32_bf16 v[120:123], v[148:151], a[88:91], v[120:123]\n"
"v_rcp_f32 v156, v156\n"
"v_mfma_f32_16x16x32_bf16 v[124:127], v[144:147], a[76:79], v[124:127]\n"
"v_rcp_f32 v157, v157\n"
"v_mfma_f32_16x16x32_bf16 v[124:127], v[148:151], a[92:95], v[124:127]\n"
"v_rcp_f32 v158, v158\n"
"s_waitcnt lgkmcnt(4)\n"
"v_mfma_f32_16x16x32_bf16 v[64:67], v[128:131], a[96:99], v[64:67]\n"
"ds_read_b64_tr_b16 v[144:145], v170 offset:0\n"
"v_mul_f32 v20, v20, v156\n"
"v_mfma_f32_16x16x32_bf16 v[64:67], v[132:135], a[112:115], v[64:67]\n"
"ds_read_b64_tr_b16 v[146:147], v170 offset:512\n"
"v_mul_f32 v21, v21, v157\n"
"v_mfma_f32_16x16x32_bf16 v[68:71], v[128:131], a[100:103], v[68:71]\n"
"ds_read_b64_tr_b16 v[148:149], v170 offset:2048\n"
"v_mul_f32 v22, v22, v158\n"
"v_mfma_f32_16x16x32_bf16 v[68:71], v[132:135], a[116:119], v[68:71]\n"
"ds_read_b64_tr_b16 v[150:151], v170 offset:2560\n"
"v_cvt_pk_bf16_f32 v164, v20, v21\n"
"v_mfma_f32_16x16x32_bf16 v[72:75], v[128:131], a[104:107], v[72:75]\n"
"buffer_load_dwordx4 v177, s[40:43], s71 offen offset:2048 lds\n"
"v_mfma_f32_16x16x32_bf16 v[72:75], v[132:135], a[120:123], v[72:75]\n"
"v_rcp_f32 v159, v159\n"
"v_mfma_f32_16x16x32_bf16 v[76:79], v[128:131], a[108:111], v[76:79]\n"
"v_mul_f32 v23, v23, v159\n"
"v_mfma_f32_16x16x32_bf16 v[76:79], v[132:135], a[124:127], v[76:79]\n"
"v_cvt_pk_bf16_f32 v165, v22, v23\n"
"v_mul_f32 v217, v37, v37\n"
"s_waitcnt lgkmcnt(4)\n"
"v_mfma_f32_16x16x32_bf16 v[80:83], v[136:139], a[96:99], v[80:83]\n"
"ds_read_b64_tr_b16 v[128:129], v170 offset:1024\n"
"v_mul_f32 v218, v38, v38\n"
"v_mfma_f32_16x16x32_bf16 v[80:83], v[140:143], a[112:115], v[80:83]\n"
"ds_read_b64_tr_b16 v[130:131], v170 offset:1536\n"
"v_mul_f32 v219, v39, v39\n"
"v_mfma_f32_16x16x32_bf16 v[84:87], v[136:139], a[100:103], v[84:87]\n"
"ds_read_b64_tr_b16 v[132:133], v170 offset:3072\n"
"v_fma_f32 v216, v216, s94, v193\n"
"v_mfma_f32_16x16x32_bf16 v[84:87], v[140:143], a[116:119], v[84:87]\n"
"ds_read_b64_tr_b16 v[134:135], v170 offset:3584\n"
"v_fma_f32 v217, v217, s94, v193\n"
"v_mfma_f32_16x16x32_bf16 v[88:91], v[136:139], a[104:107], v[88:91]\n"
"buffer_load_dwordx4 v177, s[44:47], s71 offen offset:3072 lds\n"
"v_mfma_f32_16x16x32_bf16 v[88:91], v[140:143], a[120:123], v[88:91]\n"
"v_fma_f32 v218, v218, s94, v193\n"
"v_fma_f32 v219, v219, s94, v193\n"
"v_mfma_f32_16x16x32_bf16 v[92:95], v[136:139], a[108:111], v[92:95]\n"
"v_mul_f32 v216, v216, v36\n"
"v_mul_f32 v217, v217, v37\n"
"v_mfma_f32_16x16x32_bf16 v[92:95], v[140:143], a[124:127], v[92:95]\n"
"v_mul_f32 v218, v218, v38\n"
"v_mul_f32 v219, v219, v39\n"
"s_waitcnt lgkmcnt(4)\n"
"v_mfma_f32_16x16x32_bf16 v[96:99], v[144:147], a[96:99], v[96:99]\n"
"v_exp_f32 v216, v216\n"
"v_mfma_f32_16x16x32_bf16 v[96:99], v[148:151], a[112:115], v[96:99]\n"
"v_mul_f32 v52, v36, v52\n"
"v_mfma_f32_16x16x32_bf16 v[100:103], v[144:147], a[100:103], v[100:103]\n"
"v_exp_f32 v217, v217\n"
"v_mfma_f32_16x16x32_bf16 v[100:103], v[148:151], a[116:119], v[100:103]\n"
"s_waitcnt lgkmcnt(0)\n"
"s_waitcnt vmcnt(12)\n"
"s_barrier\n"
"v_exp_f32 v218, v218\n"
"v_mfma_f32_16x16x32_bf16 v[104:107], v[144:147], a[104:107], v[104:107]\n"
"ds_read_b64_tr_b16 v[136:137], v168 offset:16384\n"
"s_add_u32 m0, s91, 0\n"
"v_mul_f32 v53, v37, v53\n"
"v_mfma_f32_16x16x32_bf16 v[104:107], v[148:151], a[120:123], v[104:107]\n"
"ds_read_b64_tr_b16 v[138:139], v168 offset:16896\n"
"buffer_load_dwordx4 v176, s[40:43], s72 offen offset:0 lds\n"
"v_mfma_f32_16x16x32_bf16 v[108:111], v[144:147], a[108:111], v[108:111]\n"
"ds_read_b64_tr_b16 v[140:141], v168 offset:18432\n"
"v_mul_f32 v54, v38, v54\n"
"v_mfma_f32_16x16x32_bf16 v[108:111], v[148:151], a[124:127], v[108:111]\n"
"ds_read_b64_tr_b16 v[142:143], v168 offset:18944\n"
"v_mul_f32 v55, v39, v55\n"
"v_mfma_f32_16x16x32_bf16 v[112:115], v[128:131], a[96:99], v[112:115]\n"
"ds_read_b64_tr_b16 v[144:145], v168 offset:17408\n"
"v_add_f32 v216, 1.0, v216\n"
"v_mfma_f32_16x16x32_bf16 v[112:115], v[132:135], a[112:115], v[112:115]\n"
"ds_read_b64_tr_b16 v[146:147], v168 offset:17920\n"
"v_add_f32 v217, 1.0, v217\n"
"v_mfma_f32_16x16x32_bf16 v[116:119], v[128:131], a[100:103], v[116:119]\n"
"ds_read_b64_tr_b16 v[148:149], v168 offset:19456\n"
"v_add_f32 v218, 1.0, v218\n"
"v_mfma_f32_16x16x32_bf16 v[116:119], v[132:135], a[116:119], v[116:119]\n"
"ds_read_b64_tr_b16 v[150:151], v168 offset:19968\n"
"v_mul_f32 v152, v8, v8\n"
"v_mfma_f32_16x16x32_bf16 v[120:123], v[128:131], a[104:107], v[120:123]\n"
"buffer_load_dwordx4 v176, s[44:47], s72 offen offset:1024 lds\n"
"v_mfma_f32_16x16x32_bf16 v[120:123], v[132:135], a[120:123], v[120:123]\n"
"v_exp_f32 v219, v219\n"
"v_mfma_f32_16x16x32_bf16 v[124:127], v[128:131], a[108:111], v[124:127]\n"
"v_add_f32 v219, 1.0, v219\n"
"v_mul_f32 v153, v9, v9\n"
"v_mfma_f32_16x16x32_bf16 v[124:127], v[132:135], a[124:127], v[124:127]\n"
"v_rcp_f32 v216, v216\n"
"s_waitcnt lgkmcnt(4)\n"
"v_mfma_f32_16x16x32_bf16 v[64:67], v[136:139], a[128:131], v[64:67]\n"
"ds_read_b64_tr_b16 v[128:129], v170 offset:16384\n"
"v_mul_f32 v52, v52, v216\n"
"v_mfma_f32_16x16x32_bf16 v[64:67], v[140:143], a[144:147], v[64:67]\n"
"ds_read_b64_tr_b16 v[130:131], v170 offset:16896\n"
"v_mfma_f32_16x16x32_bf16 v[68:71], v[136:139], a[132:135], v[68:71]\n"
"ds_read_b64_tr_b16 v[132:133], v170 offset:18432\n"
"v_mfma_f32_16x16x32_bf16 v[68:71], v[140:143], a[148:151], v[68:71]\n"
"ds_read_b64_tr_b16 v[134:135], v170 offset:18944\n"
"v_rcp_f32 v217, v217\n"
"v_mfma_f32_16x16x32_bf16 v[72:75], v[136:139], a[136:139], v[72:75]\n"
"buffer_load_dwordx4 v177, s[40:43], s72 offen offset:2048 lds\n"
"v_mfma_f32_16x16x32_bf16 v[72:75], v[140:143], a[152:155], v[72:75]\n"
"ds_write_b128 v174, v[160:163] offset:32768\n"
"v_mfma_f32_16x16x32_bf16 v[76:79], v[136:139], a[140:143], v[76:79]\n"
"v_rcp_f32 v218, v218\n"
"v_mfma_f32_16x16x32_bf16 v[76:79], v[140:143], a[156:159], v[76:79]\n"
"v_mul_f32 v53, v53, v217\n"
"s_waitcnt lgkmcnt(5)\n"
"v_mfma_f32_16x16x32_bf16 v[80:83], v[144:147], a[128:131], v[80:83]\n"
"ds_read_b64_tr_b16 v[136:137], v170 offset:17408\n"
"v_mul_f32 v54, v54, v218\n"
"v_mfma_f32_16x16x32_bf16 v[80:83], v[148:151], a[144:147], v[80:83]\n"
"ds_read_b64_tr_b16 v[138:139], v170 offset:17920\n"
"v_cvt_pk_bf16_f32 v166, v52, v53\n"
"v_mfma_f32_16x16x32_bf16 v[84:87], v[144:147], a[132:135], v[84:87]\n"
"ds_read_b64_tr_b16 v[140:141], v170 offset:19456\n"
"v_mul_f32 v154, v10, v10\n"
"v_mfma_f32_16x16x32_bf16 v[84:87], v[148:151], a[148:151], v[84:87]\n"
"ds_read_b64_tr_b16 v[142:143], v170 offset:19968\n"
"v_mul_f32 v155, v11, v11\n"
"v_mfma_f32_16x16x32_bf16 v[88:91], v[144:147], a[136:139], v[88:91]\n"
"buffer_load_dwordx4 v177, s[44:47], s72 offen offset:3072 lds\n"
"v_mfma_f32_16x16x32_bf16 v[88:91], v[148:151], a[152:155], v[88:91]\n"
"v_rcp_f32 v219, v219\n"
"v_mfma_f32_16x16x32_bf16 v[92:95], v[144:147], a[140:143], v[92:95]\n"
"v_mul_f32 v55, v55, v219\n"
"v_cvt_pk_bf16_f32 v167, v54, v55\n"
"v_mfma_f32_16x16x32_bf16 v[92:95], v[148:151], a[156:159], v[92:95]\n"
"v_fma_f32 v152, v152, s94, v193\n"
"v_fma_f32 v153, v153, s94, v193\n"
"s_waitcnt lgkmcnt(5)\n"
"v_mfma_f32_16x16x32_bf16 v[96:99], v[128:131], a[128:131], v[96:99]\n"
"v_fma_f32 v154, v154, s94, v193\n"
"v_fma_f32 v155, v155, s94, v193\n"
"v_mfma_f32_16x16x32_bf16 v[96:99], v[132:135], a[144:147], v[96:99]\n"
"v_mul_f32 v152, v152, v8\n"
"v_mfma_f32_16x16x32_bf16 v[100:103], v[128:131], a[132:135], v[100:103]\n"
"v_mul_f32 v153, v153, v9\n"
"v_mul_f32 v154, v154, v10\n"
"v_mfma_f32_16x16x32_bf16 v[100:103], v[132:135], a[148:151], v[100:103]\n"
"s_waitcnt lgkmcnt(0)\n"
"s_waitcnt vmcnt(12)\n"
"s_barrier\n"
"v_mul_f32 v155, v155, v11\n"
"v_mul_f32 v24, v8, v24\n"
"v_mfma_f32_16x16x32_bf16 v[104:107], v[128:131], a[136:139], v[104:107]\n"
"ds_read_b64_tr_b16 v[144:145], v169 offset:0\n"
"s_add_u32 m0, s92, 0x4000\n"
"v_mul_f32 v25, v9, v25\n"
"v_mfma_f32_16x16x32_bf16 v[104:107], v[132:135], a[152:155], v[104:107]\n"
"ds_read_b64_tr_b16 v[146:147], v169 offset:512\n"
"buffer_load_dwordx4 v178, s[40:43], s64 offen offset:0 lds\n"
"v_mfma_f32_16x16x32_bf16 v[108:111], v[128:131], a[140:143], v[108:111]\n"
"ds_read_b64_tr_b16 v[148:149], v169 offset:2048\n"
"v_mul_f32 v26, v10, v26\n"
"v_mfma_f32_16x16x32_bf16 v[108:111], v[132:135], a[156:159], v[108:111]\n"
"ds_read_b64_tr_b16 v[150:151], v169 offset:2560\n"
"v_mul_f32 v27, v11, v27\n"
"v_mfma_f32_16x16x32_bf16 v[112:115], v[136:139], a[128:131], v[112:115]\n"
"ds_read_b64_tr_b16 v[128:129], v169 offset:1024\n"
"v_mul_f32 v156, v40, v40\n"
"v_mfma_f32_16x16x32_bf16 v[112:115], v[140:143], a[144:147], v[112:115]\n"
"ds_read_b64_tr_b16 v[130:131], v169 offset:1536\n"
"v_mul_f32 v157, v41, v41\n"
"v_mfma_f32_16x16x32_bf16 v[116:119], v[136:139], a[132:135], v[116:119]\n"
"ds_read_b64_tr_b16 v[132:133], v169 offset:3072\n"
"v_mul_f32 v158, v42, v42\n"
"v_mfma_f32_16x16x32_bf16 v[116:119], v[140:143], a[148:151], v[116:119]\n"
"ds_read_b64_tr_b16 v[134:135], v169 offset:3584\n"
"v_mul_f32 v159, v43, v43\n"
"v_mfma_f32_16x16x32_bf16 v[120:123], v[136:139], a[136:139], v[120:123]\n"
"buffer_load_dwordx4 v178, s[44:47], s64 offen offset:1024 lds\n"
"v_mfma_f32_16x16x32_bf16 v[120:123], v[140:143], a[152:155], v[120:123]\n"
"v_exp_f32 v152, v152\n"
"v_mfma_f32_16x16x32_bf16 v[124:127], v[136:139], a[140:143], v[124:127]\n"
"s_add_u32 m0, s92, 0x6000\n"
"v_exp_f32 v153, v153\n"
"v_mfma_f32_16x16x32_bf16 v[124:127], v[140:143], a[156:159], v[124:127]\n"
"buffer_load_dwordx4 v178, s[40:43], s65 offen offset:0 lds\n"
"s_waitcnt lgkmcnt(4)\n"
"v_mfma_f32_16x16x32_bf16 v[64:67], v[144:147], a[160:163], v[64:67]\n"
"ds_read_b64_tr_b16 v[136:137], v171 offset:0\n"
"v_add_f32 v152, 1.0, v152\n"
"v_mfma_f32_16x16x32_bf16 v[64:67], v[148:151], a[176:179], v[64:67]\n"
"ds_read_b64_tr_b16 v[138:139], v171 offset:512\n"
"v_add_f32 v153, 1.0, v153\n"
"v_mfma_f32_16x16x32_bf16 v[68:71], v[144:147], a[164:167], v[68:71]\n"
"ds_read_b64_tr_b16 v[140:141], v171 offset:2048\n"
"v_fma_f32 v156, v156, s94, v193\n"
"v_mfma_f32_16x16x32_bf16 v[68:71], v[148:151], a[180:183], v[68:71]\n"
"ds_read_b64_tr_b16 v[142:143], v171 offset:2560\n"
"v_fma_f32 v157, v157, s94, v193\n"
"v_mfma_f32_16x16x32_bf16 v[72:75], v[144:147], a[168:171], v[72:75]\n"
"buffer_load_dwordx4 v178, s[44:47], s65 offen offset:1024 lds\n"
"v_mfma_f32_16x16x32_bf16 v[72:75], v[148:151], a[184:187], v[72:75]\n"
"v_exp_f32 v154, v154\n"
"v_mfma_f32_16x16x32_bf16 v[76:79], v[144:147], a[172:175], v[76:79]\n"
"v_exp_f32 v155, v155\n"
"v_mfma_f32_16x16x32_bf16 v[76:79], v[148:151], a[188:191], v[76:79]\n"
"v_add_f32 v154, 1.0, v154\n"
"v_add_f32 v155, 1.0, v155\n"
"s_waitcnt lgkmcnt(4)\n"
"v_mfma_f32_16x16x32_bf16 v[80:83], v[128:131], a[160:163], v[80:83]\n"
"ds_read_b64_tr_b16 v[144:145], v171 offset:1024\n"
"v_fma_f32 v158, v158, s94, v193\n"
"v_mfma_f32_16x16x32_bf16 v[80:83], v[132:135], a[176:179], v[80:83]\n"
"ds_read_b64_tr_b16 v[146:147], v171 offset:1536\n"
"v_fma_f32 v159, v159, s94, v193\n"
"v_mfma_f32_16x16x32_bf16 v[84:87], v[128:131], a[164:167], v[84:87]\n"
"ds_read_b64_tr_b16 v[148:149], v171 offset:3072\n"
"v_mul_f32 v156, v156, v40\n"
"v_mfma_f32_16x16x32_bf16 v[84:87], v[132:135], a[180:183], v[84:87]\n"
"ds_read_b64_tr_b16 v[150:151], v171 offset:3584\n"
"s_add_u32 m0, s92, 0x14000\n"
"v_mul_f32 v157, v157, v41\n"
"v_mfma_f32_16x16x32_bf16 v[88:91], v[128:131], a[168:171], v[88:91]\n"
"buffer_load_dwordx4 v178, s[40:43], s72 offen offset:0 lds\n"
"v_mfma_f32_16x16x32_bf16 v[88:91], v[132:135], a[184:187], v[88:91]\n"
"v_rcp_f32 v152, v152\n"
"v_mfma_f32_16x16x32_bf16 v[92:95], v[128:131], a[172:175], v[92:95]\n"
"buffer_load_dwordx4 v178, s[44:47], s72 offen offset:1024 lds\n"
"v_mfma_f32_16x16x32_bf16 v[92:95], v[132:135], a[188:191], v[92:95]\n"
"v_rcp_f32 v153, v153\n"
"s_waitcnt lgkmcnt(4)\n"
"v_mfma_f32_16x16x32_bf16 v[96:99], v[136:139], a[160:163], v[96:99]\n"
"v_rcp_f32 v154, v154\n"
"v_mfma_f32_16x16x32_bf16 v[96:99], v[140:143], a[176:179], v[96:99]\n"
"v_mul_f32 v24, v24, v152\n"
"v_mfma_f32_16x16x32_bf16 v[100:103], v[136:139], a[164:167], v[100:103]\n"
"v_rcp_f32 v155, v155\n"
"v_mfma_f32_16x16x32_bf16 v[100:103], v[140:143], a[180:183], v[100:103]\n"
"s_waitcnt lgkmcnt(0)\n"
"s_waitcnt vmcnt(14)\n"
"s_barrier\n"
"v_mul_f32 v25, v25, v153\n"
"v_mul_f32 v26, v26, v154\n"
"v_mfma_f32_16x16x32_bf16 v[104:107], v[136:139], a[168:171], v[104:107]\n"
"ds_read_b64_tr_b16 v[128:129], v169 offset:16384\n"
"s_add_u32 m0, s92, 0x8000\n"
"v_mul_f32 v27, v27, v155\n"
"v_mfma_f32_16x16x32_bf16 v[104:107], v[140:143], a[184:187], v[104:107]\n"
"ds_read_b64_tr_b16 v[130:131], v169 offset:16896\n"
"buffer_load_dwordx4 v178, s[40:43], s66 offen offset:0 lds\n"
"v_mfma_f32_16x16x32_bf16 v[108:111], v[136:139], a[172:175], v[108:111]\n"
"ds_read_b64_tr_b16 v[132:133], v169 offset:18432\n"
"v_cvt_pk_bf16_f32 v160, v24, v25\n"
"v_mfma_f32_16x16x32_bf16 v[108:111], v[140:143], a[188:191], v[108:111]\n"
"ds_read_b64_tr_b16 v[134:135], v169 offset:18944\n"
"v_cvt_pk_bf16_f32 v161, v26, v27\n"
"v_mfma_f32_16x16x32_bf16 v[112:115], v[144:147], a[160:163], v[112:115]\n"
"ds_read_b64_tr_b16 v[136:137], v169 offset:17408\n"
"v_mul_f32 v158, v158, v42\n"
"v_mfma_f32_16x16x32_bf16 v[112:115], v[148:151], a[176:179], v[112:115]\n"
"ds_read_b64_tr_b16 v[138:139], v169 offset:17920\n"
"v_mul_f32 v159, v159, v43\n"
"v_mfma_f32_16x16x32_bf16 v[116:119], v[144:147], a[164:167], v[116:119]\n"
"ds_read_b64_tr_b16 v[140:141], v169 offset:19456\n"
"v_mul_f32 v56, v40, v56\n"
"v_mfma_f32_16x16x32_bf16 v[116:119], v[148:151], a[180:183], v[116:119]\n"
"ds_read_b64_tr_b16 v[142:143], v169 offset:19968\n"
"v_mul_f32 v57, v41, v57\n"
"v_mfma_f32_16x16x32_bf16 v[120:123], v[144:147], a[168:171], v[120:123]\n"
"buffer_load_dwordx4 v178, s[44:47], s66 offen offset:1024 lds\n"
"v_mfma_f32_16x16x32_bf16 v[120:123], v[148:151], a[184:187], v[120:123]\n"
"v_exp_f32 v156, v156\n"
"v_mfma_f32_16x16x32_bf16 v[124:127], v[144:147], a[172:175], v[124:127]\n"
"v_exp_f32 v157, v157\n"
"v_mfma_f32_16x16x32_bf16 v[124:127], v[148:151], a[188:191], v[124:127]\n"
"v_exp_f32 v158, v158\n"
"s_waitcnt lgkmcnt(4)\n"
"v_mfma_f32_16x16x32_bf16 v[64:67], v[128:131], a[192:195], v[64:67]\n"
"ds_read_b64_tr_b16 v[144:145], v171 offset:16384\n"
"v_mul_f32 v58, v42, v58\n"
"v_mfma_f32_16x16x32_bf16 v[64:67], v[132:135], a[208:211], v[64:67]\n"
"ds_read_b64_tr_b16 v[146:147], v171 offset:16896\n"
"v_mul_f32 v59, v43, v59\n"
"v_mfma_f32_16x16x32_bf16 v[68:71], v[128:131], a[196:199], v[68:71]\n"
"ds_read_b64_tr_b16 v[148:149], v171 offset:18432\n"
"v_add_f32 v156, 1.0, v156\n"
"v_mfma_f32_16x16x32_bf16 v[68:71], v[132:135], a[212:215], v[68:71]\n"
"ds_read_b64_tr_b16 v[150:151], v171 offset:18944\n"
"s_add_u32 m0, s92, 0xa000\n"
"v_add_f32 v157, 1.0, v157\n"
"v_mfma_f32_16x16x32_bf16 v[72:75], v[128:131], a[200:203], v[72:75]\n"
"buffer_load_dwordx4 v178, s[40:43], s67 offen offset:0 lds\n"
"v_mfma_f32_16x16x32_bf16 v[72:75], v[132:135], a[216:219], v[72:75]\n"
"v_exp_f32 v159, v159\n"
"v_mfma_f32_16x16x32_bf16 v[76:79], v[128:131], a[204:207], v[76:79]\n"
"v_add_f32 v158, 1.0, v158\n"
"v_add_f32 v159, 1.0, v159\n"
"v_mfma_f32_16x16x32_bf16 v[76:79], v[132:135], a[220:223], v[76:79]\n"
"v_mul_f32 v216, v12, v12\n"
"s_waitcnt lgkmcnt(4)\n"
"v_mfma_f32_16x16x32_bf16 v[80:83], v[136:139], a[192:195], v[80:83]\n"
"ds_read_b64_tr_b16 v[128:129], v171 offset:17408\n"
"v_mul_f32 v217, v13, v13\n"
"v_mfma_f32_16x16x32_bf16 v[80:83], v[140:143], a[208:211], v[80:83]\n"
"ds_read_b64_tr_b16 v[130:131], v171 offset:17920\n"
"v_mfma_f32_16x16x32_bf16 v[84:87], v[136:139], a[196:199], v[84:87]\n"
"ds_read_b64_tr_b16 v[132:133], v171 offset:19456\n"
"v_mfma_f32_16x16x32_bf16 v[84:87], v[140:143], a[212:215], v[84:87]\n"
"ds_read_b64_tr_b16 v[134:135], v171 offset:19968\n"
"v_rcp_f32 v156, v156\n"
"v_mfma_f32_16x16x32_bf16 v[88:91], v[136:139], a[200:203], v[88:91]\n"
"buffer_load_dwordx4 v178, s[44:47], s67 offen offset:1024 lds\n"
"v_mfma_f32_16x16x32_bf16 v[88:91], v[140:143], a[216:219], v[88:91]\n"
"ds_write_b128 v174, v[164:167] offset:33792\n"
"v_mfma_f32_16x16x32_bf16 v[92:95], v[136:139], a[204:207], v[92:95]\n"
"v_rcp_f32 v157, v157\n"
"v_mfma_f32_16x16x32_bf16 v[92:95], v[140:143], a[220:223], v[92:95]\n"
"v_rcp_f32 v158, v158\n"
"s_waitcnt lgkmcnt(5)\n"
"v_mfma_f32_16x16x32_bf16 v[96:99], v[144:147], a[192:195], v[96:99]\n"
"v_rcp_f32 v159, v159\n"
"v_mfma_f32_16x16x32_bf16 v[96:99], v[148:151], a[208:211], v[96:99]\n"
"v_mul_f32 v56, v56, v156\n"
"v_mul_f32 v57, v57, v157\n"
"v_mfma_f32_16x16x32_bf16 v[100:103], v[144:147], a[196:199], v[100:103]\n"
"v_mul_f32 v58, v58, v158\n"
"v_mfma_f32_16x16x32_bf16 v[100:103], v[148:151], a[212:215], v[100:103]\n"
"s_waitcnt lgkmcnt(1)\n"
"s_waitcnt vmcnt(14)\n"
"s_barrier\n"
"v_mul_f32 v59, v59, v159\n"
"v_cvt_pk_bf16_f32 v162, v56, v57\n"
"v_mfma_f32_16x16x32_bf16 v[104:107], v[144:147], a[200:203], v[104:107]\n"
"ds_read_b64_tr_b16 v[136:137], v169 offset:32768\n"
"s_add_u32 m0, s92, 0xc000\n"
"v_cvt_pk_bf16_f32 v163, v58, v59\n"
"v_mfma_f32_16x16x32_bf16 v[104:107], v[148:151], a[216:219], v[104:107]\n"
"ds_read_b64_tr_b16 v[138:139], v169 offset:33280\n"
"buffer_load_dwordx4 v178, s[40:43], s68 offen offset:0 lds\n"
"v_mfma_f32_16x16x32_bf16 v[108:111], v[144:147], a[204:207], v[108:111]\n"
"ds_read_b64_tr_b16 v[140:141], v169 offset:34816\n"
"v_mul_f32 v218, v14, v14\n"
"v_mfma_f32_16x16x32_bf16 v[108:111], v[148:151], a[220:223], v[108:111]\n"
"ds_read_b64_tr_b16 v[142:143], v169 offset:35328\n"
"v_mul_f32 v219, v15, v15\n"
"v_mfma_f32_16x16x32_bf16 v[112:115], v[128:131], a[192:195], v[112:115]\n"
"ds_read_b64_tr_b16 v[144:145], v169 offset:33792\n"
"v_fma_f32 v216, v216, s94, v193\n"
"v_mfma_f32_16x16x32_bf16 v[112:115], v[132:135], a[208:211], v[112:115]\n"
"ds_read_b64_tr_b16 v[146:147], v169 offset:34304\n"
"v_fma_f32 v217, v217, s94, v193\n"
"v_mfma_f32_16x16x32_bf16 v[116:119], v[128:131], a[196:199], v[116:119]\n"
"ds_read_b64_tr_b16 v[148:149], v169 offset:35840\n"
"v_fma_f32 v218, v218, s94, v193\n"
"v_mfma_f32_16x16x32_bf16 v[116:119], v[132:135], a[212:215], v[116:119]\n"
"ds_read_b64_tr_b16 v[150:151], v169 offset:36352\n"
"v_fma_f32 v219, v219, s94, v193\n"
"v_mfma_f32_16x16x32_bf16 v[120:123], v[128:131], a[200:203], v[120:123]\n"
"buffer_load_dwordx4 v178, s[44:47], s68 offen offset:1024 lds\n"
"v_mfma_f32_16x16x32_bf16 v[120:123], v[132:135], a[216:219], v[120:123]\n"
"v_mul_f32 v216, v216, v12\n"
"v_mul_f32 v217, v217, v13\n"
"v_mfma_f32_16x16x32_bf16 v[124:127], v[128:131], a[204:207], v[124:127]\n"
"v_mul_f32 v218, v218, v14\n"
"v_mul_f32 v219, v219, v15\n"
"v_mfma_f32_16x16x32_bf16 v[124:127], v[132:135], a[220:223], v[124:127]\n"
"v_exp_f32 v216, v216\n"
"s_waitcnt lgkmcnt(4)\n"
"v_mfma_f32_16x16x32_bf16 v[64:67], v[136:139], a[224:227], v[64:67]\n"
"ds_read_b64_tr_b16 v[128:129], v171 offset:32768\n"
"v_mul_f32 v28, v12, v28\n"
"v_mfma_f32_16x16x32_bf16 v[64:67], v[140:143], a[240:243], v[64:67]\n"
"ds_read_b64_tr_b16 v[130:131], v171 offset:33280\n"
"v_mul_f32 v29, v13, v29\n"
"v_mfma_f32_16x16x32_bf16 v[68:71], v[136:139], a[228:231], v[68:71]\n"
"ds_read_b64_tr_b16 v[132:133], v171 offset:34816\n"
"v_mul_f32 v30, v14, v30\n"
"v_mfma_f32_16x16x32_bf16 v[68:71], v[140:143], a[244:247], v[68:71]\n"
"ds_read_b64_tr_b16 v[134:135], v171 offset:35328\n"
"s_add_u32 m0, s92, 0xe000\n"
"v_mul_f32 v31, v15, v31\n"
"v_mfma_f32_16x16x32_bf16 v[72:75], v[136:139], a[232:235], v[72:75]\n"
"buffer_load_dwordx4 v178, s[40:43], s69 offen offset:0 lds\n"
"v_mfma_f32_16x16x32_bf16 v[72:75], v[140:143], a[248:251], v[72:75]\n"
"v_exp_f32 v217, v217\n"
"v_mfma_f32_16x16x32_bf16 v[76:79], v[136:139], a[236:239], v[76:79]\n"
"v_exp_f32 v218, v218\n"
"v_mfma_f32_16x16x32_bf16 v[76:79], v[140:143], a[252:255], v[76:79]\n"
"v_add_f32 v216, 1.0, v216\n"
"s_waitcnt lgkmcnt(4)\n"
"v_mfma_f32_16x16x32_bf16 v[80:83], v[144:147], a[224:227], v[80:83]\n"
"ds_read_b64_tr_b16 v[136:137], v171 offset:33792\n"
"v_add_f32 v217, 1.0, v217\n"
"v_mfma_f32_16x16x32_bf16 v[80:83], v[148:151], a[240:243], v[80:83]\n"
"ds_read_b64_tr_b16 v[138:139], v171 offset:34304\n"
"v_add_f32 v218, 1.0, v218\n"
"v_mfma_f32_16x16x32_bf16 v[84:87], v[144:147], a[228:231], v[84:87]\n"
"ds_read_b64_tr_b16 v[140:141], v171 offset:35840\n"
"v_mul_f32 v152, v44, v44\n"
"v_mfma_f32_16x16x32_bf16 v[84:87], v[148:151], a[244:247], v[84:87]\n"
"ds_read_b64_tr_b16 v[142:143], v171 offset:36352\n"
"v_mul_f32 v153, v45, v45\n"
"v_mfma_f32_16x16x32_bf16 v[88:91], v[144:147], a[232:235], v[88:91]\n"
"buffer_load_dwordx4 v178, s[44:47], s69 offen offset:1024 lds\n"
"v_mfma_f32_16x16x32_bf16 v[88:91], v[148:151], a[248:251], v[88:91]\n"
"ds_write_b128 v174, v[160:163] offset:34816\n"
"v_mfma_f32_16x16x32_bf16 v[92:95], v[144:147], a[236:239], v[92:95]\n"
"v_mul_f32 v154, v46, v46\n"
"v_mfma_f32_16x16x32_bf16 v[92:95], v[148:151], a[252:255], v[92:95]\n"
"v_exp_f32 v219, v219\n"
"s_waitcnt lgkmcnt(5)\n"
"v_mfma_f32_16x16x32_bf16 v[96:99], v[128:131], a[224:227], v[96:99]\n"
"v_add_f32 v219, 1.0, v219\n"
"v_mul_f32 v155, v47, v47\n"
"v_mfma_f32_16x16x32_bf16 v[96:99], v[132:135], a[240:243], v[96:99]\n"
"v_rcp_f32 v216, v216\n"
"v_mfma_f32_16x16x32_bf16 v[100:103], v[128:131], a[228:231], v[100:103]\n"
"v_mul_f32 v28, v28, v216\n"
"v_mfma_f32_16x16x32_bf16 v[100:103], v[132:135], a[244:247], v[100:103]\n"
"s_waitcnt lgkmcnt(1)\n"
"s_waitcnt vmcnt(14)\n"
"s_barrier\n"
"v_rcp_f32 v217, v217\n"
"v_mfma_f32_16x16x32_bf16 v[104:107], v[128:131], a[232:235], v[104:107]\n"
"ds_read_b64_tr_b16 v[144:145], v168 offset:0\n"
"s_add_u32 m0, s92, 0x10000\n"
"v_mul_f32 v29, v29, v217\n"
"v_mfma_f32_16x16x32_bf16 v[104:107], v[132:135], a[248:251], v[104:107]\n"
"ds_read_b64_tr_b16 v[146:147], v168 offset:512\n"
"buffer_load_dwordx4 v178, s[40:43], s70 offen offset:0 lds\n"
"v_mfma_f32_16x16x32_bf16 v[108:111], v[128:131], a[236:239], v[108:111]\n"
"ds_read_b64_tr_b16 v[148:149], v168 offset:2048\n"
"v_cvt_pk_bf16_f32 v164, v28, v29\n"
"v_mfma_f32_16x16x32_bf16 v[108:111], v[132:135], a[252:255], v[108:111]\n"
"ds_read_b64_tr_b16 v[150:151], v168 offset:2560\n"
"v_fma_f32 v152, v152, s94, v193\n"
"v_mfma_f32_16x16x32_bf16 v[112:115], v[136:139], a[224:227], v[112:115]\n"
"ds_read_b64_tr_b16 v[128:129], v168 offset:1024\n"
"v_fma_f32 v153, v153, s94, v193\n"
"v_mfma_f32_16x16x32_bf16 v[112:115], v[140:143], a[240:243], v[112:115]\n"
"ds_read_b64_tr_b16 v[130:131], v168 offset:1536\n"
"v_fma_f32 v154, v154, s94, v193\n"
"v_mfma_f32_16x16x32_bf16 v[116:119], v[136:139], a[228:231], v[116:119]\n"
"ds_read_b64_tr_b16 v[132:133], v168 offset:3072\n"
"v_fma_f32 v155, v155, s94, v193\n"
"v_mfma_f32_16x16x32_bf16 v[116:119], v[140:143], a[244:247], v[116:119]\n"
"ds_read_b64_tr_b16 v[134:135], v168 offset:3584\n"
"v_mul_f32 v152, v152, v44\n"
"v_mfma_f32_16x16x32_bf16 v[120:123], v[136:139], a[232:235], v[120:123]\n"
"buffer_load_dwordx4 v178, s[44:47], s70 offen offset:1024 lds\n"
"v_mfma_f32_16x16x32_bf16 v[120:123], v[140:143], a[248:251], v[120:123]\n"
"v_rcp_f32 v218, v218\n"
"v_mfma_f32_16x16x32_bf16 v[124:127], v[136:139], a[236:239], v[124:127]\n"
"v_rcp_f32 v219, v219\n"
"v_mfma_f32_16x16x32_bf16 v[124:127], v[140:143], a[252:255], v[124:127]\n"
"v_mul_f32 v30, v30, v218\n"
"v_mul_f32 v31, v31, v219\n"
"s_waitcnt lgkmcnt(4)\n"
"v_mfma_f32_16x16x32_bf16 v[64:67], v[144:147], v[224:227], v[64:67]\n"
"ds_read_b64_tr_b16 v[136:137], v170 offset:0\n"
"v_cvt_pk_bf16_f32 v165, v30, v31\n"
"v_mfma_f32_16x16x32_bf16 v[64:67], v[148:151], v[240:243], v[64:67]\n"
"ds_read_b64_tr_b16 v[138:139], v170 offset:512\n"
"v_mul_f32 v153, v153, v45\n"
"v_mfma_f32_16x16x32_bf16 v[68:71], v[144:147], v[228:231], v[68:71]\n"
"ds_read_b64_tr_b16 v[140:141], v170 offset:2048\n"
"v_mul_f32 v154, v154, v46\n"
"v_mfma_f32_16x16x32_bf16 v[68:71], v[148:151], v[244:247], v[68:71]\n"
"ds_read_b64_tr_b16 v[142:143], v170 offset:2560\n"
"s_add_u32 m0, s92, 0x12000\n"
"v_mul_f32 v155, v155, v47\n"
"v_mfma_f32_16x16x32_bf16 v[72:75], v[144:147], v[232:235], v[72:75]\n"
"buffer_load_dwordx4 v178, s[40:43], s71 offen offset:0 lds\n"
"v_mfma_f32_16x16x32_bf16 v[72:75], v[148:151], v[248:251], v[72:75]\n"
"v_exp_f32 v152, v152\n"
"v_mfma_f32_16x16x32_bf16 v[76:79], v[144:147], v[236:239], v[76:79]\n"
"v_exp_f32 v153, v153\n"
"v_mfma_f32_16x16x32_bf16 v[76:79], v[148:151], v[252:255], v[76:79]\n"
"v_exp_f32 v154, v154\n"
"s_waitcnt lgkmcnt(4)\n"
"v_mfma_f32_16x16x32_bf16 v[80:83], v[128:131], v[224:227], v[80:83]\n"
"ds_read_b64_tr_b16 v[144:145], v170 offset:1024\n"
"v_mul_f32 v60, v44, v60\n"
"v_mfma_f32_16x16x32_bf16 v[80:83], v[132:135], v[240:243], v[80:83]\n"
"ds_read_b64_tr_b16 v[146:147], v170 offset:1536\n"
"v_mul_f32 v61, v45, v61\n"
"v_mfma_f32_16x16x32_bf16 v[84:87], v[128:131], v[228:231], v[84:87]\n"
"ds_read_b64_tr_b16 v[148:149], v170 offset:3072\n"
"v_mul_f32 v62, v46, v62\n"
"v_mfma_f32_16x16x32_bf16 v[84:87], v[132:135], v[244:247], v[84:87]\n"
"ds_read_b64_tr_b16 v[150:151], v170 offset:3584\n"
"v_mul_f32 v63, v47, v63\n"
"v_mfma_f32_16x16x32_bf16 v[88:91], v[128:131], v[232:235], v[88:91]\n"
"buffer_load_dwordx4 v178, s[44:47], s71 offen offset:1024 lds\n"
"v_mfma_f32_16x16x32_bf16 v[88:91], v[132:135], v[248:251], v[88:91]\n"
"v_exp_f32 v155, v155\n"
"v_mfma_f32_16x16x32_bf16 v[92:95], v[128:131], v[236:239], v[92:95]\n"
"v_add_f32 v152, 1.0, v152\n"
"v_mfma_f32_16x16x32_bf16 v[92:95], v[132:135], v[252:255], v[92:95]\n"
"v_add_f32 v153, 1.0, v153\n"
"v_add_f32 v154, 1.0, v154\n"
"s_waitcnt lgkmcnt(4)\n"
"v_mfma_f32_16x16x32_bf16 v[96:99], v[136:139], v[224:227], v[96:99]\n"
"v_add_f32 v155, 1.0, v155\n"
"v_mfma_f32_16x16x32_bf16 v[96:99], v[140:143], v[240:243], v[96:99]\n"
"v_rcp_f32 v152, v152\n"
"v_mfma_f32_16x16x32_bf16 v[100:103], v[136:139], v[228:231], v[100:103]\n"
"v_rcp_f32 v153, v153\n"
"v_mfma_f32_16x16x32_bf16 v[100:103], v[140:143], v[244:247], v[100:103]\n"
"s_waitcnt lgkmcnt(0)\n"
"s_waitcnt vmcnt(10)\n"
"s_barrier\n"
"v_rcp_f32 v154, v154\n"
"v_mfma_f32_16x16x32_bf16 v[104:107], v[136:139], v[232:235], v[104:107]\n"
"ds_read_b64_tr_b16 v[128:129], v172 offset:16384\n"
"v_mul_f32 v60, v60, v152\n"
"v_mfma_f32_16x16x32_bf16 v[104:107], v[140:143], v[248:251], v[104:107]\n"
"ds_read_b64_tr_b16 v[130:131], v172 offset:16640\n"
"v_mul_f32 v61, v61, v153\n"
"v_mfma_f32_16x16x32_bf16 v[108:111], v[136:139], v[236:239], v[108:111]\n"
"ds_read_b64_tr_b16 v[132:133], v172 offset:20480\n"
"v_mul_f32 v62, v62, v154\n"
"v_mfma_f32_16x16x32_bf16 v[108:111], v[140:143], v[252:255], v[108:111]\n"
"ds_read_b64_tr_b16 v[134:135], v172 offset:20736\n"
"v_cvt_pk_bf16_f32 v166, v60, v61\n"
"v_mfma_f32_16x16x32_bf16 v[112:115], v[144:147], v[224:227], v[112:115]\n"
"ds_read_b64_tr_b16 v[136:137], v172 offset:17408\n"
"v_mfma_f32_16x16x32_bf16 v[112:115], v[148:151], v[240:243], v[112:115]\n"
"ds_read_b64_tr_b16 v[138:139], v172 offset:17664\n"
"v_mfma_f32_16x16x32_bf16 v[116:119], v[144:147], v[228:231], v[116:119]\n"
"ds_read_b64_tr_b16 v[140:141], v172 offset:21504\n"
"v_rcp_f32 v155, v155\n"
"v_mfma_f32_16x16x32_bf16 v[116:119], v[148:151], v[244:247], v[116:119]\n"
"ds_read_b64_tr_b16 v[142:143], v172 offset:21760\n"
"v_mul_f32 v63, v63, v155\n"
"v_mfma_f32_16x16x32_bf16 v[120:123], v[144:147], v[232:235], v[120:123]\n"
"v_cvt_pk_bf16_f32 v167, v62, v63\n"
"v_mfma_f32_16x16x32_bf16 v[120:123], v[148:151], v[248:251], v[120:123]\n"
"ds_write_b128 v174, v[164:167] offset:35840\n"
"v_mfma_f32_16x16x32_bf16 v[124:127], v[144:147], v[236:239], v[124:127]\n"
"v_mfma_f32_16x16x32_bf16 v[124:127], v[148:151], v[252:255], v[124:127]\n"
"s_waitcnt lgkmcnt(5)\n"
"v_mfma_f32_16x16x32_bf16 v[0:3], v[128:131], a[0:3], 0\n"
"ds_read_b64_tr_b16 v[144:145], v172 offset:24576\n"
"v_mfma_f32_16x16x32_bf16 v[0:3], v[132:135], a[16:19], v[0:3]\n"
"ds_read_b64_tr_b16 v[146:147], v172 offset:24832\n"
"v_mul_f32 v156, v64, v64\n"
"v_mul_f32 v157, v65, v65\n"
"v_mfma_f32_16x16x32_bf16 v[4:7], v[128:131], a[4:7], 0\n"
"ds_read_b64_tr_b16 v[148:149], v172 offset:28672\n"
"v_mul_f32 v158, v66, v66\n"
"v_mul_f32 v159, v67, v67\n"
"v_mfma_f32_16x16x32_bf16 v[4:7], v[132:135], a[20:23], v[4:7]\n"
"ds_read_b64_tr_b16 v[150:151], v172 offset:28928\n"
"v_fma_f32 v156, v156, s94, v193\n"
"v_fma_f32 v157, v157, s94, v193\n"
"v_mfma_f32_16x16x32_bf16 v[8:11], v[128:131], a[8:11], 0\n"
"v_fma_f32 v158, v158, s94, v193\n"
"v_fma_f32 v159, v159, s94, v193\n"
"v_mul_f32 v156, v156, v64\n"
"v_mfma_f32_16x16x32_bf16 v[8:11], v[132:135], a[24:27], v[8:11]\n"
"v_mul_f32 v157, v157, v65\n"
"v_mul_f32 v158, v158, v66\n"
"v_mul_f32 v159, v159, v67\n"
"v_mfma_f32_16x16x32_bf16 v[12:15], v[128:131], a[12:15], 0\n"
"v_exp_f32 v156, v156\n"
"v_mul_f32 v80, v64, v80\n"
"v_mfma_f32_16x16x32_bf16 v[12:15], v[132:135], a[28:31], v[12:15]\n"
"v_exp_f32 v157, v157\n"
"v_mul_f32 v81, v65, v81\n"
"s_waitcnt lgkmcnt(5)\n"
"v_mfma_f32_16x16x32_bf16 v[16:19], v[136:139], a[0:3], 0\n"
"ds_read_b64_tr_b16 v[128:129], v172 offset:25600\n"
"v_exp_f32 v158, v158\n"
"v_mfma_f32_16x16x32_bf16 v[16:19], v[140:143], a[16:19], v[16:19]\n"
"ds_read_b64_tr_b16 v[130:131], v172 offset:25856\n"
"v_exp_f32 v159, v159\n"
"v_mfma_f32_16x16x32_bf16 v[20:23], v[136:139], a[4:7], 0\n"
"ds_read_b64_tr_b16 v[132:133], v172 offset:29696\n"
"v_mul_f32 v82, v66, v82\n"
"v_mul_f32 v83, v67, v83\n"
"v_mfma_f32_16x16x32_bf16 v[20:23], v[140:143], a[20:23], v[20:23]\n"
"ds_read_b64_tr_b16 v[134:135], v172 offset:29952\n"
"v_add_f32 v156, 1.0, v156\n"
"v_add_f32 v157, 1.0, v157\n"
"v_mfma_f32_16x16x32_bf16 v[24:27], v[136:139], a[8:11], 0\n"
"v_add_f32 v158, 1.0, v158\n"
"v_add_f32 v159, 1.0, v159\n"
"v_mul_f32 v216, v96, v96\n"
"v_mfma_f32_16x16x32_bf16 v[24:27], v[140:143], a[24:27], v[24:27]\n"
"v_rcp_f32 v156, v156\n"
"v_mul_f32 v217, v97, v97\n"
"v_mfma_f32_16x16x32_bf16 v[28:31], v[136:139], a[12:15], 0\n"
"v_rcp_f32 v157, v157\n"
"v_mul_f32 v80, v80, v156\n"
"v_mfma_f32_16x16x32_bf16 v[28:31], v[140:143], a[28:31], v[28:31]\n"
"v_rcp_f32 v158, v158\n"
"v_mul_f32 v81, v81, v157\n"
"s_waitcnt lgkmcnt(4)\n"
"v_mfma_f32_16x16x32_bf16 v[0:3], v[144:147], a[32:35], v[0:3]\n"
"ds_read_b64_tr_b16 v[136:137], v172 offset:32768\n"
"v_rcp_f32 v159, v159\n"
"v_mfma_f32_16x16x32_bf16 v[0:3], v[148:151], a[48:51], v[0:3]\n"
"ds_read_b64_tr_b16 v[138:139], v172 offset:33024\n"
"v_mul_f32 v82, v82, v158\n"
"v_mul_f32 v83, v83, v159\n"
"v_mfma_f32_16x16x32_bf16 v[4:7], v[144:147], a[36:39], v[4:7]\n"
"ds_read_b64_tr_b16 v[140:141], v172 offset:36864\n"
"v_cvt_pk_bf16_f32 v160, v80, v81\n"
"v_cvt_pk_bf16_f32 v161, v82, v83\n"
"v_mfma_f32_16x16x32_bf16 v[4:7], v[148:151], a[52:55], v[4:7]\n"
"ds_read_b64_tr_b16 v[142:143], v172 offset:37120\n"
"v_mul_f32 v218, v98, v98\n"
"v_mul_f32 v219, v99, v99\n"
"v_mfma_f32_16x16x32_bf16 v[8:11], v[144:147], a[40:43], v[8:11]\n"
"ds_read_b128 a[0:3], v190 offset:0\n"
"v_fma_f32 v216, v216, s94, v193\n"
"v_fma_f32 v217, v217, s94, v193\n"
"v_mfma_f32_16x16x32_bf16 v[8:11], v[148:151], a[56:59], v[8:11]\n"
"ds_read_b128 a[4:7], v190 offset:1024\n"
"v_fma_f32 v218, v218, s94, v193\n"
"v_mfma_f32_16x16x32_bf16 v[12:15], v[144:147], a[44:47], v[12:15]\n"
"ds_read_b128 a[8:11], v190 offset:2048\n"
"v_fma_f32 v219, v219, s94, v193\n"
"v_mul_f32 v216, v216, v96\n"
"v_mfma_f32_16x16x32_bf16 v[12:15], v[148:151], a[60:63], v[12:15]\n"
"ds_read_b128 a[12:15], v190 offset:3072\n"
"v_mul_f32 v217, v217, v97\n"
"v_mul_f32 v218, v218, v98\n"
"s_waitcnt lgkmcnt(8)\n"
"v_mfma_f32_16x16x32_bf16 v[16:19], v[128:131], a[32:35], v[16:19]\n"
"ds_read_b64_tr_b16 v[144:145], v172 offset:33792\n"
"v_mul_f32 v219, v219, v99\n"
"v_mul_f32 v112, v96, v112\n"
"v_mfma_f32_16x16x32_bf16 v[16:19], v[132:135], a[48:51], v[16:19]\n"
"ds_read_b64_tr_b16 v[146:147], v172 offset:34048\n"
"v_exp_f32 v216, v216\n"
"v_mfma_f32_16x16x32_bf16 v[20:23], v[128:131], a[36:39], v[20:23]\n"
"ds_read_b64_tr_b16 v[148:149], v172 offset:37888\n"
"v_exp_f32 v217, v217\n"
"v_mfma_f32_16x16x32_bf16 v[20:23], v[132:135], a[52:55], v[20:23]\n"
"ds_read_b64_tr_b16 v[150:151], v172 offset:38144\n"
"v_exp_f32 v218, v218\n"
"v_mfma_f32_16x16x32_bf16 v[24:27], v[128:131], a[40:43], v[24:27]\n"
"ds_read_b128 a[16:19], v190 offset:8192\n"
"v_exp_f32 v219, v219\n"
"v_mfma_f32_16x16x32_bf16 v[24:27], v[132:135], a[56:59], v[24:27]\n"
"ds_read_b128 a[20:23], v190 offset:9216\n"
"v_mul_f32 v113, v97, v113\n"
"v_mfma_f32_16x16x32_bf16 v[28:31], v[128:131], a[44:47], v[28:31]\n"
"ds_read_b128 a[24:27], v190 offset:10240\n"
"v_mul_f32 v114, v98, v114\n"
"v_mul_f32 v115, v99, v115\n"
"v_mfma_f32_16x16x32_bf16 v[28:31], v[132:135], a[60:63], v[28:31]\n"
"s_waitcnt lgkmcnt(14)\n"
"ds_read_b128 a[28:31], v190 offset:11264\n"
"v_add_f32 v216, 1.0, v216\n"
"v_add_f32 v217, 1.0, v217\n"
"s_waitcnt lgkmcnt(12)\n"
"v_mfma_f32_16x16x32_bf16 v[0:3], v[136:139], a[64:67], v[0:3]\n"
"v_add_f32 v218, 1.0, v218\n"
"v_add_f32 v219, 1.0, v219\n"
"v_mul_f32 v152, v68, v68\n"
"v_mfma_f32_16x16x32_bf16 v[0:3], v[140:143], a[80:83], v[0:3]\n"
"v_rcp_f32 v216, v216\n"
"v_mul_f32 v153, v69, v69\n"
"v_mfma_f32_16x16x32_bf16 v[4:7], v[136:139], a[68:71], v[4:7]\n"
"v_rcp_f32 v217, v217\n"
"v_mul_f32 v112, v112, v216\n"
"v_mfma_f32_16x16x32_bf16 v[4:7], v[140:143], a[84:87], v[4:7]\n"
"s_waitcnt lgkmcnt(4)\n"
"s_waitcnt vmcnt(2)\n"
"s_barrier\n"
"v_rcp_f32 v218, v218\n"
"v_mul_f32 v113, v113, v217\n"
"v_mfma_f32_16x16x32_bf16 v[8:11], v[136:139], a[72:75], v[8:11]\n"
"ds_read_b64_tr_b16 v[128:129], v172 offset:40960\n"
"ds_read_b128 a[32:35], v190 offset:16384\n"
"v_mul_f32 v114, v114, v218\n"
"v_mfma_f32_16x16x32_bf16 v[8:11], v[140:143], a[88:91], v[8:11]\n"
"ds_read_b64_tr_b16 v[130:131], v172 offset:41216\n"
"ds_read_b128 a[36:39], v190 offset:17408\n"
"v_mfma_f32_16x16x32_bf16 v[12:15], v[136:139], a[76:79], v[12:15]\n"
"ds_read_b64_tr_b16 v[132:133], v172 offset:45056\n"
"ds_read_b128 a[40:43], v190 offset:18432\n"
"v_cvt_pk_bf16_f32 v162, v112, v113\n"
"v_mfma_f32_16x16x32_bf16 v[12:15], v[140:143], a[92:95], v[12:15]\n"
"ds_read_b64_tr_b16 v[134:135], v172 offset:45312\n"
"ds_read_b128 a[44:47], v190 offset:19456\n"
"s_add_u32 m0, s93, 0\n"
"v_mfma_f32_16x16x32_bf16 v[16:19], v[144:147], a[64:67], v[16:19]\n"
"ds_read_b64_tr_b16 v[136:137], v172 offset:41984\n"
"buffer_load_dwordx4 v189, s[48:51], s82 offen offset:0 lds\n"
"v_mfma_f32_16x16x32_bf16 v[16:19], v[148:151], a[80:83], v[16:19]\n"
"ds_read_b64_tr_b16 v[138:139], v172 offset:42240\n"
"v_rcp_f32 v219, v219\n"
"v_mfma_f32_16x16x32_bf16 v[20:23], v[144:147], a[68:71], v[20:23]\n"
"ds_read_b64_tr_b16 v[140:141], v172 offset:46080\n"
"buffer_load_dwordx4 v189, s[48:51], s83 offen offset:1024 lds\n"
"v_mfma_f32_16x16x32_bf16 v[20:23], v[148:151], a[84:87], v[20:23]\n"
"s_waitcnt lgkmcnt(14)\n"
"ds_read_b64_tr_b16 v[142:143], v172 offset:46336\n"
"v_mul_f32 v115, v115, v219\n"
"v_cvt_pk_bf16_f32 v163, v114, v115\n"
"v_mfma_f32_16x16x32_bf16 v[24:27], v[144:147], a[72:75], v[24:27]\n"
"s_waitcnt lgkmcnt(14)\n"
"ds_read_b128 a[48:51], v190 offset:24576\n"
"v_mul_f32 v154, v70, v70\n"
"v_mul_f32 v155, v71, v71\n"
"v_mfma_f32_16x16x32_bf16 v[24:27], v[148:151], a[88:91], v[24:27]\n"
"s_waitcnt lgkmcnt(14)\n"
"ds_read_b128 a[52:55], v190 offset:25600\n"
"v_fma_f32 v152, v152, s94, v193\n"
"v_fma_f32 v153, v153, s94, v193\n"
"v_mfma_f32_16x16x32_bf16 v[28:31], v[144:147], a[76:79], v[28:31]\n"
"s_waitcnt lgkmcnt(14)\n"
"ds_read_b128 a[56:59], v190 offset:26624\n"
"v_fma_f32 v154, v154, s94, v193\n"
"v_fma_f32 v155, v155, s94, v193\n"
"v_mfma_f32_16x16x32_bf16 v[28:31], v[148:151], a[92:95], v[28:31]\n"
"s_waitcnt lgkmcnt(14)\n"
"ds_read_b128 a[60:63], v190 offset:27648\n"
"buffer_load_dwordx4 v189, s[48:51], s84 offen offset:2048 lds\n"
"s_waitcnt lgkmcnt(9)\n"
"v_mfma_f32_16x16x32_bf16 v[0:3], v[128:131], a[96:99], v[0:3]\n"
"ds_read_b64_tr_b16 v[144:145], v173 offset:16384\n"
"v_mul_f32 v152, v152, v68\n"
"v_mul_f32 v153, v153, v69\n"
"v_mfma_f32_16x16x32_bf16 v[0:3], v[132:135], a[112:115], v[0:3]\n"
"ds_read_b64_tr_b16 v[146:147], v173 offset:16640\n"
"v_mul_f32 v154, v154, v70\n"
"v_mul_f32 v155, v155, v71\n"
"v_mfma_f32_16x16x32_bf16 v[4:7], v[128:131], a[100:103], v[4:7]\n"
"ds_read_b64_tr_b16 v[148:149], v173 offset:20480\n"
"v_exp_f32 v152, v152\n"
"v_mfma_f32_16x16x32_bf16 v[4:7], v[132:135], a[116:119], v[4:7]\n"
"ds_read_b64_tr_b16 v[150:151], v173 offset:20736\n"
"v_exp_f32 v153, v153\n"
"v_mfma_f32_16x16x32_bf16 v[8:11], v[128:131], a[104:107], v[8:11]\n"
"ds_read_b128 a[64:67], v190 offset:32768\n"
"buffer_load_dwordx4 v189, s[48:51], s85 offen offset:3072 lds\n"
"v_mfma_f32_16x16x32_bf16 v[8:11], v[132:135], a[120:123], v[8:11]\n"
"ds_read_b128 a[68:71], v190 offset:33792\n"
"v_exp_f32 v154, v154\n"
"v_mfma_f32_16x16x32_bf16 v[12:15], v[128:131], a[108:111], v[12:15]\n"
"s_waitcnt lgkmcnt(14)\n"
"ds_read_b128 a[72:75], v190 offset:34816\n"
"v_exp_f32 v155, v155\n"
"v_mfma_f32_16x16x32_bf16 v[12:15], v[132:135], a[124:127], v[12:15]\n"
"s_waitcnt lgkmcnt(14)\n"
"ds_read_b128 a[76:79], v190 offset:35840\n"
"v_mul_f32 v84, v68, v84\n"
"v_mul_f32 v85, v69, v85\n"
"s_waitcnt lgkmcnt(12)\n"
"v_mfma_f32_16x16x32_bf16 v[16:19], v[136:139], a[96:99], v[16:19]\n"
"ds_read_b64_tr_b16 v[128:129], v173 offset:17408\n"
"s_add_u32 m0, s93, 0x1000\n"
"v_mul_f32 v86, v70, v86\n"
"v_mul_f32 v87, v71, v87\n"
"v_mfma_f32_16x16x32_bf16 v[16:19], v[140:143], a[112:115], v[16:19]\n"
"ds_read_b64_tr_b16 v[130:131], v173 offset:17664\n"
"buffer_load_dwordx4 v189, s[48:51], s86 offen offset:0 lds\n"
"v_mfma_f32_16x16x32_bf16 v[20:23], v[136:139], a[100:103], v[20:23]\n"
"ds_read_b64_tr_b16 v[132:133], v173 offset:21504\n"
"v_add_f32 v152, 1.0, v152\n"
"v_add_f32 v153, 1.0, v153\n"
"v_mfma_f32_16x16x32_bf16 v[20:23], v[140:143], a[116:119], v[20:23]\n"
"s_waitcnt lgkmcnt(14)\n"
"ds_read_b64_tr_b16 v[134:135], v173 offset:21760\n"
"v_add_f32 v154, 1.0, v154\n"
"v_add_f32 v155, 1.0, v155\n"
"v_mfma_f32_16x16x32_bf16 v[24:27], v[136:139], a[104:107], v[24:27]\n"
"s_waitcnt lgkmcnt(14)\n"
"ds_read_b128 a[80:83], v190 offset:40960\n"
"v_rcp_f32 v152, v152\n"
"v_mfma_f32_16x16x32_bf16 v[24:27], v[140:143], a[120:123], v[24:27]\n"
"s_waitcnt lgkmcnt(14)\n"
"ds_read_b128 a[84:87], v190 offset:41984\n"
"v_rcp_f32 v153, v153\n"
"v_mfma_f32_16x16x32_bf16 v[28:31], v[136:139], a[108:111], v[28:31]\n"
"s_waitcnt lgkmcnt(14)\n"
"ds_read_b128 a[88:91], v190 offset:43008\n"
"buffer_load_dwordx4 v189, s[48:51], s87 offen offset:1024 lds\n"
"v_mfma_f32_16x16x32_bf16 v[28:31], v[140:143], a[124:127], v[28:31]\n"
"s_waitcnt lgkmcnt(14)\n"
"ds_read_b128 a[92:95], v190 offset:44032\n"
"v_rcp_f32 v154, v154\n"
"s_waitcnt lgkmcnt(12)\n"
"v_mfma_f32_16x16x32_bf16 v[0:3], v[144:147], a[128:131], v[0:3]\n"
"ds_read_b64_tr_b16 v[136:137], v173 offset:24576\n"
"v_rcp_f32 v155, v155\n"
"v_mfma_f32_16x16x32_bf16 v[0:3], v[148:151], a[144:147], v[0:3]\n"
"ds_read_b64_tr_b16 v[138:139], v173 offset:24832\n"
"v_mul_f32 v84, v84, v152\n"
"v_mfma_f32_16x16x32_bf16 v[4:7], v[144:147], a[132:135], v[4:7]\n"
"ds_read_b64_tr_b16 v[140:141], v173 offset:28672\n"
"v_mul_f32 v85, v85, v153\n"
"v_mul_f32 v86, v86, v154\n"
"v_mfma_f32_16x16x32_bf16 v[4:7], v[148:151], a[148:151], v[4:7]\n"
"s_waitcnt lgkmcnt(14)\n"
"ds_read_b64_tr_b16 v[142:143], v173 offset:28928\n"
"buffer_load_dwordx4 v189, s[48:51], s88 offen offset:2048 lds\n"
"v_mfma_f32_16x16x32_bf16 v[8:11], v[144:147], a[136:139], v[8:11]\n"
"s_waitcnt lgkmcnt(14)\n"
"ds_write_b128 v174, v[160:163] offset:49152\n"
"v_mfma_f32_16x16x32_bf16 v[8:11], v[148:151], a[152:155], v[8:11]\n"
"v_mul_f32 v87, v87, v155\n"
"v_cvt_pk_bf16_f32 v164, v84, v85\n"
"v_cvt_pk_bf16_f32 v165, v86, v87\n"
"v_mfma_f32_16x16x32_bf16 v[12:15], v[144:147], a[140:143], v[12:15]\n"
"v_mul_f32 v156, v100, v100\n"
"v_mul_f32 v157, v101, v101\n"
"v_mul_f32 v158, v102, v102\n"
"v_mfma_f32_16x16x32_bf16 v[12:15], v[148:151], a[156:159], v[12:15]\n"
"v_mul_f32 v159, v103, v103\n"
"v_fma_f32 v156, v156, s94, v193\n"
"v_fma_f32 v157, v157, s94, v193\n"
"s_waitcnt lgkmcnt(9)\n"
"v_mfma_f32_16x16x32_bf16 v[16:19], v[128:131], a[128:131], v[16:19]\n"
"ds_read_b64_tr_b16 v[144:145], v173 offset:25600\n"
"buffer_load_dwordx4 v189, s[48:51], s89 offen offset:3072 lds\n"
"v_mfma_f32_16x16x32_bf16 v[16:19], v[132:135], a[144:147], v[16:19]\n"
"ds_read_b64_tr_b16 v[146:147], v173 offset:25856\n"
"v_fma_f32 v158, v158, s94, v193\n"
"v_fma_f32 v159, v159, s94, v193\n"
"v_mfma_f32_16x16x32_bf16 v[20:23], v[128:131], a[132:135], v[20:23]\n"
"ds_read_b64_tr_b16 v[148:149], v173 offset:29696\n"
"v_mul_f32 v156, v156, v100\n"
"v_mul_f32 v157, v157, v101\n"
"v_mfma_f32_16x16x32_bf16 v[20:23], v[132:135], a[148:151], v[20:23]\n"
"ds_read_b64_tr_b16 v[150:151], v173 offset:29952\n"
"v_mul_f32 v158, v158, v102\n"
"v_mul_f32 v159, v159, v103\n"
"v_mfma_f32_16x16x32_bf16 v[24:27], v[128:131], a[136:139], v[24:27]\n"
"s_add_u32 m0, s93, 0x2000\n"
"v_exp_f32 v156, v156\n"
"v_mul_f32 v116, v100, v116\n"
"v_mfma_f32_16x16x32_bf16 v[24:27], v[132:135], a[152:155], v[24:27]\n"
"buffer_load_dwordx4 v189, s[48:51], s90 offen offset:0 lds\n"
"v_mul_f32 v117, v101, v117\n"
"v_mfma_f32_16x16x32_bf16 v[28:31], v[128:131], a[140:143], v[28:31]\n"
"v_exp_f32 v157, v157\n"
"v_mul_f32 v118, v102, v118\n"
"v_mfma_f32_16x16x32_bf16 v[28:31], v[132:135], a[156:159], v[28:31]\n"
"v_exp_f32 v158, v158\n"
"v_mul_f32 v119, v103, v119\n"
"s_waitcnt lgkmcnt(5)\n"
"v_mfma_f32_16x16x32_bf16 v[0:3], v[136:139], a[160:163], v[0:3]\n"
"ds_read_b64_tr_b16 v[128:129], v173 offset:32768\n"
"v_exp_f32 v159, v159\n"
"v_mfma_f32_16x16x32_bf16 v[0:3], v[140:143], a[176:179], v[0:3]\n"
"ds_read_b64_tr_b16 v[130:131], v173 offset:33024\n"
"v_add_f32 v156, 1.0, v156\n"
"v_add_f32 v157, 1.0, v157\n"
"v_mfma_f32_16x16x32_bf16 v[4:7], v[136:139], a[164:167], v[4:7]\n"
"ds_read_b64_tr_b16 v[132:133], v173 offset:36864\n"
"v_add_f32 v158, 1.0, v158\n"
"v_add_f32 v159, 1.0, v159\n"
"v_mfma_f32_16x16x32_bf16 v[4:7], v[140:143], a[180:183], v[4:7]\n"
"ds_read_b64_tr_b16 v[134:135], v173 offset:37120\n"
"v_rcp_f32 v156, v156\n"
"v_mfma_f32_16x16x32_bf16 v[8:11], v[136:139], a[168:171], v[8:11]\n"
"v_rcp_f32 v157, v157\n"
"v_mul_f32 v116, v116, v156\n"
"v_mfma_f32_16x16x32_bf16 v[8:11], v[140:143], a[184:187], v[8:11]\n"
"v_rcp_f32 v158, v158\n"
"v_mul_f32 v117, v117, v157\n"
"v_mfma_f32_16x16x32_bf16 v[12:15], v[136:139], a[172:175], v[12:15]\n"
"v_rcp_f32 v159, v159\n"
"v_mul_f32 v118, v118, v158\n"
"v_mfma_f32_16x16x32_bf16 v[12:15], v[140:143], a[188:191], v[12:15]\n"
"v_mul_f32 v119, v119, v159\n"
"v_cvt_pk_bf16_f32 v166, v116, v117\n"
"v_cvt_pk_bf16_f32 v167, v118, v119\n"
"s_waitcnt lgkmcnt(4)\n"
"v_mfma_f32_16x16x32_bf16 v[16:19], v[144:147], a[160:163], v[16:19]\n"
"ds_read_b64_tr_b16 v[136:137], v173 offset:33792\n"
"v_mul_f32 v216, v72, v72\n"
"v_mul_f32 v217, v73, v73\n"
"v_mfma_f32_16x16x32_bf16 v[16:19], v[148:151], a[176:179], v[16:19]\n"
"ds_read_b64_tr_b16 v[138:139], v173 offset:34048\n"
"v_mul_f32 v218, v74, v74\n"
"v_mul_f32 v219, v75, v75\n"
"v_mfma_f32_16x16x32_bf16 v[20:23], v[144:147], a[164:167], v[20:23]\n"
"ds_read_b64_tr_b16 v[140:141], v173 offset:37888\n"
"v_fma_f32 v216, v216, s94, v193\n"
"v_fma_f32 v217, v217, s94, v193\n"
"v_mfma_f32_16x16x32_bf16 v[20:23], v[148:151], a[180:183], v[20:23]\n"
"ds_read_b64_tr_b16 v[142:143], v173 offset:38144\n"
"v_fma_f32 v218, v218, s94, v193\n"
"v_fma_f32 v219, v219, s94, v193\n"
"v_mfma_f32_16x16x32_bf16 v[24:27], v[144:147], a[168:171], v[24:27]\n"
"ds_write_b128 v174, v[164:167] offset:50176\n"
"v_mfma_f32_16x16x32_bf16 v[24:27], v[148:151], a[184:187], v[24:27]\n"
"v_mul_f32 v216, v216, v72\n"
"v_mul_f32 v217, v217, v73\n"
"v_mul_f32 v218, v218, v74\n"
"v_mfma_f32_16x16x32_bf16 v[28:31], v[144:147], a[172:175], v[28:31]\n"
"v_mul_f32 v219, v219, v75\n"
"v_exp_f32 v216, v216\n"
"v_mfma_f32_16x16x32_bf16 v[28:31], v[148:151], a[188:191], v[28:31]\n"
"v_exp_f32 v217, v217\n"
"v_mul_f32 v88, v72, v88\n"
"s_waitcnt lgkmcnt(5)\n"
"v_mfma_f32_16x16x32_bf16 v[0:3], v[128:131], a[192:195], v[0:3]\n"
"v_exp_f32 v218, v218\n"
"v_mul_f32 v89, v73, v89\n"
"v_mfma_f32_16x16x32_bf16 v[0:3], v[132:135], a[208:211], v[0:3]\n"
"v_exp_f32 v219, v219\n"
"v_mul_f32 v90, v74, v90\n"
"v_mfma_f32_16x16x32_bf16 v[4:7], v[128:131], a[196:199], v[4:7]\n"
"v_mul_f32 v91, v75, v91\n"
"v_add_f32 v216, 1.0, v216\n"
"v_add_f32 v217, 1.0, v217\n"
"v_mfma_f32_16x16x32_bf16 v[4:7], v[132:135], a[212:215], v[4:7]\n"
"s_waitcnt lgkmcnt(1)\n"
"s_waitcnt vmcnt(0)\n"
"s_barrier\n"
"v_add_f32 v218, 1.0, v218\n"
"v_add_f32 v219, 1.0, v219\n"
"v_mul_f32 v152, v104, v104\n"
"v_mfma_f32_16x16x32_bf16 v[8:11], v[128:131], a[200:203], v[8:11]\n"
"ds_read_b64_tr_b16 v[144:145], v173 offset:40960\n"
"v_add_u32 v189, 0x80, v189\n"
"s_add_u32 m0, s93, 0x9000\n"
"v_mul_f32 v153, v105, v105\n"
"v_mfma_f32_16x16x32_bf16 v[8:11], v[132:135], a[216:219], v[8:11]\n"
"ds_read_b64_tr_b16 v[146:147], v173 offset:41216\n"
"buffer_load_dwordx4 v189, s[48:51], s82 offen offset:0 lds\n"
"v_mfma_f32_16x16x32_bf16 v[12:15], v[128:131], a[204:207], v[12:15]\n"
"ds_read_b64_tr_b16 v[148:149], v173 offset:45056\n"
"v_rcp_f32 v216, v216\n"
"v_mfma_f32_16x16x32_bf16 v[12:15], v[132:135], a[220:223], v[12:15]\n"
"ds_read_b64_tr_b16 v[150:151], v173 offset:45312\n"
"v_rcp_f32 v217, v217\n"
"v_mfma_f32_16x16x32_bf16 v[16:19], v[136:139], a[192:195], v[16:19]\n"
"ds_read_b64_tr_b16 v[128:129], v173 offset:41984\n"
"v_rcp_f32 v218, v218\n"
"v_mfma_f32_16x16x32_bf16 v[16:19], v[140:143], a[208:211], v[16:19]\n"
"ds_read_b64_tr_b16 v[130:131], v173 offset:42240\n"
"v_rcp_f32 v219, v219\n"
"v_mfma_f32_16x16x32_bf16 v[20:23], v[136:139], a[196:199], v[20:23]\n"
"ds_read_b64_tr_b16 v[132:133], v173 offset:46080\n"
"v_mul_f32 v88, v88, v216\n"
"v_mul_f32 v89, v89, v217\n"
"v_mfma_f32_16x16x32_bf16 v[20:23], v[140:143], a[212:215], v[20:23]\n"
"ds_read_b64_tr_b16 v[134:135], v173 offset:46336\n"
"v_mul_f32 v90, v90, v218\n"
"v_mul_f32 v91, v91, v219\n"
"v_mfma_f32_16x16x32_bf16 v[24:27], v[136:139], a[200:203], v[24:27]\n"
"buffer_load_dwordx4 v189, s[48:51], s83 offen offset:1024 lds\n"
"v_cvt_pk_bf16_f32 v160, v88, v89\n"
"v_mfma_f32_16x16x32_bf16 v[24:27], v[140:143], a[216:219], v[24:27]\n"
"v_cvt_pk_bf16_f32 v161, v90, v91\n"
"v_mul_f32 v154, v106, v106\n"
"v_mul_f32 v155, v107, v107\n"
"v_mfma_f32_16x16x32_bf16 v[28:31], v[136:139], a[204:207], v[28:31]\n"
"v_fma_f32 v152, v152, s94, v193\n"
"v_fma_f32 v153, v153, s94, v193\n"
"v_fma_f32 v154, v154, s94, v193\n"
"v_mfma_f32_16x16x32_bf16 v[28:31], v[140:143], a[220:223], v[28:31]\n"
"buffer_load_dwordx4 v189, s[48:51], s84 offen offset:2048 lds\n"
"v_fma_f32 v155, v155, s94, v193\n"
"s_waitcnt lgkmcnt(4)\n"
"v_mfma_f32_16x16x32_bf16 v[0:3], v[144:147], a[224:227], v[0:3]\n"
"ds_read_b64_tr_b16 v[136:137], v173 offset:49152\n"
"v_mul_f32 v152, v152, v104\n"
"v_mfma_f32_16x16x32_bf16 v[0:3], v[148:151], a[240:243], v[0:3]\n"
"ds_read_b64_tr_b16 v[138:139], v173 offset:49408\n"
"v_mul_f32 v153, v153, v105\n"
"v_mul_f32 v154, v154, v106\n"
"v_mfma_f32_16x16x32_bf16 v[4:7], v[144:147], a[228:231], v[4:7]\n"
"ds_read_b64_tr_b16 v[140:141], v173 offset:53248\n"
"v_mul_f32 v155, v155, v107\n"
"v_mul_f32 v120, v104, v120\n"
"v_mfma_f32_16x16x32_bf16 v[4:7], v[148:151], a[244:247], v[4:7]\n"
"ds_read_b64_tr_b16 v[142:143], v173 offset:53504\n"
"v_exp_f32 v152, v152\n"
"v_mfma_f32_16x16x32_bf16 v[8:11], v[144:147], a[232:235], v[8:11]\n"
"buffer_load_dwordx4 v189, s[48:51], s85 offen offset:3072 lds\n"
"v_mfma_f32_16x16x32_bf16 v[8:11], v[148:151], a[248:251], v[8:11]\n"
"v_exp_f32 v153, v153\n"
"v_mul_f32 v121, v105, v121\n"
"v_mfma_f32_16x16x32_bf16 v[12:15], v[144:147], a[236:239], v[12:15]\n"
"v_exp_f32 v154, v154\n"
"v_mul_f32 v122, v106, v122\n"
"v_mfma_f32_16x16x32_bf16 v[12:15], v[148:151], a[252:255], v[12:15]\n"
"v_exp_f32 v155, v155\n"
"v_mul_f32 v123, v107, v123\n"
"s_waitcnt lgkmcnt(4)\n"
"v_mfma_f32_16x16x32_bf16 v[16:19], v[128:131], a[224:227], v[16:19]\n"
"ds_read_b64_tr_b16 v[144:145], v173 offset:50176\n"
"v_add_f32 v152, 1.0, v152\n"
"v_add_f32 v153, 1.0, v153\n"
"v_mfma_f32_16x16x32_bf16 v[16:19], v[132:135], a[240:243], v[16:19]\n"
"ds_read_b64_tr_b16 v[146:147], v173 offset:50432\n"
"v_add_f32 v154, 1.0, v154\n"
"v_add_f32 v155, 1.0, v155\n"
"v_mfma_f32_16x16x32_bf16 v[20:23], v[128:131], a[228:231], v[20:23]\n"
"ds_read_b64_tr_b16 v[148:149], v173 offset:54272\n"
"v_rcp_f32 v152, v152\n"
"v_mfma_f32_16x16x32_bf16 v[20:23], v[132:135], a[244:247], v[20:23]\n"
"ds_read_b64_tr_b16 v[150:151], v173 offset:54528\n"
"s_add_u32 m0, s93, 0xa000\n"
"v_rcp_f32 v153, v153\n"
"v_mfma_f32_16x16x32_bf16 v[24:27], v[128:131], a[232:235], v[24:27]\n"
"buffer_load_dwordx4 v189, s[48:51], s86 offen offset:0 lds\n"
"v_mul_f32 v120, v120, v152\n"
"v_mfma_f32_16x16x32_bf16 v[24:27], v[132:135], a[248:251], v[24:27]\n"
"v_rcp_f32 v154, v154\n"
"v_mul_f32 v121, v121, v153\n"
"v_mfma_f32_16x16x32_bf16 v[28:31], v[128:131], a[236:239], v[28:31]\n"
"buffer_load_dwordx4 v189, s[48:51], s87 offen offset:1024 lds\n"
"v_mfma_f32_16x16x32_bf16 v[28:31], v[132:135], a[252:255], v[28:31]\n"
"v_rcp_f32 v155, v155\n"
"v_mul_f32 v122, v122, v154\n"
"s_waitcnt lgkmcnt(4)\n"
"v_mfma_f32_16x16x32_bf16 v[0:3], v[136:139], v[224:227], v[0:3]\n"
"ds_read_b64_tr_b16 v[128:129], v185 offset:0\n"
"v_mul_f32 v123, v123, v155\n"
"v_cvt_pk_bf16_f32 v162, v120, v121\n"
"v_mfma_f32_16x16x32_bf16 v[0:3], v[140:143], v[240:243], v[0:3]\n"
"ds_read_b64_tr_b16 v[130:131], v185 offset:18432\n"
"v_cvt_pk_bf16_f32 v163, v122, v123\n"
"v_mul_f32 v156, v76, v76\n"
"v_mfma_f32_16x16x32_bf16 v[4:7], v[136:139], v[228:231], v[4:7]\n"
"ds_read_b64_tr_b16 v[132:133], v185 offset:1024\n"
"v_mul_f32 v157, v77, v77\n"
"v_mul_f32 v158, v78, v78\n"
"v_mfma_f32_16x16x32_bf16 v[4:7], v[140:143], v[244:247], v[4:7]\n"
"ds_read_b64_tr_b16 v[134:135], v185 offset:19456\n"
"v_mul_f32 v159, v79, v79\n"
"v_fma_f32 v156, v156, s94, v193\n"
"v_mfma_f32_16x16x32_bf16 v[8:11], v[136:139], v[232:235], v[8:11]\n"
"buffer_load_dwordx4 v189, s[48:51], s88 offen offset:2048 lds\n"
"v_mfma_f32_16x16x32_bf16 v[8:11], v[140:143], v[248:251], v[8:11]\n"
"ds_write_b128 v174, v[160:163] offset:51200\n"
"v_mfma_f32_16x16x32_bf16 v[12:15], v[136:139], v[236:239], v[12:15]\n"
"v_fma_f32 v157, v157, s94, v193\n"
"v_fma_f32 v158, v158, s94, v193\n"
"v_fma_f32 v159, v159, s94, v193\n"
"v_mfma_f32_16x16x32_bf16 v[12:15], v[140:143], v[252:255], v[12:15]\n"
"v_mul_f32 v156, v156, v76\n"
"v_mul_f32 v157, v157, v77\n"
"v_mul_f32 v158, v158, v78\n"
"s_waitcnt lgkmcnt(5)\n"
"v_mfma_f32_16x16x32_bf16 v[16:19], v[144:147], v[224:227], v[16:19]\n"
"ds_read_b64_tr_b16 v[136:137], v187 offset:0\n"
"buffer_load_dwordx4 v189, s[48:51], s89 offen offset:3072 lds\n"
"v_mfma_f32_16x16x32_bf16 v[16:19], v[148:151], v[240:243], v[16:19]\n"
"ds_read_b64_tr_b16 v[138:139], v187 offset:18432\n"
"v_mul_f32 v159, v159, v79\n"
"v_mul_f32 v92, v76, v92\n"
"v_mfma_f32_16x16x32_bf16 v[20:23], v[144:147], v[228:231], v[20:23]\n"
"ds_read_b64_tr_b16 v[140:141], v187 offset:1024\n"
"v_exp_f32 v156, v156\n"
"v_mfma_f32_16x16x32_bf16 v[20:23], v[148:151], v[244:247], v[20:23]\n"
"ds_read_b64_tr_b16 v[142:143], v187 offset:19456\n"
"v_exp_f32 v157, v157\n"
"v_mfma_f32_16x16x32_bf16 v[24:27], v[144:147], v[232:235], v[24:27]\n"
"s_add_u32 m0, s93, 0xb000\n"
"v_exp_f32 v158, v158\n"
"v_mul_f32 v93, v77, v93\n"
"v_mfma_f32_16x16x32_bf16 v[24:27], v[148:151], v[248:251], v[24:27]\n"
"buffer_load_dwordx4 v189, s[48:51], s90 offen offset:0 lds\n"
"v_mul_f32 v94, v78, v94\n"
"v_mfma_f32_16x16x32_bf16 v[28:31], v[144:147], v[236:239], v[28:31]\n"
"v_exp_f32 v159, v159\n"
"v_mul_f32 v95, v79, v95\n"
"v_mfma_f32_16x16x32_bf16 v[28:31], v[148:151], v[252:255], v[28:31]\n"
"v_add_f32 v156, 1.0, v156\n"
"v_add_f32 v157, 1.0, v157\n"
"v_add_f32 v158, 1.0, v158\n"
"s_waitcnt lgkmcnt(5)\n"
"v_mfma_f32_16x16x32_bf16 v[32:35], v[128:131], a[0:3], 0\n"
"ds_read_b64_tr_b16 v[144:145], v185 offset:2048\n"
"v_add_f32 v159, 1.0, v159\n"
"v_mul_f32 v216, v108, v108\n"
"v_mfma_f32_16x16x32_bf16 v[32:35], v[132:135], a[16:19], v[32:35]\n"
"ds_read_b64_tr_b16 v[146:147], v185 offset:20480\n"
"v_rcp_f32 v156, v156\n"
"v_mfma_f32_16x16x32_bf16 v[40:43], v[128:131], a[4:7], 0\n"
"ds_read_b64_tr_b16 v[148:149], v185 offset:3072\n"
"v_rcp_f32 v157, v157\n"
"v_mfma_f32_16x16x32_bf16 v[40:43], v[132:135], a[20:23], v[40:43]\n"
"ds_read_b64_tr_b16 v[150:151], v185 offset:21504\n"
"v_rcp_f32 v158, v158\n"
"v_mfma_f32_16x16x32_bf16 v[48:51], v[128:131], a[8:11], 0\n"
"v_rcp_f32 v159, v159\n"
"v_mul_f32 v92, v92, v156\n"
"v_mfma_f32_16x16x32_bf16 v[48:51], v[132:135], a[24:27], v[48:51]\n"
"v_mul_f32 v93, v93, v157\n"
"v_mul_f32 v94, v94, v158\n"
"v_mul_f32 v95, v95, v159\n"
"v_mfma_f32_16x16x32_bf16 v[56:59], v[128:131], a[12:15], 0\n"
"v_cvt_pk_bf16_f32 v164, v92, v93\n"
"v_cvt_pk_bf16_f32 v165, v94, v95\n"
"v_mul_f32 v217, v109, v109\n"
"v_mfma_f32_16x16x32_bf16 v[56:59], v[132:135], a[28:31], v[56:59]\n"
"v_mul_f32 v218, v110, v110\n"
"v_mul_f32 v219, v111, v111\n"
"v_fma_f32 v216, v216, s94, v193\n"
"s_waitcnt lgkmcnt(4)\n"
"v_mfma_f32_16x16x32_bf16 v[36:39], v[136:139], a[0:3], 0\n"
"ds_read_b64_tr_b16 v[128:129], v187 offset:2048\n"
"v_fma_f32 v217, v217, s94, v193\n"
"v_fma_f32 v218, v218, s94, v193\n"
"v_mfma_f32_16x16x32_bf16 v[36:39], v[140:143], a[16:19], v[36:39]\n"
"ds_read_b64_tr_b16 v[130:131], v187 offset:20480\n"
"v_fma_f32 v219, v219, s94, v193\n"
"v_mul_f32 v216, v216, v108\n"
"v_mfma_f32_16x16x32_bf16 v[44:47], v[136:139], a[4:7], 0\n"
"ds_read_b64_tr_b16 v[132:133], v187 offset:3072\n"
"v_mul_f32 v217, v217, v109\n"
"v_mul_f32 v218, v218, v110\n"
"v_mfma_f32_16x16x32_bf16 v[44:47], v[140:143], a[20:23], v[44:47]\n"
"ds_read_b64_tr_b16 v[134:135], v187 offset:21504\n"
"v_mul_f32 v219, v219, v111\n"
"v_mul_f32 v124, v108, v124\n"
"v_mfma_f32_16x16x32_bf16 v[52:55], v[136:139], a[8:11], 0\n"
"v_exp_f32 v216, v216\n"
"v_mul_f32 v125, v109, v125\n"
"v_mfma_f32_16x16x32_bf16 v[52:55], v[140:143], a[24:27], v[52:55]\n"
"v_exp_f32 v217, v217\n"
"v_mul_f32 v126, v110, v126\n"
"v_mfma_f32_16x16x32_bf16 v[60:63], v[136:139], a[12:15], 0\n"
"v_exp_f32 v218, v218\n"
"v_mul_f32 v127, v111, v127\n"
"v_mfma_f32_16x16x32_bf16 v[60:63], v[140:143], a[28:31], v[60:63]\n"
"v_exp_f32 v219, v219\n"
"v_add_f32 v216, 1.0, v216\n"
"s_waitcnt lgkmcnt(4)\n"
"v_mfma_f32_16x16x32_bf16 v[32:35], v[144:147], a[32:35], v[32:35]\n"
"ds_read_b64_tr_b16 v[136:137], v185 offset:4096\n"
"v_add_f32 v217, 1.0, v217\n"
"v_add_f32 v218, 1.0, v218\n"
"v_mfma_f32_16x16x32_bf16 v[32:35], v[148:151], a[48:51], v[32:35]\n"
"ds_read_b64_tr_b16 v[138:139], v185 offset:22528\n"
"v_add_f32 v219, 1.0, v219\n"
"v_mul_f32 v152, v0, v0\n"
"v_mfma_f32_16x16x32_bf16 v[40:43], v[144:147], a[36:39], v[40:43]\n"
"ds_read_b64_tr_b16 v[140:141], v185 offset:5120\n"
"v_rcp_f32 v216, v216\n"
"v_mfma_f32_16x16x32_bf16 v[40:43], v[148:151], a[52:55], v[40:43]\n"
"ds_read_b64_tr_b16 v[142:143], v185 offset:23552\n"
"v_rcp_f32 v217, v217\n"
"v_mfma_f32_16x16x32_bf16 v[48:51], v[144:147], a[40:43], v[48:51]\n"
"v_rcp_f32 v218, v218\n"
"v_mul_f32 v124, v124, v216\n"
"v_mfma_f32_16x16x32_bf16 v[48:51], v[148:151], a[56:59], v[48:51]\n"
"v_rcp_f32 v219, v219\n"
"v_mul_f32 v125, v125, v217\n"
"v_mfma_f32_16x16x32_bf16 v[56:59], v[144:147], a[44:47], v[56:59]\n"
"v_mul_f32 v126, v126, v218\n"
"v_mul_f32 v127, v127, v219\n"
"v_cvt_pk_bf16_f32 v166, v124, v125\n"
"v_mfma_f32_16x16x32_bf16 v[56:59], v[148:151], a[60:63], v[56:59]\n"
"v_cvt_pk_bf16_f32 v167, v126, v127\n"
"v_mul_f32 v153, v1, v1\n"
"v_mul_f32 v154, v2, v2\n"
"s_waitcnt lgkmcnt(4)\n"
"v_mfma_f32_16x16x32_bf16 v[36:39], v[128:131], a[32:35], v[36:39]\n"
"ds_read_b64_tr_b16 v[144:145], v187 offset:4096\n"
"v_mul_f32 v155, v3, v3\n"
"v_fma_f32 v152, v152, s94, v193\n"
"v_mfma_f32_16x16x32_bf16 v[36:39], v[132:135], a[48:51], v[36:39]\n"
"ds_read_b64_tr_b16 v[146:147], v187 offset:22528\n"
"v_fma_f32 v153, v153, s94, v193\n"
"v_fma_f32 v154, v154, s94, v193\n"
"v_mfma_f32_16x16x32_bf16 v[44:47], v[128:131], a[36:39], v[44:47]\n"
"ds_read_b64_tr_b16 v[148:149], v187 offset:5120\n"
"v_fma_f32 v155, v155, s94, v193\n"
"v_mul_f32 v152, v152, v0\n"
"v_mfma_f32_16x16x32_bf16 v[44:47], v[132:135], a[52:55], v[44:47]\n"
"ds_read_b64_tr_b16 v[150:151], v187 offset:23552\n"
"v_mul_f32 v153, v153, v1\n"
"v_mfma_f32_16x16x32_bf16 v[52:55], v[128:131], a[40:43], v[52:55]\n"
"ds_write_b128 v174, v[164:167] offset:52224\n"
"v_mfma_f32_16x16x32_bf16 v[52:55], v[132:135], a[56:59], v[52:55]\n"
"v_mul_f32 v154, v154, v2\n"
"v_mul_f32 v155, v155, v3\n"
"v_mul_f32 v16, v0, v16\n"
"v_mfma_f32_16x16x32_bf16 v[60:63], v[128:131], a[44:47], v[60:63]\n"
"v_exp_f32 v152, v152\n"
"v_mul_f32 v17, v1, v17\n"
"v_mfma_f32_16x16x32_bf16 v[60:63], v[132:135], a[60:63], v[60:63]\n"
"v_exp_f32 v153, v153\n"
"v_mul_f32 v18, v2, v18\n"
"s_waitcnt lgkmcnt(5)\n"
"v_mfma_f32_16x16x32_bf16 v[32:35], v[136:139], a[64:67], v[32:35]\n"
"v_exp_f32 v154, v154\n"
"v_mul_f32 v19, v3, v19\n"
"v_mfma_f32_16x16x32_bf16 v[32:35], v[140:143], a[80:83], v[32:35]\n"
"v_exp_f32 v155, v155\n"
"v_add_f32 v152, 1.0, v152\n"
"v_mfma_f32_16x16x32_bf16 v[40:43], v[136:139], a[68:71], v[40:43]\n"
"v_add_f32 v153, 1.0, v153\n"
"v_add_f32 v154, 1.0, v154\n"
"v_add_f32 v155, 1.0, v155\n"
"v_mfma_f32_16x16x32_bf16 v[40:43], v[140:143], a[84:87], v[40:43]\n"
"s_waitcnt lgkmcnt(0)\n"
"s_waitcnt vmcnt(0)\n"
"s_barrier\n"
"v_rcp_f32 v152, v152\n"
"v_mul_f32 v156, v4, v4\n"
"v_mfma_f32_16x16x32_bf16 v[48:51], v[136:139], a[72:75], v[48:51]\n"
"ds_read_b64_tr_b16 v[128:129], v185 offset:36864\n"
"v_rcp_f32 v153, v153\n"
"v_mfma_f32_16x16x32_bf16 v[48:51], v[140:143], a[88:91], v[48:51]\n"
"ds_read_b64_tr_b16 v[130:131], v185 offset:55296\n"
"v_rcp_f32 v154, v154\n"
"v_mfma_f32_16x16x32_bf16 v[56:59], v[136:139], a[76:79], v[56:59]\n"
"ds_read_b64_tr_b16 v[132:133], v185 offset:37888\n"
"v_rcp_f32 v155, v155\n"
"v_mfma_f32_16x16x32_bf16 v[56:59], v[140:143], a[92:95], v[56:59]\n"
"ds_read_b64_tr_b16 v[134:135], v185 offset:56320\n"
"v_mul_f32 v16, v16, v152\n"
"v_mul_f32 v17, v17, v153\n"
"v_mfma_f32_16x16x32_bf16 v[36:39], v[144:147], a[64:67], v[36:39]\n"
"ds_read_b64_tr_b16 v[136:137], v187 offset:36864\n"
"ds_read_b128 a[96:99], v190 offset:49152\n"
"v_mul_f32 v18, v18, v154\n"
"v_mfma_f32_16x16x32_bf16 v[36:39], v[148:151], a[80:83], v[36:39]\n"
"ds_read_b64_tr_b16 v[138:139], v187 offset:55296\n"
"v_mul_f32 v19, v19, v155\n"
"v_cvt_pk_bf16_f32 v160, v16, v17\n"
"v_mfma_f32_16x16x32_bf16 v[44:47], v[144:147], a[68:71], v[44:47]\n"
"ds_read_b64_tr_b16 v[140:141], v187 offset:37888\n"
"ds_read_b128 a[100:103], v190 offset:50176\n"
"v_cvt_pk_bf16_f32 v161, v18, v19\n"
"v_mfma_f32_16x16x32_bf16 v[44:47], v[148:151], a[84:87], v[44:47]\n"
"ds_read_b64_tr_b16 v[142:143], v187 offset:56320\n"
"ds_write_b64 v175, v[160:161] offset:0\n"
"v_mfma_f32_16x16x32_bf16 v[52:55], v[144:147], a[72:75], v[52:55]\n"
"ds_read_b128 a[104:107], v190 offset:51200\n"
"v_add_u32 v189, 0x80, v189\n"
"s_add_u32 m0, s93, 0x16000\n"
"v_mfma_f32_16x16x32_bf16 v[52:55], v[148:151], a[88:91], v[52:55]\n"
"buffer_load_dwordx4 v189, s[48:51], s82 offen offset:0 lds\n"
"v_mfma_f32_16x16x32_bf16 v[60:63], v[144:147], a[76:79], v[60:63]\n"
"ds_read_b128 a[108:111], v190 offset:52224\n"
"v_mul_f32 v157, v5, v5\n"
"v_mul_f32 v158, v6, v6\n"
"v_mfma_f32_16x16x32_bf16 v[60:63], v[148:151], a[92:95], v[60:63]\n"
"buffer_load_dwordx4 v189, s[48:51], s83 offen offset:1024 lds\n"
"v_mul_f32 v159, v7, v7\n"
"s_waitcnt lgkmcnt(9)\n"
"v_mfma_f32_16x16x32_bf16 v[64:67], v[128:131], a[0:3], 0\n"
"ds_read_b64_tr_b16 v[144:145], v185 offset:38912\n"
"ds_read_b128 a[112:115], v190 offset:57344\n"
"v_mfma_f32_16x16x32_bf16 v[64:67], v[132:135], a[16:19], v[64:67]\n"
"ds_read_b64_tr_b16 v[146:147], v185 offset:57344\n"
"v_fma_f32 v156, v156, s94, v193\n"
"v_fma_f32 v157, v157, s94, v193\n"
"v_mfma_f32_16x16x32_bf16 v[72:75], v[128:131], a[4:7], 0\n"
"ds_read_b64_tr_b16 v[148:149], v185 offset:39936\n"
"ds_read_b128 a[116:119], v190 offset:58368\n"
"v_fma_f32 v158, v158, s94, v193\n"
"v_mfma_f32_16x16x32_bf16 v[72:75], v[132:135], a[20:23], v[72:75]\n"
"ds_read_b64_tr_b16 v[150:151], v185 offset:58368\n"
"v_fma_f32 v159, v159, s94, v193\n"
"v_mfma_f32_16x16x32_bf16 v[80:83], v[128:131], a[8:11], 0\n"
"s_waitcnt lgkmcnt(14)\n"
"ds_read_b128 a[120:123], v190 offset:59392\n"
"v_mul_f32 v156, v156, v4\n"
"v_mul_f32 v157, v157, v5\n"
"v_mfma_f32_16x16x32_bf16 v[80:83], v[132:135], a[24:27], v[80:83]\n"
"buffer_load_dwordx4 v189, s[48:51], s84 offen offset:2048 lds\n"
"v_mul_f32 v158, v158, v6\n"
"v_mfma_f32_16x16x32_bf16 v[88:91], v[128:131], a[12:15], 0\n"
"s_waitcnt lgkmcnt(14)\n"
"ds_read_b128 a[124:127], v190 offset:60416\n"
"v_mul_f32 v159, v159, v7\n"
"v_mfma_f32_16x16x32_bf16 v[88:91], v[132:135], a[28:31], v[88:91]\n"
"v_exp_f32 v156, v156\n"
"v_mul_f32 v20, v4, v20\n"
"s_waitcnt lgkmcnt(11)\n"
"v_mfma_f32_16x16x32_bf16 v[68:71], v[136:139], a[0:3], 0\n"
"ds_read_b64_tr_b16 v[128:129], v187 offset:38912\n"
"v_exp_f32 v157, v157\n"
"v_mfma_f32_16x16x32_bf16 v[68:71], v[140:143], a[16:19], v[68:71]\n"
"ds_read_b64_tr_b16 v[130:131], v187 offset:57344\n"
"v_exp_f32 v158, v158\n"
"v_mfma_f32_16x16x32_bf16 v[76:79], v[136:139], a[4:7], 0\n"
"ds_read_b64_tr_b16 v[132:133], v187 offset:39936\n"
"v_exp_f32 v159, v159\n"
"v_mfma_f32_16x16x32_bf16 v[76:79], v[140:143], a[20:23], v[76:79]\n"
"ds_read_b64_tr_b16 v[134:135], v187 offset:58368\n"
"v_mul_f32 v21, v5, v21\n"
"v_mul_f32 v22, v6, v22\n"
"v_mfma_f32_16x16x32_bf16 v[84:87], v[136:139], a[8:11], 0\n"
"buffer_load_dwordx4 v189, s[48:51], s85 offen offset:3072 lds\n"
"v_mfma_f32_16x16x32_bf16 v[84:87], v[140:143], a[24:27], v[84:87]\n"
"v_mul_f32 v23, v7, v23\n"
"v_add_f32 v156, 1.0, v156\n"
"v_add_f32 v157, 1.0, v157\n"
"v_mfma_f32_16x16x32_bf16 v[92:95], v[136:139], a[12:15], 0\n"
"v_add_f32 v158, 1.0, v158\n"
"v_add_f32 v159, 1.0, v159\n"
"v_mul_f32 v216, v8, v8\n"
"v_mfma_f32_16x16x32_bf16 v[92:95], v[140:143], a[28:31], v[92:95]\n"
"v_rcp_f32 v156, v156\n"
"v_mul_f32 v217, v9, v9\n"
"s_waitcnt lgkmcnt(6)\n"
"v_mfma_f32_16x16x32_bf16 v[64:67], v[144:147], a[32:35], v[64:67]\n"
"ds_read_b64_tr_b16 v[136:137], v185 offset:40960\n"
"v_rcp_f32 v157, v157\n"
"v_mfma_f32_16x16x32_bf16 v[64:67], v[148:151], a[48:51], v[64:67]\n"
"ds_read_b64_tr_b16 v[138:139], v185 offset:59392\n"
"v_rcp_f32 v158, v158\n"
"v_mfma_f32_16x16x32_bf16 v[72:75], v[144:147], a[36:39], v[72:75]\n"
"ds_read_b64_tr_b16 v[140:141], v185 offset:41984\n"
"v_rcp_f32 v159, v159\n"
"v_mfma_f32_16x16x32_bf16 v[72:75], v[148:151], a[52:55], v[72:75]\n"
"ds_read_b64_tr_b16 v[142:143], v185 offset:60416\n"
"s_add_u32 m0, s93, 0x17000\n"
"v_mul_f32 v20, v20, v156\n"
"v_mul_f32 v21, v21, v157\n"
"v_mfma_f32_16x16x32_bf16 v[80:83], v[144:147], a[40:43], v[80:83]\n"
"buffer_load_dwordx4 v189, s[48:51], s86 offen offset:0 lds\n"
"v_mul_f32 v22, v22, v158\n"
"v_mfma_f32_16x16x32_bf16 v[80:83], v[148:151], a[56:59], v[80:83]\n"
"v_mul_f32 v23, v23, v159\n"
"v_cvt_pk_bf16_f32 v164, v20, v21\n"
"v_cvt_pk_bf16_f32 v165, v22, v23\n"
"v_mfma_f32_16x16x32_bf16 v[88:91], v[144:147], a[44:47], v[88:91]\n"
"ds_write_b64 v175, v[164:165] offset:1024\n"
"v_mul_f32 v218, v10, v10\n"
"v_mfma_f32_16x16x32_bf16 v[88:91], v[148:151], a[60:63], v[88:91]\n"
"buffer_load_dwordx4 v189, s[48:51], s87 offen offset:1024 lds\n"
"s_waitcnt lgkmcnt(5)\n"
"v_mfma_f32_16x16x32_bf16 v[68:71], v[128:131], a[32:35], v[68:71]\n"
"ds_read_b64_tr_b16 v[144:145], v187 offset:40960\n"
"v_mul_f32 v219, v11, v11\n"
"v_fma_f32 v216, v216, s94, v193\n"
"v_mfma_f32_16x16x32_bf16 v[68:71], v[132:135], a[48:51], v[68:71]\n"
"ds_read_b64_tr_b16 v[146:147], v187 offset:59392\n"
"v_fma_f32 v217, v217, s94, v193\n"
"v_fma_f32 v218, v218, s94, v193\n"
"v_mfma_f32_16x16x32_bf16 v[76:79], v[128:131], a[36:39], v[76:79]\n"
"ds_read_b64_tr_b16 v[148:149], v187 offset:41984\n"
"v_fma_f32 v219, v219, s94, v193\n"
"v_mul_f32 v216, v216, v8\n"
"v_mfma_f32_16x16x32_bf16 v[76:79], v[132:135], a[52:55], v[76:79]\n"
"ds_read_b64_tr_b16 v[150:151], v187 offset:60416\n"
"v_mul_f32 v217, v217, v9\n"
"v_mul_f32 v218, v218, v10\n"
"v_mfma_f32_16x16x32_bf16 v[84:87], v[128:131], a[40:43], v[84:87]\n"
"v_mul_f32 v219, v219, v11\n"
"v_exp_f32 v216, v216\n"
"v_mfma_f32_16x16x32_bf16 v[84:87], v[132:135], a[56:59], v[84:87]\n"
"buffer_load_dwordx4 v189, s[48:51], s88 offen offset:2048 lds\n"
"v_mfma_f32_16x16x32_bf16 v[92:95], v[128:131], a[44:47], v[92:95]\n"
"v_exp_f32 v217, v217\n"
"v_mul_f32 v24, v8, v24\n"
"v_mfma_f32_16x16x32_bf16 v[92:95], v[132:135], a[60:63], v[92:95]\n"
"v_exp_f32 v218, v218\n"
"v_mul_f32 v25, v9, v25\n"
"s_waitcnt lgkmcnt(5)\n"
"v_mfma_f32_16x16x32_bf16 v[64:67], v[136:139], a[64:67], v[64:67]\n"
"ds_read_b64_tr_b16 v[128:129], v185 offset:6144\n"
"v_exp_f32 v219, v219\n"
"v_mfma_f32_16x16x32_bf16 v[64:67], v[140:143], a[80:83], v[64:67]\n"
"ds_read_b64_tr_b16 v[130:131], v185 offset:24576\n"
"v_mul_f32 v26, v10, v26\n"
"v_mul_f32 v27, v11, v27\n"
"v_mfma_f32_16x16x32_bf16 v[72:75], v[136:139], a[68:71], v[72:75]\n"
"ds_read_b64_tr_b16 v[132:133], v185 offset:7168\n"
"v_add_f32 v216, 1.0, v216\n"
"v_add_f32 v217, 1.0, v217\n"
"v_mfma_f32_16x16x32_bf16 v[72:75], v[140:143], a[84:87], v[72:75]\n"
"ds_read_b64_tr_b16 v[134:135], v185 offset:25600\n"
"v_add_f32 v218, 1.0, v218\n"
"v_add_f32 v219, 1.0, v219\n"
"v_mfma_f32_16x16x32_bf16 v[80:83], v[136:139], a[72:75], v[80:83]\n"
"buffer_load_dwordx4 v189, s[48:51], s89 offen offset:3072 lds\n"
"v_mfma_f32_16x16x32_bf16 v[80:83], v[140:143], a[88:91], v[80:83]\n"
"v_rcp_f32 v216, v216\n"
"v_mul_f32 v152, v12, v12\n"
"v_mfma_f32_16x16x32_bf16 v[88:91], v[136:139], a[76:79], v[88:91]\n"
"v_rcp_f32 v217, v217\n"
"v_mul_f32 v24, v24, v216\n"
"v_mfma_f32_16x16x32_bf16 v[88:91], v[140:143], a[92:95], v[88:91]\n"
"v_rcp_f32 v218, v218\n"
"v_mul_f32 v25, v25, v217\n"
"s_waitcnt lgkmcnt(4)\n"
"v_mfma_f32_16x16x32_bf16 v[68:71], v[144:147], a[64:67], v[68:71]\n"
"ds_read_b64_tr_b16 v[136:137], v187 offset:6144\n"
"v_rcp_f32 v219, v219\n"
"v_mfma_f32_16x16x32_bf16 v[68:71], v[148:151], a[80:83], v[68:71]\n"
"ds_read_b64_tr_b16 v[138:139], v187 offset:24576\n"
"v_mul_f32 v26, v26, v218\n"
"v_mul_f32 v27, v27, v219\n"
"v_mfma_f32_16x16x32_bf16 v[76:79], v[144:147], a[68:71], v[76:79]\n"
"ds_read_b64_tr_b16 v[140:141], v187 offset:7168\n"
"v_cvt_pk_bf16_f32 v160, v24, v25\n"
"v_cvt_pk_bf16_f32 v161, v26, v27\n"
"v_mfma_f32_16x16x32_bf16 v[76:79], v[148:151], a[84:87], v[76:79]\n"
"ds_read_b64_tr_b16 v[142:143], v187 offset:25600\n"
"s_add_u32 m0, s93, 0x18000\n"
"ds_write_b64 v175, v[160:161] offset:2048\n"
"v_mfma_f32_16x16x32_bf16 v[84:87], v[144:147], a[72:75], v[84:87]\n"
"buffer_load_dwordx4 v189, s[48:51], s90 offen offset:0 lds\n"
"v_mul_f32 v153, v13, v13\n"
"v_mfma_f32_16x16x32_bf16 v[84:87], v[148:151], a[88:91], v[84:87]\n"
"v_mul_f32 v154, v14, v14\n"
"v_mul_f32 v155, v15, v15\n"
"v_fma_f32 v152, v152, s94, v193\n"
"v_mfma_f32_16x16x32_bf16 v[92:95], v[144:147], a[76:79], v[92:95]\n"
"v_fma_f32 v153, v153, s94, v193\n"
"v_fma_f32 v154, v154, s94, v193\n"
"v_fma_f32 v155, v155, s94, v193\n"
"v_mfma_f32_16x16x32_bf16 v[92:95], v[148:151], a[92:95], v[92:95]\n"
"v_mul_f32 v152, v152, v12\n"
"v_mul_f32 v153, v153, v13\n"
"v_mul_f32 v154, v154, v14\n"
"s_waitcnt lgkmcnt(5)\n"
"v_mfma_f32_16x16x32_bf16 v[32:35], v[128:131], a[96:99], v[32:35]\n"
"ds_read_b64_tr_b16 v[144:145], v185 offset:43008\n"
"v_mul_f32 v155, v155, v15\n"
"v_mul_f32 v28, v12, v28\n"
"v_mfma_f32_16x16x32_bf16 v[32:35], v[132:135], a[112:115], v[32:35]\n"
"ds_read_b64_tr_b16 v[146:147], v185 offset:61440\n"
"v_exp_f32 v152, v152\n"
"v_mfma_f32_16x16x32_bf16 v[40:43], v[128:131], a[100:103], v[40:43]\n"
"ds_read_b64_tr_b16 v[148:149], v185 offset:44032\n"
"v_exp_f32 v153, v153\n"
"v_mfma_f32_16x16x32_bf16 v[40:43], v[132:135], a[116:119], v[40:43]\n"
"ds_read_b64_tr_b16 v[150:151], v185 offset:62464\n"
"v_exp_f32 v154, v154\n"
"v_mfma_f32_16x16x32_bf16 v[48:51], v[128:131], a[104:107], v[48:51]\n"
"v_exp_f32 v155, v155\n"
"v_mfma_f32_16x16x32_bf16 v[48:51], v[132:135], a[120:123], v[48:51]\n"
"v_mul_f32 v29, v13, v29\n"
"v_mul_f32 v30, v14, v30\n"
"v_mul_f32 v31, v15, v31\n"
"v_mfma_f32_16x16x32_bf16 v[56:59], v[128:131], a[108:111], v[56:59]\n"
"v_add_f32 v152, 1.0, v152\n"
"v_add_f32 v153, 1.0, v153\n"
"v_add_f32 v154, 1.0, v154\n"
"v_mfma_f32_16x16x32_bf16 v[56:59], v[132:135], a[124:127], v[56:59]\n"
"v_add_f32 v155, 1.0, v155\n"
"v_rcp_f32 v152, v152\n"
"s_waitcnt lgkmcnt(5)\n"
"v_mfma_f32_16x16x32_bf16 v[36:39], v[136:139], a[96:99], v[36:39]\n"
"ds_read_b64_tr_b16 v[128:129], v187 offset:43008\n"
"v_rcp_f32 v153, v153\n"
"v_mfma_f32_16x16x32_bf16 v[36:39], v[140:143], a[112:115], v[36:39]\n"
"ds_read_b64_tr_b16 v[130:131], v187 offset:61440\n"
"v_rcp_f32 v154, v154\n"
"v_mfma_f32_16x16x32_bf16 v[44:47], v[136:139], a[100:103], v[44:47]\n"
"ds_read_b64_tr_b16 v[132:133], v187 offset:44032\n"
"v_rcp_f32 v155, v155\n"
"v_mfma_f32_16x16x32_bf16 v[44:47], v[140:143], a[116:119], v[44:47]\n"
"ds_read_b64_tr_b16 v[134:135], v187 offset:62464\n"
"v_mul_f32 v28, v28, v152\n"
"v_mul_f32 v29, v29, v153\n"
"v_mfma_f32_16x16x32_bf16 v[52:55], v[136:139], a[104:107], v[52:55]\n"
"v_mul_f32 v30, v30, v154\n"
"v_mul_f32 v31, v31, v155\n"
"v_cvt_pk_bf16_f32 v164, v28, v29\n"
"v_mfma_f32_16x16x32_bf16 v[52:55], v[140:143], a[120:123], v[52:55]\n"
"v_cvt_pk_bf16_f32 v165, v30, v31\n"
"ds_write_b64 v175, v[164:165] offset:3072\n"
"v_mfma_f32_16x16x32_bf16 v[60:63], v[136:139], a[108:111], v[60:63]\n"
"v_mfma_f32_16x16x32_bf16 v[60:63], v[140:143], a[124:127], v[60:63]\n"
"s_waitcnt lgkmcnt(5)\n"
"v_mfma_f32_16x16x32_bf16 v[64:67], v[144:147], a[96:99], v[64:67]\n"
"s_waitcnt lgkmcnt(0)\n"
"s_barrier\n"
"v_mfma_f32_16x16x32_bf16 v[64:67], v[148:151], a[112:115], v[64:67]\n"
"ds_read_b128 a[128:131], v191 offset:0\n"
"v_mfma_f32_16x16x32_bf16 v[72:75], v[144:147], a[100:103], v[72:75]\n"
"ds_read_b128 a[132:135], v191 offset:1024\n"
"v_mfma_f32_16x16x32_bf16 v[72:75], v[148:151], a[116:119], v[72:75]\n"
"ds_read_b128 a[136:139], v191 offset:2048\n"
"v_mfma_f32_16x16x32_bf16 v[80:83], v[144:147], a[104:107], v[80:83]\n"
"ds_read_b64_tr_b16 v[136:137], v185 offset:8192\n"
"ds_read_b128 a[140:143], v191 offset:3072\n"
"v_mfma_f32_16x16x32_bf16 v[80:83], v[148:151], a[120:123], v[80:83]\n"
"ds_read_b64_tr_b16 v[138:139], v185 offset:26624\n"
"v_mfma_f32_16x16x32_bf16 v[88:91], v[144:147], a[108:111], v[88:91]\n"
"v_mfma_f32_16x16x32_bf16 v[88:91], v[148:151], a[124:127], v[88:91]\n"
"v_mfma_f32_16x16x32_bf16 v[68:71], v[128:131], a[96:99], v[68:71]\n"
"ds_read_b64_tr_b16 v[144:145], v187 offset:8192\n"
"v_mfma_f32_16x16x32_bf16 v[68:71], v[132:135], a[112:115], v[68:71]\n"
"ds_read_b64_tr_b16 v[146:147], v187 offset:26624\n"
"v_mfma_f32_16x16x32_bf16 v[76:79], v[128:131], a[100:103], v[76:79]\n"
"v_mfma_f32_16x16x32_bf16 v[76:79], v[132:135], a[116:119], v[76:79]\n"
"v_mfma_f32_16x16x32_bf16 v[84:87], v[128:131], a[104:107], v[84:87]\n"
"v_mfma_f32_16x16x32_bf16 v[84:87], v[132:135], a[120:123], v[84:87]\n"
"v_mfma_f32_16x16x32_bf16 v[92:95], v[128:131], a[108:111], v[92:95]\n"
"v_mfma_f32_16x16x32_bf16 v[92:95], v[132:135], a[124:127], v[92:95]\n"
"s_waitcnt lgkmcnt(2)\n"
"v_mfma_f32_16x16x32_bf16 v[32:35], v[136:139], a[128:131], v[32:35]\n"
"ds_read_b64_tr_b16 v[128:129], v185 offset:45056\n"
"v_mfma_f32_16x16x32_bf16 v[40:43], v[136:139], a[132:135], v[40:43]\n"
"ds_read_b64_tr_b16 v[130:131], v185 offset:63488\n"
"v_mfma_f32_16x16x32_bf16 v[48:51], v[136:139], a[136:139], v[48:51]\n"
"v_mfma_f32_16x16x32_bf16 v[56:59], v[136:139], a[140:143], v[56:59]\n"
"s_waitcnt lgkmcnt(2)\n"
"v_mfma_f32_16x16x32_bf16 v[36:39], v[144:147], a[128:131], v[36:39]\n"
"ds_read_b64_tr_b16 v[136:137], v187 offset:45056\n"
"v_mfma_f32_16x16x32_bf16 v[44:47], v[144:147], a[132:135], v[44:47]\n"
"ds_read_b64_tr_b16 v[138:139], v187 offset:63488\n"
"v_mfma_f32_16x16x32_bf16 v[52:55], v[144:147], a[136:139], v[52:55]\n"
"v_mfma_f32_16x16x32_bf16 v[60:63], v[144:147], a[140:143], v[60:63]\n"
"s_waitcnt lgkmcnt(0)\n"
"s_waitcnt vmcnt(0)\n"
"s_barrier\n"
"v_mfma_f32_16x16x32_bf16 v[64:67], v[128:131], a[128:131], v[64:67]\n"
"ds_read_b64_tr_b16 v[144:145], v186 offset:8192\n"
"v_mfma_f32_16x16x32_bf16 v[72:75], v[128:131], a[132:135], v[72:75]\n"
"ds_read_b64_tr_b16 v[146:147], v186 offset:26624\n"
"v_add_u32 v189, 0x80, v189\n"
"s_add_u32 m0, s93, 0x1f000\n"
"v_mfma_f32_16x16x32_bf16 v[80:83], v[128:131], a[136:139], v[80:83]\n"
"buffer_load_dwordx4 v189, s[48:51], s82 offen offset:0 lds\n"
"v_mfma_f32_16x16x32_bf16 v[88:91], v[128:131], a[140:143], v[88:91]\n"
"v_mfma_f32_16x16x32_bf16 v[68:71], v[136:139], a[128:131], v[68:71]\n"
"ds_read_b64_tr_b16 v[128:129], v188 offset:8192\n"
"v_mfma_f32_16x16x32_bf16 v[76:79], v[136:139], a[132:135], v[76:79]\n"
"ds_read_b64_tr_b16 v[130:131], v188 offset:26624\n"
"v_mfma_f32_16x16x32_bf16 v[84:87], v[136:139], a[136:139], v[84:87]\n"
"buffer_load_dwordx4 v189, s[48:51], s83 offen offset:1024 lds\n"
"v_mfma_f32_16x16x32_bf16 v[92:95], v[136:139], a[140:143], v[92:95]\n"
"s_waitcnt lgkmcnt(2)\n"
"v_mfma_f32_16x16x32_bf16 v[0:3], v[144:147], a[128:131], 0\n"
"ds_read_b64_tr_b16 v[136:137], v186 offset:0\n"
"v_mfma_f32_16x16x32_bf16 v[8:11], v[144:147], a[132:135], 0\n"
"ds_read_b64_tr_b16 v[138:139], v186 offset:18432\n"
"v_mfma_f32_16x16x32_bf16 v[16:19], v[144:147], a[136:139], 0\n"
"ds_read_b64_tr_b16 v[140:141], v186 offset:1024\n"
"v_cvt_pk_bf16_f32 v32, v32, v33\n"
"buffer_load_dwordx4 v189, s[48:51], s84 offen offset:2048 lds\n"
"v_mfma_f32_16x16x32_bf16 v[24:27], v[144:147], a[140:143], 0\n"
"ds_read_b64_tr_b16 v[142:143], v186 offset:19456\n"
"v_cvt_pk_bf16_f32 v33, v34, v35\n"
"s_waitcnt lgkmcnt(4)\n"
"v_mfma_f32_16x16x32_bf16 v[4:7], v[128:131], a[128:131], 0\n"
"ds_read_b64_tr_b16 v[144:145], v188 offset:0\n"
"v_cvt_pk_bf16_f32 v34, v36, v37\n"
"v_mfma_f32_16x16x32_bf16 v[12:15], v[128:131], a[132:135], 0\n"
"ds_read_b64_tr_b16 v[146:147], v188 offset:18432\n"
"v_cvt_pk_bf16_f32 v35, v38, v39\n"
"v_mfma_f32_16x16x32_bf16 v[20:23], v[128:131], a[136:139], 0\n"
"ds_read_b64_tr_b16 v[148:149], v188 offset:1024\n"
"v_cvt_pk_bf16_f32 v40, v40, v41\n"
"buffer_load_dwordx4 v189, s[48:51], s85 offen offset:3072 lds\n"
"v_mfma_f32_16x16x32_bf16 v[28:31], v[128:131], a[140:143], 0\n"
"ds_read_b64_tr_b16 v[150:151], v188 offset:19456\n"
"v_cvt_pk_bf16_f32 v41, v42, v43\n"
"s_waitcnt lgkmcnt(4)\n"
"v_mfma_f32_16x16x32_bf16 v[0:3], v[136:139], a[0:3], v[0:3]\n"
"ds_read_b64_tr_b16 v[128:129], v186 offset:2048\n"
"v_cvt_pk_bf16_f32 v42, v44, v45\n"
"v_mfma_f32_16x16x32_bf16 v[0:3], v[140:143], a[16:19], v[0:3]\n"
"ds_read_b64_tr_b16 v[130:131], v186 offset:20480\n"
"v_cvt_pk_bf16_f32 v43, v46, v47\n"
"s_add_u32 m0, s93, 0x20000\n"
"v_mfma_f32_16x16x32_bf16 v[8:11], v[136:139], a[4:7], v[8:11]\n"
"ds_read_b64_tr_b16 v[132:133], v186 offset:3072\n"
"v_cvt_pk_bf16_f32 v48, v48, v49\n"
"buffer_load_dwordx4 v189, s[48:51], s86 offen offset:0 lds\n"
"v_mfma_f32_16x16x32_bf16 v[8:11], v[140:143], a[20:23], v[8:11]\n"
"ds_read_b64_tr_b16 v[134:135], v186 offset:21504\n"
"v_cvt_pk_bf16_f32 v49, v50, v51\n"
"v_mfma_f32_16x16x32_bf16 v[16:19], v[136:139], a[8:11], v[16:19]\n"
"v_cvt_pk_bf16_f32 v50, v52, v53\n"
"v_cvt_pk_bf16_f32 v51, v54, v55\n"
"v_cvt_pk_bf16_f32 v64, v64, v65\n"
"v_mfma_f32_16x16x32_bf16 v[16:19], v[140:143], a[24:27], v[16:19]\n"
"v_cvt_pk_bf16_f32 v56, v56, v57\n"
"v_cvt_pk_bf16_f32 v57, v58, v59\n"
"v_mfma_f32_16x16x32_bf16 v[24:27], v[136:139], a[12:15], v[24:27]\n"
"v_cvt_pk_bf16_f32 v58, v60, v61\n"
"v_cvt_pk_bf16_f32 v59, v62, v63\n"
"buffer_load_dwordx4 v189, s[48:51], s87 offen offset:1024 lds\n"
"v_mfma_f32_16x16x32_bf16 v[24:27], v[140:143], a[28:31], v[24:27]\n"
"v_cvt_pk_bf16_f32 v65, v66, v67\n"
"v_cvt_pk_bf16_f32 v66, v68, v69\n"
"s_waitcnt lgkmcnt(4)\n"
"v_mfma_f32_16x16x32_bf16 v[4:7], v[144:147], a[0:3], v[4:7]\n"
"ds_read_b64_tr_b16 v[136:137], v188 offset:2048\n"
"v_cvt_pk_bf16_f32 v67, v70, v71\n"
"v_mfma_f32_16x16x32_bf16 v[4:7], v[148:151], a[16:19], v[4:7]\n"
"ds_read_b64_tr_b16 v[138:139], v188 offset:20480\n"
"v_cvt_pk_bf16_f32 v72, v72, v73\n"
"v_mfma_f32_16x16x32_bf16 v[12:15], v[144:147], a[4:7], v[12:15]\n"
"ds_read_b64_tr_b16 v[140:141], v188 offset:3072\n"
"v_cvt_pk_bf16_f32 v73, v74, v75\n"
"buffer_load_dwordx4 v189, s[48:51], s88 offen offset:2048 lds\n"
"v_mfma_f32_16x16x32_bf16 v[12:15], v[148:151], a[20:23], v[12:15]\n"
"ds_read_b64_tr_b16 v[142:143], v188 offset:21504\n"
"v_cvt_pk_bf16_f32 v74, v76, v77\n"
"v_mfma_f32_16x16x32_bf16 v[20:23], v[144:147], a[8:11], v[20:23]\n"
"v_cvt_pk_bf16_f32 v75, v78, v79\n"
"v_cvt_pk_bf16_f32 v80, v80, v81\n"
"v_mfma_f32_16x16x32_bf16 v[20:23], v[148:151], a[24:27], v[20:23]\n"
"v_cvt_pk_bf16_f32 v81, v82, v83\n"
"v_cvt_pk_bf16_f32 v82, v84, v85\n"
"v_mfma_f32_16x16x32_bf16 v[28:31], v[144:147], a[12:15], v[28:31]\n"
"v_cvt_pk_bf16_f32 v83, v86, v87\n"
"v_cvt_pk_bf16_f32 v88, v88, v89\n"
"buffer_load_dwordx4 v189, s[48:51], s89 offen offset:3072 lds\n"
"v_mfma_f32_16x16x32_bf16 v[28:31], v[148:151], a[28:31], v[28:31]\n"
"v_cvt_pk_bf16_f32 v89, v90, v91\n"
"v_cvt_pk_bf16_f32 v90, v92, v93\n"
"s_waitcnt lgkmcnt(4)\n"
"v_mfma_f32_16x16x32_bf16 v[0:3], v[128:131], a[32:35], v[0:3]\n"
"ds_read_b64_tr_b16 v[144:145], v186 offset:4096\n"
"v_cvt_pk_bf16_f32 v91, v94, v95\n"
"v_mfma_f32_16x16x32_bf16 v[0:3], v[132:135], a[48:51], v[0:3]\n"
"ds_read_b64_tr_b16 v[146:147], v186 offset:22528\n"
"s_add_u32 m0, s93, 0x21000\n"
"v_mfma_f32_16x16x32_bf16 v[8:11], v[128:131], a[36:39], v[8:11]\n"
"ds_read_b64_tr_b16 v[148:149], v186 offset:5120\n"
"buffer_load_dwordx4 v189, s[48:51], s90 offen offset:0 lds\n"
"v_mfma_f32_16x16x32_bf16 v[8:11], v[132:135], a[52:55], v[8:11]\n"
"ds_read_b64_tr_b16 v[150:151], v186 offset:23552\n"
"v_mfma_f32_16x16x32_bf16 v[16:19], v[128:131], a[40:43], v[16:19]\n"
"v_mfma_f32_16x16x32_bf16 v[16:19], v[132:135], a[56:59], v[16:19]\n"
"v_add_u32 v189, 0x80, v189\n"
"s_add_u32 m0, s93, 0\n"
"v_mfma_f32_16x16x32_bf16 v[24:27], v[128:131], a[44:47], v[24:27]\n"
"buffer_load_dwordx4 v189, s[48:51], s82 offen offset:0 lds\n"
"v_mfma_f32_16x16x32_bf16 v[24:27], v[132:135], a[60:63], v[24:27]\n"
"s_waitcnt lgkmcnt(4)\n"
"v_mfma_f32_16x16x32_bf16 v[4:7], v[136:139], a[32:35], v[4:7]\n"
"ds_read_b64_tr_b16 v[128:129], v188 offset:4096\n"
"v_mfma_f32_16x16x32_bf16 v[4:7], v[140:143], a[48:51], v[4:7]\n"
"ds_read_b64_tr_b16 v[130:131], v188 offset:22528\n"
"v_mfma_f32_16x16x32_bf16 v[12:15], v[136:139], a[36:39], v[12:15]\n"
"ds_read_b64_tr_b16 v[132:133], v188 offset:5120\n"
"buffer_load_dwordx4 v189, s[48:51], s83 offen offset:1024 lds\n"
"v_mfma_f32_16x16x32_bf16 v[12:15], v[140:143], a[52:55], v[12:15]\n"
"ds_read_b64_tr_b16 v[134:135], v188 offset:23552\n"
"v_mfma_f32_16x16x32_bf16 v[20:23], v[136:139], a[40:43], v[20:23]\n"
"v_mfma_f32_16x16x32_bf16 v[20:23], v[140:143], a[56:59], v[20:23]\n"
"v_mfma_f32_16x16x32_bf16 v[28:31], v[136:139], a[44:47], v[28:31]\n"
"buffer_load_dwordx4 v189, s[48:51], s84 offen offset:2048 lds\n"
"v_mfma_f32_16x16x32_bf16 v[28:31], v[140:143], a[60:63], v[28:31]\n"
"s_waitcnt lgkmcnt(4)\n"
"v_mfma_f32_16x16x32_bf16 v[0:3], v[144:147], a[64:67], v[0:3]\n"
"ds_read_b64_tr_b16 v[136:137], v186 offset:6144\n"
"v_mfma_f32_16x16x32_bf16 v[0:3], v[148:151], a[80:83], v[0:3]\n"
"ds_read_b64_tr_b16 v[138:139], v186 offset:24576\n"
"v_mfma_f32_16x16x32_bf16 v[8:11], v[144:147], a[68:71], v[8:11]\n"
"ds_read_b64_tr_b16 v[140:141], v186 offset:7168\n"
"buffer_load_dwordx4 v189, s[48:51], s85 offen offset:3072 lds\n"
"v_mfma_f32_16x16x32_bf16 v[8:11], v[148:151], a[84:87], v[8:11]\n"
"ds_read_b64_tr_b16 v[142:143], v186 offset:25600\n"
"v_mfma_f32_16x16x32_bf16 v[16:19], v[144:147], a[72:75], v[16:19]\n"
"v_mfma_f32_16x16x32_bf16 v[16:19], v[148:151], a[88:91], v[16:19]\n"
"s_add_u32 m0, s93, 0x1000\n"
"v_mfma_f32_16x16x32_bf16 v[24:27], v[144:147], a[76:79], v[24:27]\n"
"buffer_load_dwordx4 v189, s[48:51], s86 offen offset:0 lds\n"
"v_mfma_f32_16x16x32_bf16 v[24:27], v[148:151], a[92:95], v[24:27]\n"
"s_waitcnt lgkmcnt(4)\n"
"v_mfma_f32_16x16x32_bf16 v[4:7], v[128:131], a[64:67], v[4:7]\n"
"ds_read_b64_tr_b16 v[144:145], v188 offset:6144\n"
"v_mfma_f32_16x16x32_bf16 v[4:7], v[132:135], a[80:83], v[4:7]\n"
"ds_read_b64_tr_b16 v[146:147], v188 offset:24576\n"
"v_mfma_f32_16x16x32_bf16 v[12:15], v[128:131], a[68:71], v[12:15]\n"
"ds_read_b64_tr_b16 v[148:149], v188 offset:7168\n"
"buffer_load_dwordx4 v189, s[48:51], s87 offen offset:1024 lds\n"
"v_mfma_f32_16x16x32_bf16 v[12:15], v[132:135], a[84:87], v[12:15]\n"
"ds_read_b64_tr_b16 v[150:151], v188 offset:25600\n"
"v_mfma_f32_16x16x32_bf16 v[20:23], v[128:131], a[72:75], v[20:23]\n"
"v_mfma_f32_16x16x32_bf16 v[20:23], v[132:135], a[88:91], v[20:23]\n"
"v_mfma_f32_16x16x32_bf16 v[28:31], v[128:131], a[76:79], v[28:31]\n"
"buffer_load_dwordx4 v189, s[48:51], s88 offen offset:2048 lds\n"
"v_mfma_f32_16x16x32_bf16 v[28:31], v[132:135], a[92:95], v[28:31]\n"
"s_waitcnt lgkmcnt(4)\n"
"v_mfma_f32_16x16x32_bf16 v[0:3], v[136:139], a[96:99], v[0:3]\n"
"v_mfma_f32_16x16x32_bf16 v[0:3], v[140:143], a[112:115], v[0:3]\n"
"v_mfma_f32_16x16x32_bf16 v[8:11], v[136:139], a[100:103], v[8:11]\n"
"buffer_load_dwordx4 v189, s[48:51], s89 offen offset:3072 lds\n"
"v_mfma_f32_16x16x32_bf16 v[8:11], v[140:143], a[116:119], v[8:11]\n"
"s_waitcnt lgkmcnt(0)\n"
"s_waitcnt vmcnt(8)\n"
"s_barrier\n"
"v_mfma_f32_16x16x32_bf16 v[16:19], v[136:139], a[104:107], v[16:19]\n"
"ds_read_b64_tr_b16 v[128:129], v186 offset:45056\n"
"v_mfma_f32_16x16x32_bf16 v[16:19], v[140:143], a[120:123], v[16:19]\n"
"ds_read_b64_tr_b16 v[130:131], v186 offset:63488\n"
"s_add_u32 m0, s93, 0x2000\n"
"v_mfma_f32_16x16x32_bf16 v[24:27], v[136:139], a[108:111], v[24:27]\n"
"buffer_load_dwordx4 v189, s[48:51], s90 offen offset:0 lds\n"
"v_mfma_f32_16x16x32_bf16 v[24:27], v[140:143], a[124:127], v[24:27]\n"
"v_mfma_f32_16x16x32_bf16 v[4:7], v[144:147], a[96:99], v[4:7]\n"
"ds_read_b64_tr_b16 v[136:137], v188 offset:45056\n"
"v_mfma_f32_16x16x32_bf16 v[4:7], v[148:151], a[112:115], v[4:7]\n"
"ds_read_b64_tr_b16 v[138:139], v188 offset:63488\n"
"s_mov_b32 s95, 0\n"
"v_mfma_f32_16x16x32_bf16 v[12:15], v[144:147], a[100:103], v[12:15]\n"
"buffer_store_dwordx4 v[32:35], v192, s[52:55], s95 offen offset:0 sc1\n"
"v_mfma_f32_16x16x32_bf16 v[12:15], v[148:151], a[116:119], v[12:15]\n"
"v_mfma_f32_16x16x32_bf16 v[20:23], v[144:147], a[104:107], v[20:23]\n"
"v_mfma_f32_16x16x32_bf16 v[20:23], v[148:151], a[120:123], v[20:23]\n"
"s_mov_b32 s95, 0\n"
"v_mfma_f32_16x16x32_bf16 v[28:31], v[144:147], a[108:111], v[28:31]\n"
"buffer_store_dwordx4 v[40:43], v192, s[52:55], s95 offen offset:1024 sc1\n"
"v_mfma_f32_16x16x32_bf16 v[28:31], v[148:151], a[124:127], v[28:31]\n"
"s_waitcnt lgkmcnt(2)\n"
"v_mfma_f32_16x16x32_bf16 v[96:99], v[128:131], a[128:131], 0\n"
"ds_read_b64_tr_b16 v[144:145], v186 offset:36864\n"
"v_mfma_f32_16x16x32_bf16 v[104:107], v[128:131], a[132:135], 0\n"
"ds_read_b64_tr_b16 v[146:147], v186 offset:55296\n"
"s_mov_b32 s95, 0\n"
"v_mfma_f32_16x16x32_bf16 v[112:115], v[128:131], a[136:139], 0\n"
"ds_read_b64_tr_b16 v[148:149], v186 offset:37888\n"
"v_cvt_pk_bf16_f32 v0, v0, v1\n"
"buffer_store_dwordx4 v[48:51], v192, s[52:55], s95 offen offset:2048 sc1\n"
"v_mfma_f32_16x16x32_bf16 v[120:123], v[128:131], a[140:143], 0\n"
"ds_read_b64_tr_b16 v[150:151], v186 offset:56320\n"
"v_cvt_pk_bf16_f32 v1, v2, v3\n"
"s_waitcnt lgkmcnt(4)\n"
"v_mfma_f32_16x16x32_bf16 v[100:103], v[136:139], a[128:131], 0\n"
"ds_read_b64_tr_b16 v[128:129], v188 offset:36864\n"
"v_cvt_pk_bf16_f32 v2, v4, v5\n"
"v_mfma_f32_16x16x32_bf16 v[108:111], v[136:139], a[132:135], 0\n"
"ds_read_b64_tr_b16 v[130:131], v188 offset:55296\n"
"v_cvt_pk_bf16_f32 v3, v6, v7\n"
"s_mov_b32 s95, 0\n"
"v_mfma_f32_16x16x32_bf16 v[116:119], v[136:139], a[136:139], 0\n"
"ds_read_b64_tr_b16 v[132:133], v188 offset:37888\n"
"v_cvt_pk_bf16_f32 v8, v8, v9\n"
"buffer_store_dwordx4 v[56:59], v192, s[52:55], s95 offen offset:3072 sc1\n"
"v_mfma_f32_16x16x32_bf16 v[124:127], v[136:139], a[140:143], 0\n"
"ds_read_b64_tr_b16 v[134:135], v188 offset:56320\n"
"v_cvt_pk_bf16_f32 v9, v10, v11\n"
"s_waitcnt lgkmcnt(4)\n"
"v_mfma_f32_16x16x32_bf16 v[96:99], v[144:147], a[0:3], v[96:99]\n"
"ds_read_b64_tr_b16 v[136:137], v186 offset:38912\n"
"v_cvt_pk_bf16_f32 v10, v12, v13\n"
"v_mfma_f32_16x16x32_bf16 v[96:99], v[148:151], a[16:19], v[96:99]\n"
"ds_read_b64_tr_b16 v[138:139], v186 offset:57344\n"
"v_cvt_pk_bf16_f32 v11, v14, v15\n"
"s_mov_b32 s95, 0x4000\n"
"v_mfma_f32_16x16x32_bf16 v[104:107], v[144:147], a[4:7], v[104:107]\n"
"ds_read_b64_tr_b16 v[140:141], v186 offset:39936\n"
"v_cvt_pk_bf16_f32 v16, v16, v17\n"
"buffer_store_dwordx4 v[64:67], v192, s[52:55], s95 offen offset:0 sc1\n"
"v_mfma_f32_16x16x32_bf16 v[104:107], v[148:151], a[20:23], v[104:107]\n"
"ds_read_b64_tr_b16 v[142:143], v186 offset:58368\n"
"v_cvt_pk_bf16_f32 v17, v18, v19\n"
"v_mfma_f32_16x16x32_bf16 v[112:115], v[144:147], a[8:11], v[112:115]\n"
"v_cvt_pk_bf16_f32 v18, v20, v21\n"
"v_cvt_pk_bf16_f32 v19, v22, v23\n"
"v_mfma_f32_16x16x32_bf16 v[112:115], v[148:151], a[24:27], v[112:115]\n"
"v_cvt_pk_bf16_f32 v24, v24, v25\n"
"v_cvt_pk_bf16_f32 v25, v26, v27\n"
"v_mfma_f32_16x16x32_bf16 v[120:123], v[144:147], a[12:15], v[120:123]\n"
"v_cvt_pk_bf16_f32 v26, v28, v29\n"
"v_cvt_pk_bf16_f32 v27, v30, v31\n"
"s_mov_b32 s95, 0x4000\n"
"v_mfma_f32_16x16x32_bf16 v[120:123], v[148:151], a[28:31], v[120:123]\n"
"buffer_store_dwordx4 v[72:75], v192, s[52:55], s95 offen offset:1024 sc1\n"
"s_waitcnt lgkmcnt(4)\n"
"v_mfma_f32_16x16x32_bf16 v[100:103], v[128:131], a[0:3], v[100:103]\n"
"ds_read_b64_tr_b16 v[144:145], v188 offset:38912\n"
"v_mfma_f32_16x16x32_bf16 v[100:103], v[132:135], a[16:19], v[100:103]\n"
"ds_read_b64_tr_b16 v[146:147], v188 offset:57344\n"
"v_mfma_f32_16x16x32_bf16 v[108:111], v[128:131], a[4:7], v[108:111]\n"
"ds_read_b64_tr_b16 v[148:149], v188 offset:39936\n"
"v_mfma_f32_16x16x32_bf16 v[108:111], v[132:135], a[20:23], v[108:111]\n"
"ds_read_b64_tr_b16 v[150:151], v188 offset:58368\n"
"s_mov_b32 s95, 0x4000\n"
"v_mfma_f32_16x16x32_bf16 v[116:119], v[128:131], a[8:11], v[116:119]\n"
"buffer_store_dwordx4 v[80:83], v192, s[52:55], s95 offen offset:2048 sc1\n"
"v_mfma_f32_16x16x32_bf16 v[116:119], v[132:135], a[24:27], v[116:119]\n"
"v_mfma_f32_16x16x32_bf16 v[124:127], v[128:131], a[12:15], v[124:127]\n"
"v_mfma_f32_16x16x32_bf16 v[124:127], v[132:135], a[28:31], v[124:127]\n"
"s_mov_b32 s95, 0x4000\n"
"s_waitcnt lgkmcnt(4)\n"
"v_mfma_f32_16x16x32_bf16 v[96:99], v[136:139], a[32:35], v[96:99]\n"
"ds_read_b64_tr_b16 v[128:129], v186 offset:40960\n"
"buffer_store_dwordx4 v[88:91], v192, s[52:55], s95 offen offset:3072 sc1\n"
"v_mfma_f32_16x16x32_bf16 v[96:99], v[140:143], a[48:51], v[96:99]\n"
"ds_read_b64_tr_b16 v[130:131], v186 offset:59392\n"
"v_mfma_f32_16x16x32_bf16 v[104:107], v[136:139], a[36:39], v[104:107]\n"
"ds_read_b64_tr_b16 v[132:133], v186 offset:41984\n"
"v_mfma_f32_16x16x32_bf16 v[104:107], v[140:143], a[52:55], v[104:107]\n"
"ds_read_b64_tr_b16 v[134:135], v186 offset:60416\n"
"v_add_u32 v189, 0x80, v189\n"
"s_add_u32 m0, s93, 0x9000\n"
"v_mfma_f32_16x16x32_bf16 v[112:115], v[136:139], a[40:43], v[112:115]\n"
"buffer_load_dwordx4 v189, s[48:51], s82 offen offset:0 lds\n"
"v_mfma_f32_16x16x32_bf16 v[112:115], v[140:143], a[56:59], v[112:115]\n"
"v_mfma_f32_16x16x32_bf16 v[120:123], v[136:139], a[44:47], v[120:123]\n"
"v_mfma_f32_16x16x32_bf16 v[120:123], v[140:143], a[60:63], v[120:123]\n"
"s_waitcnt lgkmcnt(4)\n"
"v_mfma_f32_16x16x32_bf16 v[100:103], v[144:147], a[32:35], v[100:103]\n"
"ds_read_b64_tr_b16 v[136:137], v188 offset:40960\n"
"buffer_load_dwordx4 v189, s[48:51], s83 offen offset:1024 lds\n"
"v_mfma_f32_16x16x32_bf16 v[100:103], v[148:151], a[48:51], v[100:103]\n"
"ds_read_b64_tr_b16 v[138:139], v188 offset:59392\n"
"v_mfma_f32_16x16x32_bf16 v[108:111], v[144:147], a[36:39], v[108:111]\n"
"ds_read_b64_tr_b16 v[140:141], v188 offset:41984\n"
"v_mfma_f32_16x16x32_bf16 v[108:111], v[148:151], a[52:55], v[108:111]\n"
"ds_read_b64_tr_b16 v[142:143], v188 offset:60416\n"
"v_mfma_f32_16x16x32_bf16 v[116:119], v[144:147], a[40:43], v[116:119]\n"
"buffer_load_dwordx4 v189, s[48:51], s84 offen offset:2048 lds\n"
"v_mfma_f32_16x16x32_bf16 v[116:119], v[148:151], a[56:59], v[116:119]\n"
"v_mfma_f32_16x16x32_bf16 v[124:127], v[144:147], a[44:47], v[124:127]\n"
"v_mfma_f32_16x16x32_bf16 v[124:127], v[148:151], a[60:63], v[124:127]\n"
"s_waitcnt lgkmcnt(4)\n"
"v_mfma_f32_16x16x32_bf16 v[96:99], v[128:131], a[64:67], v[96:99]\n"
"ds_read_b64_tr_b16 v[144:145], v186 offset:43008\n"
"buffer_load_dwordx4 v189, s[48:51], s85 offen offset:3072 lds\n"
"v_mfma_f32_16x16x32_bf16 v[96:99], v[132:135], a[80:83], v[96:99]\n"
"ds_read_b64_tr_b16 v[146:147], v186 offset:61440\n"
"v_mfma_f32_16x16x32_bf16 v[104:107], v[128:131], a[68:71], v[104:107]\n"
"ds_read_b64_tr_b16 v[148:149], v186 offset:44032\n"
"v_mfma_f32_16x16x32_bf16 v[104:107], v[132:135], a[84:87], v[104:107]\n"
"ds_read_b64_tr_b16 v[150:151], v186 offset:62464\n"
"s_add_u32 m0, s93, 0xa000\n"
"v_mfma_f32_16x16x32_bf16 v[112:115], v[128:131], a[72:75], v[112:115]\n"
"buffer_load_dwordx4 v189, s[48:51], s86 offen offset:0 lds\n"
"v_mfma_f32_16x16x32_bf16 v[112:115], v[132:135], a[88:91], v[112:115]\n"
"v_mfma_f32_16x16x32_bf16 v[120:123], v[128:131], a[76:79], v[120:123]\n"
"v_mfma_f32_16x16x32_bf16 v[120:123], v[132:135], a[92:95], v[120:123]\n"
"s_waitcnt lgkmcnt(4)\n"
"v_mfma_f32_16x16x32_bf16 v[100:103], v[136:139], a[64:67], v[100:103]\n"
"ds_read_b64_tr_b16 v[128:129], v188 offset:43008\n"
"buffer_load_dwordx4 v189, s[48:51], s87 offen offset:1024 lds\n"
"v_mfma_f32_16x16x32_bf16 v[100:103], v[140:143], a[80:83], v[100:103]\n"
"ds_read_b64_tr_b16 v[130:131], v188 offset:61440\n"
"v_mfma_f32_16x16x32_bf16 v[108:111], v[136:139], a[68:71], v[108:111]\n"
"ds_read_b64_tr_b16 v[132:133], v188 offset:44032\n"
"v_mfma_f32_16x16x32_bf16 v[108:111], v[140:143], a[84:87], v[108:111]\n"
"ds_read_b64_tr_b16 v[134:135], v188 offset:62464\n"
"v_mfma_f32_16x16x32_bf16 v[116:119], v[136:139], a[72:75], v[116:119]\n"
"buffer_load_dwordx4 v189, s[48:51], s88 offen offset:2048 lds\n"
"v_mfma_f32_16x16x32_bf16 v[116:119], v[140:143], a[88:91], v[116:119]\n"
"v_mfma_f32_16x16x32_bf16 v[124:127], v[136:139], a[76:79], v[124:127]\n"
"v_mfma_f32_16x16x32_bf16 v[124:127], v[140:143], a[92:95], v[124:127]\n"
"s_waitcnt lgkmcnt(4)\n"
"v_mfma_f32_16x16x32_bf16 v[96:99], v[144:147], a[96:99], v[96:99]\n"
"buffer_load_dwordx4 v189, s[48:51], s89 offen offset:3072 lds\n"
"v_mfma_f32_16x16x32_bf16 v[96:99], v[148:151], a[112:115], v[96:99]\n"
"v_mfma_f32_16x16x32_bf16 v[104:107], v[144:147], a[100:103], v[104:107]\n"
"v_mfma_f32_16x16x32_bf16 v[104:107], v[148:151], a[116:119], v[104:107]\n"
"s_waitcnt lgkmcnt(0)\n"
"s_waitcnt vmcnt(16)\n"
"s_barrier\n"
"s_add_u32 m0, s93, 0xb000\n"
"v_mfma_f32_16x16x32_bf16 v[112:115], v[144:147], a[104:107], v[112:115]\n"
"ds_read_b64_tr_b16 v[136:137], v185 offset:8192\n"
"buffer_load_dwordx4 v189, s[48:51], s90 offen offset:0 lds\n"
"v_mfma_f32_16x16x32_bf16 v[112:115], v[148:151], a[120:123], v[112:115]\n"
"ds_read_b64_tr_b16 v[138:139], v185 offset:26624\n"
"v_mfma_f32_16x16x32_bf16 v[120:123], v[144:147], a[108:111], v[120:123]\n"
"v_mfma_f32_16x16x32_bf16 v[120:123], v[148:151], a[124:127], v[120:123]\n"
"s_mov_b32 s95, 0x8000\n"
"v_mfma_f32_16x16x32_bf16 v[100:103], v[128:131], a[96:99], v[100:103]\n"
"ds_read_b64_tr_b16 v[144:145], v187 offset:8192\n"
"buffer_store_dwordx4 v[0:3], v192, s[52:55], s95 offen offset:0 sc1\n"
"v_mfma_f32_16x16x32_bf16 v[100:103], v[132:135], a[112:115], v[100:103]\n"
"ds_read_b64_tr_b16 v[146:147], v187 offset:26624\n"
"v_mfma_f32_16x16x32_bf16 v[108:111], v[128:131], a[100:103], v[108:111]\n"
"v_mfma_f32_16x16x32_bf16 v[108:111], v[132:135], a[116:119], v[108:111]\n"
"s_mov_b32 s95, 0x8000\n"
"v_mfma_f32_16x16x32_bf16 v[116:119], v[128:131], a[104:107], v[116:119]\n"
"buffer_store_dwordx4 v[8:11], v192, s[52:55], s95 offen offset:1024 sc1\n"
"v_mfma_f32_16x16x32_bf16 v[116:119], v[132:135], a[120:123], v[116:119]\n"
"v_mfma_f32_16x16x32_bf16 v[124:127], v[128:131], a[108:111], v[124:127]\n"
"v_mfma_f32_16x16x32_bf16 v[124:127], v[132:135], a[124:127], v[124:127]\n"
"s_mov_b32 s95, 0x8000\n"
"s_waitcnt lgkmcnt(2)\n"
"v_mfma_f32_16x16x32_bf16 v[32:35], v[136:139], a[128:131], 0\n"
"ds_read_b64_tr_b16 v[128:129], v185 offset:0\n"
"buffer_store_dwordx4 v[16:19], v192, s[52:55], s95 offen offset:2048 sc1\n"
"v_mfma_f32_16x16x32_bf16 v[40:43], v[136:139], a[132:135], 0\n"
"ds_read_b64_tr_b16 v[130:131], v185 offset:18432\n"
"v_mfma_f32_16x16x32_bf16 v[48:51], v[136:139], a[136:139], 0\n"
"ds_read_b64_tr_b16 v[132:133], v185 offset:1024\n"
"v_cvt_pk_bf16_f32 v96, v96, v97\n"
"v_mfma_f32_16x16x32_bf16 v[56:59], v[136:139], a[140:143], 0\n"
"ds_read_b64_tr_b16 v[134:135], v185 offset:19456\n"
"v_cvt_pk_bf16_f32 v97, v98, v99\n"
"s_mov_b32 s95, 0x8000\n"
"s_waitcnt lgkmcnt(4)\n"
"v_mfma_f32_16x16x32_bf16 v[36:39], v[144:147], a[128:131], 0\n"
"ds_read_b64_tr_b16 v[136:137], v187 offset:0\n"
"v_cvt_pk_bf16_f32 v98, v100, v101\n"
"buffer_store_dwordx4 v[24:27], v192, s[52:55], s95 offen offset:3072 sc1\n"
"v_mfma_f32_16x16x32_bf16 v[44:47], v[144:147], a[132:135], 0\n"
"ds_read_b64_tr_b16 v[138:139], v187 offset:18432\n"
"v_cvt_pk_bf16_f32 v99, v102, v103\n"
"v_mfma_f32_16x16x32_bf16 v[52:55], v[144:147], a[136:139], 0\n"
"ds_read_b64_tr_b16 v[140:141], v187 offset:1024\n"
"v_cvt_pk_bf16_f32 v104, v104, v105\n"
"v_mfma_f32_16x16x32_bf16 v[60:63], v[144:147], a[140:143], 0\n"
"ds_read_b64_tr_b16 v[142:143], v187 offset:19456\n"
"v_cvt_pk_bf16_f32 v105, v106, v107\n"
"v_add_u32 v189, 0x80, v189\n"
"s_add_u32 m0, s93, 0x16000\n"
"s_waitcnt lgkmcnt(4)\n"
"v_mfma_f32_16x16x32_bf16 v[32:35], v[128:131], a[0:3], v[32:35]\n"
"ds_read_b64_tr_b16 v[144:145], v185 offset:2048\n"
"v_cvt_pk_bf16_f32 v106, v108, v109\n"
"buffer_load_dwordx4 v189, s[48:51], s82 offen offset:0 lds\n"
"v_mfma_f32_16x16x32_bf16 v[32:35], v[132:135], a[16:19], v[32:35]\n"
"ds_read_b64_tr_b16 v[146:147], v185 offset:20480\n"
"v_cvt_pk_bf16_f32 v107, v110, v111\n"
"v_mfma_f32_16x16x32_bf16 v[40:43], v[128:131], a[4:7], v[40:43]\n"
"ds_read_b64_tr_b16 v[148:149], v185 offset:3072\n"
"v_cvt_pk_bf16_f32 v112, v112, v113\n"
"v_mfma_f32_16x16x32_bf16 v[40:43], v[132:135], a[20:23], v[40:43]\n"
"ds_read_b64_tr_b16 v[150:151], v185 offset:21504\n"
"v_cvt_pk_bf16_f32 v113, v114, v115\n"
"v_mfma_f32_16x16x32_bf16 v[48:51], v[128:131], a[8:11], v[48:51]\n"
"v_cvt_pk_bf16_f32 v114, v116, v117\n"
"v_cvt_pk_bf16_f32 v115, v118, v119\n"
"buffer_load_dwordx4 v189, s[48:51], s83 offen offset:1024 lds\n"
"v_mfma_f32_16x16x32_bf16 v[48:51], v[132:135], a[24:27], v[48:51]\n"
"v_cvt_pk_bf16_f32 v120, v120, v121\n"
"v_cvt_pk_bf16_f32 v121, v122, v123\n"
"v_mfma_f32_16x16x32_bf16 v[56:59], v[128:131], a[12:15], v[56:59]\n"
"v_cvt_pk_bf16_f32 v122, v124, v125\n"
"v_cvt_pk_bf16_f32 v123, v126, v127\n"
"v_mfma_f32_16x16x32_bf16 v[56:59], v[132:135], a[28:31], v[56:59]\n"
"s_waitcnt lgkmcnt(4)\n"
"v_mfma_f32_16x16x32_bf16 v[36:39], v[136:139], a[0:3], v[36:39]\n"
"ds_read_b64_tr_b16 v[128:129], v187 offset:2048\n"
"buffer_load_dwordx4 v189, s[48:51], s84 offen offset:2048 lds\n"
"v_mfma_f32_16x16x32_bf16 v[36:39], v[140:143], a[16:19], v[36:39]\n"
"ds_read_b64_tr_b16 v[130:131], v187 offset:20480\n"
"v_mfma_f32_16x16x32_bf16 v[44:47], v[136:139], a[4:7], v[44:47]\n"
"ds_read_b64_tr_b16 v[132:133], v187 offset:3072\n"
"v_mfma_f32_16x16x32_bf16 v[44:47], v[140:143], a[20:23], v[44:47]\n"
"ds_read_b64_tr_b16 v[134:135], v187 offset:21504\n"
"v_mfma_f32_16x16x32_bf16 v[52:55], v[136:139], a[8:11], v[52:55]\n"
"buffer_load_dwordx4 v189, s[48:51], s85 offen offset:3072 lds\n"
"v_mfma_f32_16x16x32_bf16 v[52:55], v[140:143], a[24:27], v[52:55]\n"
"v_mfma_f32_16x16x32_bf16 v[60:63], v[136:139], a[12:15], v[60:63]\n"
"v_mfma_f32_16x16x32_bf16 v[60:63], v[140:143], a[28:31], v[60:63]\n"
"s_add_u32 m0, s93, 0x17000\n"
"s_waitcnt lgkmcnt(4)\n"
"v_mfma_f32_16x16x32_bf16 v[32:35], v[144:147], a[32:35], v[32:35]\n"
"ds_read_b64_tr_b16 v[136:137], v185 offset:4096\n"
"buffer_load_dwordx4 v189, s[48:51], s86 offen offset:0 lds\n"
"v_mfma_f32_16x16x32_bf16 v[32:35], v[148:151], a[48:51], v[32:35]\n"
"ds_read_b64_tr_b16 v[138:139], v185 offset:22528\n"
"v_mfma_f32_16x16x32_bf16 v[40:43], v[144:147], a[36:39], v[40:43]\n"
"ds_read_b64_tr_b16 v[140:141], v185 offset:5120\n"
"v_mfma_f32_16x16x32_bf16 v[40:43], v[148:151], a[52:55], v[40:43]\n"
"ds_read_b64_tr_b16 v[142:143], v185 offset:23552\n"
"v_mfma_f32_16x16x32_bf16 v[48:51], v[144:147], a[40:43], v[48:51]\n"
"buffer_load_dwordx4 v189, s[48:51], s87 offen offset:1024 lds\n"
"v_mfma_f32_16x16x32_bf16 v[48:51], v[148:151], a[56:59], v[48:51]\n"
"v_mfma_f32_16x16x32_bf16 v[56:59], v[144:147], a[44:47], v[56:59]\n"
"v_mfma_f32_16x16x32_bf16 v[56:59], v[148:151], a[60:63], v[56:59]\n"
"s_waitcnt lgkmcnt(4)\n"
"v_mfma_f32_16x16x32_bf16 v[36:39], v[128:131], a[32:35], v[36:39]\n"
"ds_read_b64_tr_b16 v[144:145], v187 offset:4096\n"
"buffer_load_dwordx4 v189, s[48:51], s88 offen offset:2048 lds\n"
"v_mfma_f32_16x16x32_bf16 v[36:39], v[132:135], a[48:51], v[36:39]\n"
"ds_read_b64_tr_b16 v[146:147], v187 offset:22528\n"
"v_mfma_f32_16x16x32_bf16 v[44:47], v[128:131], a[36:39], v[44:47]\n"
"ds_read_b64_tr_b16 v[148:149], v187 offset:5120\n"
"v_mfma_f32_16x16x32_bf16 v[44:47], v[132:135], a[52:55], v[44:47]\n"
"ds_read_b64_tr_b16 v[150:151], v187 offset:23552\n"
"v_mfma_f32_16x16x32_bf16 v[52:55], v[128:131], a[40:43], v[52:55]\n"
"buffer_load_dwordx4 v189, s[48:51], s89 offen offset:3072 lds\n"
"v_mfma_f32_16x16x32_bf16 v[52:55], v[132:135], a[56:59], v[52:55]\n"
"v_mfma_f32_16x16x32_bf16 v[60:63], v[128:131], a[44:47], v[60:63]\n"
"v_mfma_f32_16x16x32_bf16 v[60:63], v[132:135], a[60:63], v[60:63]\n"
"s_add_u32 m0, s93, 0x18000\n"
"s_waitcnt lgkmcnt(4)\n"
"v_mfma_f32_16x16x32_bf16 v[32:35], v[136:139], a[64:67], v[32:35]\n"
"ds_read_b64_tr_b16 v[128:129], v185 offset:6144\n"
"buffer_load_dwordx4 v189, s[48:51], s90 offen offset:0 lds\n"
"v_mfma_f32_16x16x32_bf16 v[32:35], v[140:143], a[80:83], v[32:35]\n"
"ds_read_b64_tr_b16 v[130:131], v185 offset:24576\n"
"v_mfma_f32_16x16x32_bf16 v[40:43], v[136:139], a[68:71], v[40:43]\n"
"ds_read_b64_tr_b16 v[132:133], v185 offset:7168\n"
"v_mfma_f32_16x16x32_bf16 v[40:43], v[140:143], a[84:87], v[40:43]\n"
"ds_read_b64_tr_b16 v[134:135], v185 offset:25600\n"
"s_mov_b32 s95, 0xc000\n"
"v_mfma_f32_16x16x32_bf16 v[48:51], v[136:139], a[72:75], v[48:51]\n"
"buffer_store_dwordx4 v[96:99], v192, s[52:55], s95 offen offset:0 sc1\n"
"v_mfma_f32_16x16x32_bf16 v[48:51], v[140:143], a[88:91], v[48:51]\n"
"v_mfma_f32_16x16x32_bf16 v[56:59], v[136:139], a[76:79], v[56:59]\n"
"v_mfma_f32_16x16x32_bf16 v[56:59], v[140:143], a[92:95], v[56:59]\n"
"s_mov_b32 s95, 0xc000\n"
"s_waitcnt lgkmcnt(4)\n"
"v_mfma_f32_16x16x32_bf16 v[36:39], v[144:147], a[64:67], v[36:39]\n"
"ds_read_b64_tr_b16 v[136:137], v187 offset:6144\n"
"buffer_store_dwordx4 v[104:107], v192, s[52:55], s95 offen offset:1024 sc1\n"
"v_mfma_f32_16x16x32_bf16 v[36:39], v[148:151], a[80:83], v[36:39]\n"
"ds_read_b64_tr_b16 v[138:139], v187 offset:24576\n"
"v_mfma_f32_16x16x32_bf16 v[44:47], v[144:147], a[68:71], v[44:47]\n"
"ds_read_b64_tr_b16 v[140:141], v187 offset:7168\n"
"v_mfma_f32_16x16x32_bf16 v[44:47], v[148:151], a[84:87], v[44:47]\n"
"ds_read_b64_tr_b16 v[142:143], v187 offset:25600\n"
"s_mov_b32 s95, 0xc000\n"
"v_mfma_f32_16x16x32_bf16 v[52:55], v[144:147], a[72:75], v[52:55]\n"
"buffer_store_dwordx4 v[112:115], v192, s[52:55], s95 offen offset:2048 sc1\n"
"v_mfma_f32_16x16x32_bf16 v[52:55], v[148:151], a[88:91], v[52:55]\n"
"v_mfma_f32_16x16x32_bf16 v[60:63], v[144:147], a[76:79], v[60:63]\n"
"v_mfma_f32_16x16x32_bf16 v[60:63], v[148:151], a[92:95], v[60:63]\n"
"s_waitcnt lgkmcnt(4)\n"
"v_mfma_f32_16x16x32_bf16 v[32:35], v[128:131], a[96:99], v[32:35]\n"
"s_waitcnt vmcnt(12)\n"
"s_mov_b64 exec, 1\n"
"s_nop 0\n"
"buffer_store_dwordx2 v[198:199], v201, s[8:11], 0 offen offset:0 sc1\n"
"s_mov_b64 exec, -1\n"
"s_nop 0\n"
"v_mfma_f32_16x16x32_bf16 v[32:35], v[132:135], a[112:115], v[32:35]\n"
"s_mov_b32 s95, 0xc000\n"
"v_mfma_f32_16x16x32_bf16 v[40:43], v[128:131], a[100:103], v[40:43]\n"
"buffer_store_dwordx4 v[120:123], v192, s[52:55], s95 offen offset:3072 sc1\n"
"v_mfma_f32_16x16x32_bf16 v[40:43], v[132:135], a[116:119], v[40:43]\n"
"s_waitcnt lgkmcnt(0)\n"
"s_barrier\n"
"v_mfma_f32_16x16x32_bf16 v[48:51], v[128:131], a[104:107], v[48:51]\n"
"ds_read_b64_tr_b16 v[144:145], v185 offset:45056\n"
"v_mfma_f32_16x16x32_bf16 v[48:51], v[132:135], a[120:123], v[48:51]\n"
"ds_read_b64_tr_b16 v[146:147], v185 offset:63488\n"
"v_add_u32 v189, 0x80, v189\n"
"s_add_u32 m0, s93, 0x1f000\n"
"v_mfma_f32_16x16x32_bf16 v[56:59], v[128:131], a[108:111], v[56:59]\n"
"buffer_load_dwordx4 v189, s[48:51], s82 offen offset:0 lds\n"
"v_mfma_f32_16x16x32_bf16 v[56:59], v[132:135], a[124:127], v[56:59]\n"
"v_mfma_f32_16x16x32_bf16 v[36:39], v[136:139], a[96:99], v[36:39]\n"
"ds_read_b64_tr_b16 v[128:129], v187 offset:45056\n"
"v_mfma_f32_16x16x32_bf16 v[36:39], v[140:143], a[112:115], v[36:39]\n"
"ds_read_b64_tr_b16 v[130:131], v187 offset:63488\n"
"v_mfma_f32_16x16x32_bf16 v[44:47], v[136:139], a[100:103], v[44:47]\n"
"buffer_load_dwordx4 v189, s[48:51], s83 offen offset:1024 lds\n"
"v_mfma_f32_16x16x32_bf16 v[44:47], v[140:143], a[116:119], v[44:47]\n"
"v_mfma_f32_16x16x32_bf16 v[52:55], v[136:139], a[104:107], v[52:55]\n"
"v_mfma_f32_16x16x32_bf16 v[52:55], v[140:143], a[120:123], v[52:55]\n"
"v_mfma_f32_16x16x32_bf16 v[60:63], v[136:139], a[108:111], v[60:63]\n"
"buffer_load_dwordx4 v189, s[48:51], s84 offen offset:2048 lds\n"
"v_mfma_f32_16x16x32_bf16 v[60:63], v[140:143], a[124:127], v[60:63]\n"
"s_waitcnt lgkmcnt(2)\n"
"v_mfma_f32_16x16x32_bf16 v[64:67], v[144:147], a[128:131], 0\n"
"ds_read_b64_tr_b16 v[136:137], v185 offset:36864\n"
"v_mfma_f32_16x16x32_bf16 v[72:75], v[144:147], a[132:135], 0\n"
"ds_read_b64_tr_b16 v[138:139], v185 offset:55296\n"
"v_mfma_f32_16x16x32_bf16 v[80:83], v[144:147], a[136:139], 0\n"
"ds_read_b64_tr_b16 v[140:141], v185 offset:37888\n"
"v_cvt_pk_bf16_f32 v32, v32, v33\n"
"buffer_load_dwordx4 v189, s[48:51], s85 offen offset:3072 lds\n"
"v_mfma_f32_16x16x32_bf16 v[88:91], v[144:147], a[140:143], 0\n"
"ds_read_b64_tr_b16 v[142:143], v185 offset:56320\n"
"v_cvt_pk_bf16_f32 v33, v34, v35\n"
"s_waitcnt lgkmcnt(4)\n"
"v_mfma_f32_16x16x32_bf16 v[68:71], v[128:131], a[128:131], 0\n"
"ds_read_b64_tr_b16 v[144:145], v187 offset:36864\n"
"v_cvt_pk_bf16_f32 v34, v36, v37\n"
"v_mfma_f32_16x16x32_bf16 v[76:79], v[128:131], a[132:135], 0\n"
"ds_read_b64_tr_b16 v[146:147], v187 offset:55296\n"
"v_cvt_pk_bf16_f32 v35, v38, v39\n"
"s_add_u32 m0, s93, 0x20000\n"
"v_mfma_f32_16x16x32_bf16 v[84:87], v[128:131], a[136:139], 0\n"
"ds_read_b64_tr_b16 v[148:149], v187 offset:37888\n"
"v_cvt_pk_bf16_f32 v40, v40, v41\n"
"buffer_load_dwordx4 v189, s[48:51], s86 offen offset:0 lds\n"
"v_mfma_f32_16x16x32_bf16 v[92:95], v[128:131], a[140:143], 0\n"
"ds_read_b64_tr_b16 v[150:151], v187 offset:56320\n"
"v_cvt_pk_bf16_f32 v41, v42, v43\n"
"s_waitcnt lgkmcnt(4)\n"
"v_mfma_f32_16x16x32_bf16 v[64:67], v[136:139], a[0:3], v[64:67]\n"
"ds_read_b64_tr_b16 v[128:129], v185 offset:38912\n"
"v_cvt_pk_bf16_f32 v42, v44, v45\n"
"v_mfma_f32_16x16x32_bf16 v[64:67], v[140:143], a[16:19], v[64:67]\n"
"ds_read_b64_tr_b16 v[130:131], v185 offset:57344\n"
"v_cvt_pk_bf16_f32 v43, v46, v47\n"
"v_mfma_f32_16x16x32_bf16 v[72:75], v[136:139], a[4:7], v[72:75]\n"
"ds_read_b64_tr_b16 v[132:133], v185 offset:39936\n"
"v_cvt_pk_bf16_f32 v48, v48, v49\n"
"buffer_load_dwordx4 v189, s[48:51], s87 offen offset:1024 lds\n"
"v_mfma_f32_16x16x32_bf16 v[72:75], v[140:143], a[20:23], v[72:75]\n"
"ds_read_b64_tr_b16 v[134:135], v185 offset:58368\n"
"v_cvt_pk_bf16_f32 v49, v50, v51\n"
"v_mfma_f32_16x16x32_bf16 v[80:83], v[136:139], a[8:11], v[80:83]\n"
"v_cvt_pk_bf16_f32 v50, v52, v53\n"
"v_cvt_pk_bf16_f32 v51, v54, v55\n"
"v_mfma_f32_16x16x32_bf16 v[80:83], v[140:143], a[24:27], v[80:83]\n"
"v_cvt_pk_bf16_f32 v56, v56, v57\n"
"v_cvt_pk_bf16_f32 v57, v58, v59\n"
"v_mfma_f32_16x16x32_bf16 v[88:91], v[136:139], a[12:15], v[88:91]\n"
"v_cvt_pk_bf16_f32 v58, v60, v61\n"
"v_cvt_pk_bf16_f32 v59, v62, v63\n"
"v_mfma_f32_16x16x32_bf16 v[88:91], v[140:143], a[28:31], v[88:91]\n"
"buffer_load_dwordx4 v189, s[48:51], s88 offen offset:2048 lds\n"
"s_waitcnt lgkmcnt(4)\n"
"v_mfma_f32_16x16x32_bf16 v[68:71], v[144:147], a[0:3], v[68:71]\n"
"ds_read_b64_tr_b16 v[136:137], v187 offset:38912\n"
"v_mfma_f32_16x16x32_bf16 v[68:71], v[148:151], a[16:19], v[68:71]\n"
"ds_read_b64_tr_b16 v[138:139], v187 offset:57344\n"
"v_mfma_f32_16x16x32_bf16 v[76:79], v[144:147], a[4:7], v[76:79]\n"
"ds_read_b64_tr_b16 v[140:141], v187 offset:39936\n"
"v_mfma_f32_16x16x32_bf16 v[76:79], v[148:151], a[20:23], v[76:79]\n"
"ds_read_b64_tr_b16 v[142:143], v187 offset:58368\n"
"v_mfma_f32_16x16x32_bf16 v[84:87], v[144:147], a[8:11], v[84:87]\n"
"buffer_load_dwordx4 v189, s[48:51], s89 offen offset:3072 lds\n"
"v_mfma_f32_16x16x32_bf16 v[84:87], v[148:151], a[24:27], v[84:87]\n"
"v_mfma_f32_16x16x32_bf16 v[92:95], v[144:147], a[12:15], v[92:95]\n"
"v_mfma_f32_16x16x32_bf16 v[92:95], v[148:151], a[28:31], v[92:95]\n"
"s_waitcnt lgkmcnt(4)\n"
"v_mfma_f32_16x16x32_bf16 v[64:67], v[128:131], a[32:35], v[64:67]\n"
"ds_read_b64_tr_b16 v[144:145], v185 offset:40960\n"
"buffer_load_dwordx2 v[206:207], v200, s[8:11], 0 offen offset:0 sc1\n"
"v_mfma_f32_16x16x32_bf16 v[64:67], v[132:135], a[48:51], v[64:67]\n"
"ds_read_b64_tr_b16 v[146:147], v185 offset:59392\n"
"s_add_u32 m0, s93, 0x21000\n"
"v_mfma_f32_16x16x32_bf16 v[72:75], v[128:131], a[36:39], v[72:75]\n"
"ds_read_b64_tr_b16 v[148:149], v185 offset:41984\n"
"buffer_load_dwordx4 v189, s[48:51], s90 offen offset:0 lds\n"
"v_mfma_f32_16x16x32_bf16 v[72:75], v[132:135], a[52:55], v[72:75]\n"
"ds_read_b64_tr_b16 v[150:151], v185 offset:60416\n"
"v_mfma_f32_16x16x32_bf16 v[80:83], v[128:131], a[40:43], v[80:83]\n"
"v_mfma_f32_16x16x32_bf16 v[80:83], v[132:135], a[56:59], v[80:83]\n"
"s_mov_b32 s95, 0x10000\n"
"v_mfma_f32_16x16x32_bf16 v[88:91], v[128:131], a[44:47], v[88:91]\n"
"buffer_store_dwordx4 v[32:35], v192, s[52:55], s95 offen offset:0 sc1\n"
"v_mfma_f32_16x16x32_bf16 v[88:91], v[132:135], a[60:63], v[88:91]\n"
"s_waitcnt lgkmcnt(4)\n"
"v_mfma_f32_16x16x32_bf16 v[68:71], v[136:139], a[32:35], v[68:71]\n"
"ds_read_b64_tr_b16 v[128:129], v187 offset:40960\n"
"v_mfma_f32_16x16x32_bf16 v[68:71], v[140:143], a[48:51], v[68:71]\n"
"ds_read_b64_tr_b16 v[130:131], v187 offset:59392\n"
"s_mov_b32 s95, 0x10000\n"
"v_mfma_f32_16x16x32_bf16 v[76:79], v[136:139], a[36:39], v[76:79]\n"
"ds_read_b64_tr_b16 v[132:133], v187 offset:41984\n"
"buffer_store_dwordx4 v[40:43], v192, s[52:55], s95 offen offset:1024 sc1\n"
"v_mfma_f32_16x16x32_bf16 v[76:79], v[140:143], a[52:55], v[76:79]\n"
"ds_read_b64_tr_b16 v[134:135], v187 offset:60416\n"
"v_mfma_f32_16x16x32_bf16 v[84:87], v[136:139], a[40:43], v[84:87]\n"
"v_mfma_f32_16x16x32_bf16 v[84:87], v[140:143], a[56:59], v[84:87]\n"
"s_mov_b32 s95, 0x10000\n"
"v_mfma_f32_16x16x32_bf16 v[92:95], v[136:139], a[44:47], v[92:95]\n"
"buffer_store_dwordx4 v[48:51], v192, s[52:55], s95 offen offset:2048 sc1\n"
"v_mfma_f32_16x16x32_bf16 v[92:95], v[140:143], a[60:63], v[92:95]\n"
"s_waitcnt lgkmcnt(4)\n"
"v_mfma_f32_16x16x32_bf16 v[64:67], v[144:147], a[64:67], v[64:67]\n"
"ds_read_b64_tr_b16 v[136:137], v185 offset:43008\n"
"v_mfma_f32_16x16x32_bf16 v[64:67], v[148:151], a[80:83], v[64:67]\n"
"ds_read_b64_tr_b16 v[138:139], v185 offset:61440\n"
"s_mov_b32 s95, 0x10000\n"
"v_mfma_f32_16x16x32_bf16 v[72:75], v[144:147], a[68:71], v[72:75]\n"
"ds_read_b64_tr_b16 v[140:141], v185 offset:44032\n"
"buffer_store_dwordx4 v[56:59], v192, s[52:55], s95 offen offset:3072 sc1\n"
"v_mfma_f32_16x16x32_bf16 v[72:75], v[148:151], a[84:87], v[72:75]\n"
"ds_read_b64_tr_b16 v[142:143], v185 offset:62464\n"
"v_mfma_f32_16x16x32_bf16 v[80:83], v[144:147], a[72:75], v[80:83]\n"
"v_mfma_f32_16x16x32_bf16 v[80:83], v[148:151], a[88:91], v[80:83]\n"
"v_add_u32 v189, 0x80, v189\n"
"s_add_u32 m0, s93, 0\n"
"v_mfma_f32_16x16x32_bf16 v[88:91], v[144:147], a[76:79], v[88:91]\n"
"buffer_load_dwordx4 v189, s[48:51], s82 offen offset:0 lds\n"
"v_mfma_f32_16x16x32_bf16 v[88:91], v[148:151], a[92:95], v[88:91]\n"
"s_waitcnt lgkmcnt(4)\n"
"v_mfma_f32_16x16x32_bf16 v[68:71], v[128:131], a[64:67], v[68:71]\n"
"ds_read_b64_tr_b16 v[144:145], v187 offset:43008\n"
"v_mfma_f32_16x16x32_bf16 v[68:71], v[132:135], a[80:83], v[68:71]\n"
"ds_read_b64_tr_b16 v[146:147], v187 offset:61440\n"
"v_mfma_f32_16x16x32_bf16 v[76:79], v[128:131], a[68:71], v[76:79]\n"
"ds_read_b64_tr_b16 v[148:149], v187 offset:44032\n"
"buffer_load_dwordx4 v189, s[48:51], s83 offen offset:1024 lds\n"
"v_mfma_f32_16x16x32_bf16 v[76:79], v[132:135], a[84:87], v[76:79]\n"
"ds_read_b64_tr_b16 v[150:151], v187 offset:62464\n"
"v_mfma_f32_16x16x32_bf16 v[84:87], v[128:131], a[72:75], v[84:87]\n"
"v_mfma_f32_16x16x32_bf16 v[84:87], v[132:135], a[88:91], v[84:87]\n"
"v_mfma_f32_16x16x32_bf16 v[92:95], v[128:131], a[76:79], v[92:95]\n"
"buffer_load_dwordx4 v189, s[48:51], s84 offen offset:2048 lds\n"
"v_mfma_f32_16x16x32_bf16 v[92:95], v[132:135], a[92:95], v[92:95]\n"
"s_waitcnt lgkmcnt(4)\n"
"v_mfma_f32_16x16x32_bf16 v[64:67], v[136:139], a[96:99], v[64:67]\n"
"v_mfma_f32_16x16x32_bf16 v[64:67], v[140:143], a[112:115], v[64:67]\n"
"v_mfma_f32_16x16x32_bf16 v[72:75], v[136:139], a[100:103], v[72:75]\n"
"buffer_load_dwordx4 v189, s[48:51], s85 offen offset:3072 lds\n"
"v_mfma_f32_16x16x32_bf16 v[72:75], v[140:143], a[116:119], v[72:75]\n"
"s_waitcnt lgkmcnt(0)\n"
"s_waitcnt vmcnt(23)\n"
"s_barrier\n"
"v_mfma_f32_16x16x32_bf16 v[80:83], v[136:139], a[104:107], v[80:83]\n"
"ds_read_b64_tr_b16 v[128:129], v186 offset:8192\n"
"v_mfma_f32_16x16x32_bf16 v[80:83], v[140:143], a[120:123], v[80:83]\n"
"ds_read_b64_tr_b16 v[130:131], v186 offset:26624\n"
"s_add_u32 m0, s93, 0x1000\n"
"v_mfma_f32_16x16x32_bf16 v[88:91], v[136:139], a[108:111], v[88:91]\n"
"buffer_load_dwordx4 v189, s[48:51], s86 offen offset:0 lds\n"
"v_mfma_f32_16x16x32_bf16 v[88:91], v[140:143], a[124:127], v[88:91]\n"
"v_mfma_f32_16x16x32_bf16 v[68:71], v[144:147], a[96:99], v[68:71]\n"
"ds_read_b64_tr_b16 v[136:137], v188 offset:8192\n"
"v_mfma_f32_16x16x32_bf16 v[68:71], v[148:151], a[112:115], v[68:71]\n"
"ds_read_b64_tr_b16 v[138:139], v188 offset:26624\n"
"v_mfma_f32_16x16x32_bf16 v[76:79], v[144:147], a[100:103], v[76:79]\n"
"s_waitcnt vmcnt(10)\n"
"v_cmp_eq_u64 vcc, s[12:13], v[206:207]\n"
"s_nop 3\n"
"s_cmp_eq_u64 vcc, -1\n"
"s_cbranch_scc1 Lok1_%=\n"
"s_mov_b32 s16, 0x8000\n"
"Lspin1_%=:\n"
"s_sleep 1\n"
"buffer_load_dwordx2 v[206:207], v200, s[8:11], 0 offen offset:0 sc1\n"
"s_waitcnt vmcnt(0)\n"
"v_cmp_eq_u64 vcc, s[12:13], v[206:207]\n"
"s_sub_u32 s16, s16, 1\n"
"s_cmp_eq_u32 s16, 0\n"
"s_cbranch_scc1 Lok1_%=\n"
"s_cmp_eq_u64 vcc, -1\n"
"s_cbranch_scc0 Lspin1_%=\n"
"Lok1_%=:\n"
"s_mov_b64 exec, 1\n"
"s_nop 0\n"
"buffer_store_dwordx2 v[198:199], v204, s[8:11], 0 offen offset:0 sc1\n"
"s_mov_b64 exec, -1\n"
"s_nop 0\n"
"v_mfma_f32_16x16x32_bf16 v[76:79], v[148:151], a[116:119], v[76:79]\n"
"buffer_load_dwordx4 v189, s[48:51], s87 offen offset:1024 lds\n"
"v_mfma_f32_16x16x32_bf16 v[84:87], v[144:147], a[104:107], v[84:87]\n"
"v_mfma_f32_16x16x32_bf16 v[84:87], v[148:151], a[120:123], v[84:87]\n"
"v_mfma_f32_16x16x32_bf16 v[92:95], v[144:147], a[108:111], v[92:95]\n"
"v_mfma_f32_16x16x32_bf16 v[92:95], v[148:151], a[124:127], v[92:95]\n"
"buffer_load_dwordx4 v189, s[48:51], s88 offen offset:2048 lds\n"
"s_waitcnt lgkmcnt(2)\n"
"v_mfma_f32_16x16x32_bf16 v[0:3], v[128:131], a[128:131], 0\n"
"ds_read_b64_tr_b16 v[144:145], v186 offset:0\n"
"v_mfma_f32_16x16x32_bf16 v[8:11], v[128:131], a[132:135], 0\n"
"ds_read_b64_tr_b16 v[146:147], v186 offset:18432\n"
"v_mfma_f32_16x16x32_bf16 v[16:19], v[128:131], a[136:139], 0\n"
"ds_read_b64_tr_b16 v[148:149], v186 offset:1024\n"
"v_cvt_pk_bf16_f32 v64, v64, v65\n"
"v_mfma_f32_16x16x32_bf16 v[24:27], v[128:131], a[140:143], 0\n"
"ds_read_b64_tr_b16 v[150:151], v186 offset:19456\n"
"v_cvt_pk_bf16_f32 v65, v66, v67\n"
"buffer_load_dwordx4 v189, s[48:51], s89 offen offset:3072 lds\n"
"s_waitcnt lgkmcnt(4)\n"
"v_mfma_f32_16x16x32_bf16 v[4:7], v[136:139], a[128:131], 0\n"
"ds_read_b64_tr_b16 v[128:129], v188 offset:0\n"
"v_cvt_pk_bf16_f32 v66, v68, v69\n"
"v_mfma_f32_16x16x32_bf16 v[12:15], v[136:139], a[132:135], 0\n"
"ds_read_b64_tr_b16 v[130:131], v188 offset:18432\n"
"v_cvt_pk_bf16_f32 v67, v70, v71\n"
"v_mfma_f32_16x16x32_bf16 v[20:23], v[136:139], a[136:139], 0\n"
"ds_read_b64_tr_b16 v[132:133], v188 offset:1024\n"
"v_cvt_pk_bf16_f32 v72, v72, v73\n"
"s_add_u32 m0, s93, 0x2000\n"
"v_mfma_f32_16x16x32_bf16 v[28:31], v[136:139], a[140:143], 0\n"
"ds_read_b64_tr_b16 v[134:135], v188 offset:19456\n"
"v_cvt_pk_bf16_f32 v73, v74, v75\n"
"buffer_load_dwordx4 v189, s[48:51], s90 offen offset:0 lds\n"
"s_waitcnt lgkmcnt(4)\n"
"v_mfma_f32_16x16x32_bf16 v[0:3], v[144:147], a[0:3], v[0:3]\n"
"ds_read_b64_tr_b16 v[136:137], v186 offset:2048\n"
"v_cvt_pk_bf16_f32 v74, v76, v77\n"
"v_mfma_f32_16x16x32_bf16 v[0:3], v[148:151], a[16:19], v[0:3]\n"
"ds_read_b64_tr_b16 v[138:139], v186 offset:20480\n"
"v_cvt_pk_bf16_f32 v75, v78, v79\n"
"v_mfma_f32_16x16x32_bf16 v[8:11], v[144:147], a[4:7], v[8:11]\n"
"ds_read_b64_tr_b16 v[140:141], v186 offset:3072\n"
"v_cvt_pk_bf16_f32 v80, v80, v81\n"
"s_mov_b32 s95, 0x14000\n"
"v_mfma_f32_16x16x32_bf16 v[8:11], v[148:151], a[20:23], v[8:11]\n"
"ds_read_b64_tr_b16 v[142:143], v186 offset:21504\n"
"v_cvt_pk_bf16_f32 v81, v82, v83\n"
"buffer_store_dwordx4 v[64:67], v192, s[52:55], s95 offen offset:0 sc1\n"
"v_mfma_f32_16x16x32_bf16 v[16:19], v[144:147], a[8:11], v[16:19]\n"
"v_cvt_pk_bf16_f32 v82, v84, v85\n"
"v_cvt_pk_bf16_f32 v83, v86, v87\n"
"v_mfma_f32_16x16x32_bf16 v[16:19], v[148:151], a[24:27], v[16:19]\n"
"v_cvt_pk_bf16_f32 v88, v88, v89\n"
"v_cvt_pk_bf16_f32 v89, v90, v91\n"
"v_mfma_f32_16x16x32_bf16 v[24:27], v[144:147], a[12:15], v[24:27]\n"
"v_cvt_pk_bf16_f32 v90, v92, v93\n"
"v_cvt_pk_bf16_f32 v91, v94, v95\n"
"s_mov_b32 s95, 0x14000\n"
"v_mfma_f32_16x16x32_bf16 v[24:27], v[148:151], a[28:31], v[24:27]\n"
"buffer_store_dwordx4 v[72:75], v192, s[52:55], s95 offen offset:1024 sc1\n"
"s_waitcnt lgkmcnt(4)\n"
"v_mfma_f32_16x16x32_bf16 v[4:7], v[128:131], a[0:3], v[4:7]\n"
"ds_read_b64_tr_b16 v[144:145], v188 offset:2048\n"
"v_mfma_f32_16x16x32_bf16 v[4:7], v[132:135], a[16:19], v[4:7]\n"
"ds_read_b64_tr_b16 v[146:147], v188 offset:20480\n"
"v_mfma_f32_16x16x32_bf16 v[12:15], v[128:131], a[4:7], v[12:15]\n"
"ds_read_b64_tr_b16 v[148:149], v188 offset:3072\n"
"v_mfma_f32_16x16x32_bf16 v[12:15], v[132:135], a[20:23], v[12:15]\n"
"ds_read_b64_tr_b16 v[150:151], v188 offset:21504\n"
"s_mov_b32 s95, 0x14000\n"
"v_mfma_f32_16x16x32_bf16 v[20:23], v[128:131], a[8:11], v[20:23]\n"
"buffer_store_dwordx4 v[80:83], v192, s[52:55], s95 offen offset:2048 sc1\n"
"v_mfma_f32_16x16x32_bf16 v[20:23], v[132:135], a[24:27], v[20:23]\n"
"v_mfma_f32_16x16x32_bf16 v[28:31], v[128:131], a[12:15], v[28:31]\n"
"v_mfma_f32_16x16x32_bf16 v[28:31], v[132:135], a[28:31], v[28:31]\n"
"s_mov_b32 s95, 0x14000\n"
"s_waitcnt lgkmcnt(4)\n"
"v_mfma_f32_16x16x32_bf16 v[0:3], v[136:139], a[32:35], v[0:3]\n"
"ds_read_b64_tr_b16 v[128:129], v186 offset:4096\n"
"buffer_store_dwordx4 v[88:91], v192, s[52:55], s95 offen offset:3072 sc1\n"
"v_mfma_f32_16x16x32_bf16 v[0:3], v[140:143], a[48:51], v[0:3]\n"
"ds_read_b64_tr_b16 v[130:131], v186 offset:22528\n"
"v_mfma_f32_16x16x32_bf16 v[8:11], v[136:139], a[36:39], v[8:11]\n"
"ds_read_b64_tr_b16 v[132:133], v186 offset:5120\n"
"v_mfma_f32_16x16x32_bf16 v[8:11], v[140:143], a[52:55], v[8:11]\n"
"ds_read_b64_tr_b16 v[134:135], v186 offset:23552\n"
"s_mov_b32 s14, 0\n"
"v_mfma_f32_16x16x32_bf16 v[16:19], v[136:139], a[40:43], v[16:19]\n"
"buffer_load_dwordx4 v[152:155], v202, s[52:55], s14 offen offset:0 sc1\n"
"v_mfma_f32_16x16x32_bf16 v[16:19], v[140:143], a[56:59], v[16:19]\n"
"v_mfma_f32_16x16x32_bf16 v[24:27], v[136:139], a[44:47], v[24:27]\n"
"v_mfma_f32_16x16x32_bf16 v[24:27], v[140:143], a[60:63], v[24:27]\n"
"s_mov_b32 s15, 0x90000\n"
"s_waitcnt lgkmcnt(4)\n"
"v_mfma_f32_16x16x32_bf16 v[4:7], v[144:147], a[32:35], v[4:7]\n"
"ds_read_b64_tr_b16 v[136:137], v188 offset:4096\n"
"buffer_load_dwordx4 v[156:159], v202, s[52:55], s15 offen offset:0 sc1\n"
"v_mfma_f32_16x16x32_bf16 v[4:7], v[148:151], a[48:51], v[4:7]\n"
"ds_read_b64_tr_b16 v[138:139], v188 offset:22528\n"
"v_mfma_f32_16x16x32_bf16 v[12:15], v[144:147], a[36:39], v[12:15]\n"
"ds_read_b64_tr_b16 v[140:141], v188 offset:5120\n"
"v_mfma_f32_16x16x32_bf16 v[12:15], v[148:151], a[52:55], v[12:15]\n"
"ds_read_b64_tr_b16 v[142:143], v188 offset:23552\n"
"s_mov_b32 s14, 0x120000\n"
"v_mfma_f32_16x16x32_bf16 v[20:23], v[144:147], a[40:43], v[20:23]\n"
"buffer_load_dwordx4 v[160:163], v202, s[52:55], s14 offen offset:0 sc1\n"
"v_mfma_f32_16x16x32_bf16 v[20:23], v[148:151], a[56:59], v[20:23]\n"
"v_mfma_f32_16x16x32_bf16 v[28:31], v[144:147], a[44:47], v[28:31]\n"
"v_mfma_f32_16x16x32_bf16 v[28:31], v[148:151], a[60:63], v[28:31]\n"
"s_mov_b32 s15, 0x1b0000\n"
"s_waitcnt lgkmcnt(4)\n"
"v_mfma_f32_16x16x32_bf16 v[0:3], v[128:131], a[64:67], v[0:3]\n"
"ds_read_b64_tr_b16 v[144:145], v186 offset:6144\n"
"buffer_load_dwordx4 v[164:167], v202, s[52:55], s15 offen offset:0 sc1\n"
"v_mfma_f32_16x16x32_bf16 v[0:3], v[132:135], a[80:83], v[0:3]\n"
"ds_read_b64_tr_b16 v[146:147], v186 offset:24576\n"
"v_mfma_f32_16x16x32_bf16 v[8:11], v[128:131], a[68:71], v[8:11]\n"
"ds_read_b64_tr_b16 v[148:149], v186 offset:7168\n"
"v_mfma_f32_16x16x32_bf16 v[8:11], v[132:135], a[84:87], v[8:11]\n"
"ds_read_b64_tr_b16 v[150:151], v186 offset:25600\n"
"s_mov_b32 s14, 0x4000\n"
"v_mfma_f32_16x16x32_bf16 v[16:19], v[128:131], a[72:75], v[16:19]\n"
"buffer_load_dwordx4 v[224:227], v202, s[52:55], s14 offen offset:0 sc1\n"
"v_mfma_f32_16x16x32_bf16 v[16:19], v[132:135], a[88:91], v[16:19]\n"
"v_mfma_f32_16x16x32_bf16 v[24:27], v[128:131], a[76:79], v[24:27]\n"
"v_mfma_f32_16x16x32_bf16 v[24:27], v[132:135], a[92:95], v[24:27]\n"
"s_mov_b32 s15, 0x94000\n"
"s_waitcnt lgkmcnt(4)\n"
"v_mfma_f32_16x16x32_bf16 v[4:7], v[136:139], a[64:67], v[4:7]\n"
"ds_read_b64_tr_b16 v[128:129], v188 offset:6144\n"
"buffer_load_dwordx4 v[228:231], v202, s[52:55], s15 offen offset:0 sc1\n"
"v_mfma_f32_16x16x32_bf16 v[4:7], v[140:143], a[80:83], v[4:7]\n"
"ds_read_b64_tr_b16 v[130:131], v188 offset:24576\n"
"v_mfma_f32_16x16x32_bf16 v[12:15], v[136:139], a[68:71], v[12:15]\n"
"ds_read_b64_tr_b16 v[132:133], v188 offset:7168\n"
"v_mfma_f32_16x16x32_bf16 v[12:15], v[140:143], a[84:87], v[12:15]\n"
"ds_read_b64_tr_b16 v[134:135], v188 offset:25600\n"
"s_mov_b32 s14, 0x124000\n"
"v_mfma_f32_16x16x32_bf16 v[20:23], v[136:139], a[72:75], v[20:23]\n"
"buffer_load_dwordx4 v[232:235], v202, s[52:55], s14 offen offset:0 sc1\n"
"v_mfma_f32_16x16x32_bf16 v[20:23], v[140:143], a[88:91], v[20:23]\n"
"v_mfma_f32_16x16x32_bf16 v[28:31], v[136:139], a[76:79], v[28:31]\n"
"v_mfma_f32_16x16x32_bf16 v[28:31], v[140:143], a[92:95], v[28:31]\n"
"s_mov_b32 s15, 0x1b4000\n"
"s_waitcnt lgkmcnt(4)\n"
"v_mfma_f32_16x16x32_bf16 v[0:3], v[144:147], a[96:99], v[0:3]\n"
"buffer_load_dwordx4 v[236:239], v202, s[52:55], s15 offen offset:0 sc1\n"
"v_mfma_f32_16x16x32_bf16 v[0:3], v[148:151], a[112:115], v[0:3]\n"
"v_mfma_f32_16x16x32_bf16 v[8:11], v[144:147], a[100:103], v[8:11]\n"
"v_mfma_f32_16x16x32_bf16 v[8:11], v[148:151], a[116:119], v[8:11]\n"
"s_waitcnt lgkmcnt(0)\n"
"s_waitcnt vmcnt(26)\n"
"s_barrier\n"
"s_mov_b32 s14, 0x8000\n"
"v_mfma_f32_16x16x32_bf16 v[16:19], v[144:147], a[104:107], v[16:19]\n"
"ds_read_b64_tr_b16 v[136:137], v186 offset:45056\n"
"buffer_load_dwordx4 v[240:243], v202, s[52:55], s14 offen offset:0 sc1\n"
"v_mfma_f32_16x16x32_bf16 v[16:19], v[148:151], a[120:123], v[16:19]\n"
"ds_read_b64_tr_b16 v[138:139], v186 offset:63488\n"
"v_mfma_f32_16x16x32_bf16 v[24:27], v[144:147], a[108:111], v[24:27]\n"
"v_mfma_f32_16x16x32_bf16 v[24:27], v[148:151], a[124:127], v[24:27]\n"
"s_mov_b32 s15, 0x98000\n"
"v_mfma_f32_16x16x32_bf16 v[4:7], v[128:131], a[96:99], v[4:7]\n"
"ds_read_b64_tr_b16 v[144:145], v188 offset:45056\n"
"buffer_load_dwordx4 v[244:247], v202, s[52:55], s15 offen offset:0 sc1\n"
"v_mfma_f32_16x16x32_bf16 v[4:7], v[132:135], a[112:115], v[4:7]\n"
"ds_read_b64_tr_b16 v[146:147], v188 offset:63488\n"
"v_mfma_f32_16x16x32_bf16 v[12:15], v[128:131], a[100:103], v[12:15]\n"
"v_mfma_f32_16x16x32_bf16 v[12:15], v[132:135], a[116:119], v[12:15]\n"
"s_mov_b32 s14, 0x128000\n"
"v_mfma_f32_16x16x32_bf16 v[20:23], v[128:131], a[104:107], v[20:23]\n"
"buffer_load_dwordx4 v[248:251], v202, s[52:55], s14 offen offset:0 sc1\n"
"v_mfma_f32_16x16x32_bf16 v[20:23], v[132:135], a[120:123], v[20:23]\n"
"v_mfma_f32_16x16x32_bf16 v[28:31], v[128:131], a[108:111], v[28:31]\n"
"v_mfma_f32_16x16x32_bf16 v[28:31], v[132:135], a[124:127], v[28:31]\n"
"s_mov_b32 s15, 0x1b8000\n"
"s_waitcnt lgkmcnt(2)\n"
"v_mfma_f32_16x16x32_bf16 v[32:35], v[136:139], a[128:131], 0\n"
"ds_read_b64_tr_b16 v[128:129], v186 offset:36864\n"
"buffer_load_dwordx4 v[252:255], v202, s[52:55], s15 offen offset:0 sc1\n"
"v_mfma_f32_16x16x32_bf16 v[40:43], v[136:139], a[132:135], 0\n"
"ds_read_b64_tr_b16 v[130:131], v186 offset:55296\n"
"v_mfma_f32_16x16x32_bf16 v[48:51], v[136:139], a[136:139], 0\n"
"ds_read_b64_tr_b16 v[132:133], v186 offset:37888\n"
"v_cvt_pk_bf16_f32 v0, v0, v1\n"
"v_mfma_f32_16x16x32_bf16 v[56:59], v[136:139], a[140:143], 0\n"
"ds_read_b64_tr_b16 v[134:135], v186 offset:56320\n"
"v_cvt_pk_bf16_f32 v1, v2, v3\n"
"s_waitcnt lgkmcnt(4)\n"
"v_mfma_f32_16x16x32_bf16 v[36:39], v[144:147], a[128:131], 0\n"
"ds_read_b64_tr_b16 v[136:137], v188 offset:36864\n"
"v_cvt_pk_bf16_f32 v2, v4, v5\n"
"s_waitcnt vmcnt(12)\n"
"s_mov_b64 exec, 1\n"
"s_nop 0\n"
"buffer_store_dwordx2 v[198:199], v201, s[8:11], 0 offen offset:512 sc1\n"
"s_mov_b64 exec, -1\n"
"s_nop 0\n"
"v_mfma_f32_16x16x32_bf16 v[44:47], v[144:147], a[132:135], 0\n"
"ds_read_b64_tr_b16 v[138:139], v188 offset:55296\n"
"v_cvt_pk_bf16_f32 v3, v6, v7\n"
"s_mov_b32 s95, 0x18000\n"
"v_mfma_f32_16x16x32_bf16 v[52:55], v[144:147], a[136:139], 0\n"
"ds_read_b64_tr_b16 v[140:141], v188 offset:37888\n"
"v_cvt_pk_bf16_f32 v8, v8, v9\n"
"buffer_store_dwordx4 v[0:3], v192, s[52:55], s95 offen offset:0 sc1\n"
"v_mfma_f32_16x16x32_bf16 v[60:63], v[144:147], a[140:143], 0\n"
"ds_read_b64_tr_b16 v[142:143], v188 offset:56320\n"
"v_cvt_pk_bf16_f32 v9, v10, v11\n"
"s_waitcnt lgkmcnt(4)\n"
"v_mfma_f32_16x16x32_bf16 v[32:35], v[128:131], a[0:3], v[32:35]\n"
"ds_read_b64_tr_b16 v[144:145], v186 offset:38912\n"
"v_cvt_pk_bf16_f32 v10, v12, v13\n"
"v_mfma_f32_16x16x32_bf16 v[32:35], v[132:135], a[16:19], v[32:35]\n"
"ds_read_b64_tr_b16 v[146:147], v186 offset:57344\n"
"v_cvt_pk_bf16_f32 v11, v14, v15\n"
"s_mov_b32 s95, 0x18000\n"
"v_mfma_f32_16x16x32_bf16 v[40:43], v[128:131], a[4:7], v[40:43]\n"
"ds_read_b64_tr_b16 v[148:149], v186 offset:39936\n"
"v_cvt_pk_bf16_f32 v16, v16, v17\n"
"buffer_store_dwordx4 v[8:11], v192, s[52:55], s95 offen offset:1024 sc1\n"
"v_mfma_f32_16x16x32_bf16 v[40:43], v[132:135], a[20:23], v[40:43]\n"
"ds_read_b64_tr_b16 v[150:151], v186 offset:58368\n"
"v_cvt_pk_bf16_f32 v17, v18, v19\n"
"v_mfma_f32_16x16x32_bf16 v[48:51], v[128:131], a[8:11], v[48:51]\n"
"v_cvt_pk_bf16_f32 v18, v20, v21\n"
"v_cvt_pk_bf16_f32 v19, v22, v23\n"
"v_mfma_f32_16x16x32_bf16 v[48:51], v[132:135], a[24:27], v[48:51]\n"
"v_cvt_pk_bf16_f32 v24, v24, v25\n"
"v_cvt_pk_bf16_f32 v25, v26, v27\n"
"v_mfma_f32_16x16x32_bf16 v[56:59], v[128:131], a[12:15], v[56:59]\n"
"v_cvt_pk_bf16_f32 v26, v28, v29\n"
"v_cvt_pk_bf16_f32 v27, v30, v31\n"
"s_mov_b32 s95, 0x18000\n"
"v_mfma_f32_16x16x32_bf16 v[56:59], v[132:135], a[28:31], v[56:59]\n"
"buffer_store_dwordx4 v[16:19], v192, s[52:55], s95 offen offset:2048 sc1\n"
"s_waitcnt lgkmcnt(4)\n"
"v_mfma_f32_16x16x32_bf16 v[36:39], v[136:139], a[0:3], v[36:39]\n"
"ds_read_b64_tr_b16 v[128:129], v188 offset:38912\n"
"v_mfma_f32_16x16x32_bf16 v[36:39], v[140:143], a[16:19], v[36:39]\n"
"ds_read_b64_tr_b16 v[130:131], v188 offset:57344\n"
"v_mfma_f32_16x16x32_bf16 v[44:47], v[136:139], a[4:7], v[44:47]\n"
"ds_read_b64_tr_b16 v[132:133], v188 offset:39936\n"
"v_mfma_f32_16x16x32_bf16 v[44:47], v[140:143], a[20:23], v[44:47]\n"
"ds_read_b64_tr_b16 v[134:135], v188 offset:58368\n"
"s_mov_b32 s95, 0x18000\n"
"v_mfma_f32_16x16x32_bf16 v[52:55], v[136:139], a[8:11], v[52:55]\n"
"buffer_store_dwordx4 v[24:27], v192, s[52:55], s95 offen offset:3072 sc1\n"
"v_mfma_f32_16x16x32_bf16 v[52:55], v[140:143], a[24:27], v[52:55]\n"
"v_mfma_f32_16x16x32_bf16 v[60:63], v[136:139], a[12:15], v[60:63]\n"
"v_mfma_f32_16x16x32_bf16 v[60:63], v[140:143], a[28:31], v[60:63]\n"
"s_waitcnt lgkmcnt(4)\n"
"v_mfma_f32_16x16x32_bf16 v[32:35], v[144:147], a[32:35], v[32:35]\n"
"ds_read_b64_tr_b16 v[136:137], v186 offset:40960\n"
"v_mfma_f32_16x16x32_bf16 v[32:35], v[148:151], a[48:51], v[32:35]\n"
"ds_read_b64_tr_b16 v[138:139], v186 offset:59392\n"
"v_mfma_f32_16x16x32_bf16 v[40:43], v[144:147], a[36:39], v[40:43]\n"
"ds_read_b64_tr_b16 v[140:141], v186 offset:41984\n"
"v_mfma_f32_16x16x32_bf16 v[40:43], v[148:151], a[52:55], v[40:43]\n"
"ds_read_b64_tr_b16 v[142:143], v186 offset:60416\n"
"v_mfma_f32_16x16x32_bf16 v[48:51], v[144:147], a[40:43], v[48:51]\n"
"v_mfma_f32_16x16x32_bf16 v[48:51], v[148:151], a[56:59], v[48:51]\n"
"v_mfma_f32_16x16x32_bf16 v[56:59], v[144:147], a[44:47], v[56:59]\n"
"v_mfma_f32_16x16x32_bf16 v[56:59], v[148:151], a[60:63], v[56:59]\n"
"s_waitcnt lgkmcnt(4)\n"
"v_mfma_f32_16x16x32_bf16 v[36:39], v[128:131], a[32:35], v[36:39]\n"
"ds_read_b64_tr_b16 v[144:145], v188 offset:40960\n"
"v_mfma_f32_16x16x32_bf16 v[36:39], v[132:135], a[48:51], v[36:39]\n"
"ds_read_b64_tr_b16 v[146:147], v188 offset:59392\n"
"v_mfma_f32_16x16x32_bf16 v[44:47], v[128:131], a[36:39], v[44:47]\n"
"ds_read_b64_tr_b16 v[148:149], v188 offset:41984\n"
"v_mfma_f32_16x16x32_bf16 v[44:47], v[132:135], a[52:55], v[44:47]\n"
"ds_read_b64_tr_b16 v[150:151], v188 offset:60416\n"
"v_mfma_f32_16x16x32_bf16 v[52:55], v[128:131], a[40:43], v[52:55]\n"
"v_mfma_f32_16x16x32_bf16 v[52:55], v[132:135], a[56:59], v[52:55]\n"
"v_mfma_f32_16x16x32_bf16 v[60:63], v[128:131], a[44:47], v[60:63]\n"
"v_mfma_f32_16x16x32_bf16 v[60:63], v[132:135], a[60:63], v[60:63]\n"
"s_waitcnt lgkmcnt(4)\n"
"v_mfma_f32_16x16x32_bf16 v[32:35], v[136:139], a[64:67], v[32:35]\n"
"ds_read_b64_tr_b16 v[128:129], v186 offset:43008\n"
"v_mfma_f32_16x16x32_bf16 v[32:35], v[140:143], a[80:83], v[32:35]\n"
"ds_read_b64_tr_b16 v[130:131], v186 offset:61440\n"
"v_mfma_f32_16x16x32_bf16 v[40:43], v[136:139], a[68:71], v[40:43]\n"
"ds_read_b64_tr_b16 v[132:133], v186 offset:44032\n"
"v_mfma_f32_16x16x32_bf16 v[40:43], v[140:143], a[84:87], v[40:43]\n"
"ds_read_b64_tr_b16 v[134:135], v186 offset:62464\n"
"v_mfma_f32_16x16x32_bf16 v[48:51], v[136:139], a[72:75], v[48:51]\n"
"buffer_load_dwordx2 v[208:209], v200, s[8:11], 0 offen offset:512 sc1\n"
"v_mfma_f32_16x16x32_bf16 v[48:51], v[140:143], a[88:91], v[48:51]\n"
"v_mfma_f32_16x16x32_bf16 v[56:59], v[136:139], a[76:79], v[56:59]\n"
"v_mfma_f32_16x16x32_bf16 v[56:59], v[140:143], a[92:95], v[56:59]\n"
"s_waitcnt lgkmcnt(4)\n"
"v_mfma_f32_16x16x32_bf16 v[36:39], v[144:147], a[64:67], v[36:39]\n"
"ds_read_b64_tr_b16 v[136:137], v188 offset:43008\n"
"v_mfma_f32_16x16x32_bf16 v[36:39], v[148:151], a[80:83], v[36:39]\n"
"ds_read_b64_tr_b16 v[138:139], v188 offset:61440\n"
"v_mfma_f32_16x16x32_bf16 v[44:47], v[144:147], a[68:71], v[44:47]\n"
"ds_read_b64_tr_b16 v[140:141], v188 offset:44032\n"
"v_mfma_f32_16x16x32_bf16 v[44:47], v[148:151], a[84:87], v[44:47]\n"
"ds_read_b64_tr_b16 v[142:143], v188 offset:62464\n"
"v_mfma_f32_16x16x32_bf16 v[52:55], v[144:147], a[72:75], v[52:55]\n"
"v_mfma_f32_16x16x32_bf16 v[52:55], v[148:151], a[88:91], v[52:55]\n"
"v_mfma_f32_16x16x32_bf16 v[60:63], v[144:147], a[76:79], v[60:63]\n"
"v_mfma_f32_16x16x32_bf16 v[60:63], v[148:151], a[92:95], v[60:63]\n"
"s_waitcnt lgkmcnt(4)\n"
"v_mfma_f32_16x16x32_bf16 v[32:35], v[128:131], a[96:99], v[32:35]\n"
"v_mfma_f32_16x16x32_bf16 v[32:35], v[132:135], a[112:115], v[32:35]\n"
"v_mfma_f32_16x16x32_bf16 v[40:43], v[128:131], a[100:103], v[40:43]\n"
"v_mfma_f32_16x16x32_bf16 v[40:43], v[132:135], a[116:119], v[40:43]\n"
"s_waitcnt lgkmcnt(0)\n"
"s_barrier\n"
"v_mfma_f32_16x16x32_bf16 v[48:51], v[128:131], a[104:107], v[48:51]\n"
"ds_read_b64_tr_b16 v[144:145], v185 offset:8192\n"
"v_mfma_f32_16x16x32_bf16 v[48:51], v[132:135], a[120:123], v[48:51]\n"
"ds_read_b64_tr_b16 v[146:147], v185 offset:26624\n"
"v_mfma_f32_16x16x32_bf16 v[56:59], v[128:131], a[108:111], v[56:59]\n"
"v_mfma_f32_16x16x32_bf16 v[56:59], v[132:135], a[124:127], v[56:59]\n"
"v_mfma_f32_16x16x32_bf16 v[36:39], v[136:139], a[96:99], v[36:39]\n"
"ds_read_b64_tr_b16 v[128:129], v187 offset:8192\n"
"v_mfma_f32_16x16x32_bf16 v[36:39], v[140:143], a[112:115], v[36:39]\n"
"ds_read_b64_tr_b16 v[130:131], v187 offset:26624\n"
"v_mfma_f32_16x16x32_bf16 v[44:47], v[136:139], a[100:103], v[44:47]\n"
"v_mfma_f32_16x16x32_bf16 v[44:47], v[140:143], a[116:119], v[44:47]\n"
"v_mfma_f32_16x16x32_bf16 v[52:55], v[136:139], a[104:107], v[52:55]\n"
"v_mfma_f32_16x16x32_bf16 v[52:55], v[140:143], a[120:123], v[52:55]\n"
"v_mfma_f32_16x16x32_bf16 v[60:63], v[136:139], a[108:111], v[60:63]\n"
"v_mfma_f32_16x16x32_bf16 v[60:63], v[140:143], a[124:127], v[60:63]\n"
"s_waitcnt lgkmcnt(2)\n"
"v_mfma_f32_16x16x32_bf16 v[64:67], v[144:147], a[128:131], 0\n"
"ds_read_b64_tr_b16 v[136:137], v185 offset:0\n"
"v_mfma_f32_16x16x32_bf16 v[72:75], v[144:147], a[132:135], 0\n"
"ds_read_b64_tr_b16 v[138:139], v185 offset:18432\n"
"v_mfma_f32_16x16x32_bf16 v[80:83], v[144:147], a[136:139], 0\n"
"ds_read_b64_tr_b16 v[140:141], v185 offset:1024\n"
"v_cvt_pk_bf16_f32 v32, v32, v33\n"
"v_mfma_f32_16x16x32_bf16 v[88:91], v[144:147], a[140:143], 0\n"
"ds_read_b64_tr_b16 v[142:143], v185 offset:19456\n"
"v_cvt_pk_bf16_f32 v33, v34, v35\n"
"s_waitcnt lgkmcnt(4)\n"
"v_mfma_f32_16x16x32_bf16 v[68:71], v[128:131], a[128:131], 0\n"
"ds_read_b64_tr_b16 v[144:145], v187 offset:0\n"
"v_cvt_pk_bf16_f32 v34, v36, v37\n"
"v_mfma_f32_16x16x32_bf16 v[76:79], v[128:131], a[132:135], 0\n"
"ds_read_b64_tr_b16 v[146:147], v187 offset:18432\n"
"v_cvt_pk_bf16_f32 v35, v38, v39\n"
"s_mov_b32 s95, 0x1c000\n"
"v_mfma_f32_16x16x32_bf16 v[84:87], v[128:131], a[136:139], 0\n"
"ds_read_b64_tr_b16 v[148:149], v187 offset:1024\n"
"v_cvt_pk_bf16_f32 v40, v40, v41\n"
"buffer_store_dwordx4 v[32:35], v192, s[52:55], s95 offen offset:0 sc1\n"
"v_mfma_f32_16x16x32_bf16 v[92:95], v[128:131], a[140:143], 0\n"
"ds_read_b64_tr_b16 v[150:151], v187 offset:19456\n"
"v_cvt_pk_bf16_f32 v41, v42, v43\n"
"s_waitcnt lgkmcnt(4)\n"
"v_mfma_f32_16x16x32_bf16 v[64:67], v[136:139], a[0:3], v[64:67]\n"
"ds_read_b64_tr_b16 v[128:129], v185 offset:2048\n"
"v_cvt_pk_bf16_f32 v42, v44, v45\n"
"v_mfma_f32_16x16x32_bf16 v[64:67], v[140:143], a[16:19], v[64:67]\n"
"ds_read_b64_tr_b16 v[130:131], v185 offset:20480\n"
"v_cvt_pk_bf16_f32 v43, v46, v47\n"
"s_mov_b32 s95, 0x1c000\n"
"v_mfma_f32_16x16x32_bf16 v[72:75], v[136:139], a[4:7], v[72:75]\n"
"ds_read_b64_tr_b16 v[132:133], v185 offset:3072\n"
"v_cvt_pk_bf16_f32 v48, v48, v49\n"
"buffer_store_dwordx4 v[40:43], v192, s[52:55], s95 offen offset:1024 sc1\n"
"v_mfma_f32_16x16x32_bf16 v[72:75], v[140:143], a[20:23], v[72:75]\n"
"ds_read_b64_tr_b16 v[134:135], v185 offset:21504\n"
"v_cvt_pk_bf16_f32 v49, v50, v51\n"
"v_mfma_f32_16x16x32_bf16 v[80:83], v[136:139], a[8:11], v[80:83]\n"
"v_cvt_pk_bf16_f32 v50, v52, v53\n"
"v_cvt_pk_bf16_f32 v51, v54, v55\n"
"v_mfma_f32_16x16x32_bf16 v[80:83], v[140:143], a[24:27], v[80:83]\n"
"v_cvt_pk_bf16_f32 v56, v56, v57\n"
"v_cvt_pk_bf16_f32 v57, v58, v59\n"
"v_mfma_f32_16x16x32_bf16 v[88:91], v[136:139], a[12:15], v[88:91]\n"
"v_cvt_pk_bf16_f32 v58, v60, v61\n"
"v_cvt_pk_bf16_f32 v59, v62, v63\n"
"s_waitcnt vmcnt(2)\n"
"v_cmp_eq_u64 vcc, s[12:13], v[208:209]\n"
"s_nop 3\n"
"s_cmp_eq_u64 vcc, -1\n"
"s_cbranch_scc1 Lok2_%=\n"
"s_mov_b32 s16, 0x8000\n"
"Lspin2_%=:\n"
"s_sleep 1\n"
"buffer_load_dwordx2 v[208:209], v200, s[8:11], 0 offen offset:512 sc1\n"
"s_waitcnt vmcnt(0)\n"
"v_cmp_eq_u64 vcc, s[12:13], v[208:209]\n"
"s_sub_u32 s16, s16, 1\n"
"s_cmp_eq_u32 s16, 0\n"
"s_cbranch_scc1 Lok2_%=\n"
"s_cmp_eq_u64 vcc, -1\n"
"s_cbranch_scc0 Lspin2_%=\n"
"Lok2_%=:\n"
"s_mov_b32 s95, 0x1c000\n"
"v_mfma_f32_16x16x32_bf16 v[88:91], v[140:143], a[28:31], v[88:91]\n"
"buffer_store_dwordx4 v[48:51], v192, s[52:55], s95 offen offset:2048 sc1\n"
"s_waitcnt lgkmcnt(4)\n"
"v_mfma_f32_16x16x32_bf16 v[68:71], v[144:147], a[0:3], v[68:71]\n"
"ds_read_b64_tr_b16 v[136:137], v187 offset:2048\n"
"v_mfma_f32_16x16x32_bf16 v[68:71], v[148:151], a[16:19], v[68:71]\n"
"ds_read_b64_tr_b16 v[138:139], v187 offset:20480\n"
"v_mfma_f32_16x16x32_bf16 v[76:79], v[144:147], a[4:7], v[76:79]\n"
"ds_read_b64_tr_b16 v[140:141], v187 offset:3072\n"
"v_mfma_f32_16x16x32_bf16 v[76:79], v[148:151], a[20:23], v[76:79]\n"
"ds_read_b64_tr_b16 v[142:143], v187 offset:21504\n"
"s_mov_b32 s95, 0x1c000\n"
"v_mfma_f32_16x16x32_bf16 v[84:87], v[144:147], a[8:11], v[84:87]\n"
"buffer_store_dwordx4 v[56:59], v192, s[52:55], s95 offen offset:3072 sc1\n"
"v_mfma_f32_16x16x32_bf16 v[84:87], v[148:151], a[24:27], v[84:87]\n"
"v_mfma_f32_16x16x32_bf16 v[92:95], v[144:147], a[12:15], v[92:95]\n"
"v_mfma_f32_16x16x32_bf16 v[92:95], v[148:151], a[28:31], v[92:95]\n"
"s_mov_b32 s14, 0xc000\n"
"s_waitcnt lgkmcnt(4)\n"
"v_mfma_f32_16x16x32_bf16 v[64:67], v[128:131], a[32:35], v[64:67]\n"
"ds_read_b64_tr_b16 v[144:145], v185 offset:4096\n"
"buffer_load_dwordx4 v[96:99], v202, s[52:55], s14 offen offset:0 sc1\n"
"v_mfma_f32_16x16x32_bf16 v[64:67], v[132:135], a[48:51], v[64:67]\n"
"ds_read_b64_tr_b16 v[146:147], v185 offset:22528\n"
"v_mfma_f32_16x16x32_bf16 v[72:75], v[128:131], a[36:39], v[72:75]\n"
"ds_read_b64_tr_b16 v[148:149], v185 offset:5120\n"
"v_mfma_f32_16x16x32_bf16 v[72:75], v[132:135], a[52:55], v[72:75]\n"
"ds_read_b64_tr_b16 v[150:151], v185 offset:23552\n"
"s_mov_b32 s15, 0x9c000\n"
"v_mfma_f32_16x16x32_bf16 v[80:83], v[128:131], a[40:43], v[80:83]\n"
"buffer_load_dwordx4 v[100:103], v202, s[52:55], s15 offen offset:0 sc1\n"
"v_mfma_f32_16x16x32_bf16 v[80:83], v[132:135], a[56:59], v[80:83]\n"
"v_mfma_f32_16x16x32_bf16 v[88:91], v[128:131], a[44:47], v[88:91]\n"
"v_mfma_f32_16x16x32_bf16 v[88:91], v[132:135], a[60:63], v[88:91]\n"
"s_mov_b32 s14, 0x12c000\n"
"s_waitcnt lgkmcnt(4)\n"
"v_mfma_f32_16x16x32_bf16 v[68:71], v[136:139], a[32:35], v[68:71]\n"
"ds_read_b64_tr_b16 v[128:129], v187 offset:4096\n"
"buffer_load_dwordx4 v[104:107], v202, s[52:55], s14 offen offset:0 sc1\n"
"v_mfma_f32_16x16x32_bf16 v[68:71], v[140:143], a[48:51], v[68:71]\n"
"ds_read_b64_tr_b16 v[130:131], v187 offset:22528\n"
"v_mfma_f32_16x16x32_bf16 v[76:79], v[136:139], a[36:39], v[76:79]\n"
"ds_read_b64_tr_b16 v[132:133], v187 offset:5120\n"
"v_mfma_f32_16x16x32_bf16 v[76:79], v[140:143], a[52:55], v[76:79]\n"
"ds_read_b64_tr_b16 v[134:135], v187 offset:23552\n"
"s_mov_b32 s15, 0x1bc000\n"
"v_mfma_f32_16x16x32_bf16 v[84:87], v[136:139], a[40:43], v[84:87]\n"
"buffer_load_dwordx4 v[108:111], v202, s[52:55], s15 offen offset:0 sc1\n"
"v_mfma_f32_16x16x32_bf16 v[84:87], v[140:143], a[56:59], v[84:87]\n"
"v_mfma_f32_16x16x32_bf16 v[92:95], v[136:139], a[44:47], v[92:95]\n"
"v_mfma_f32_16x16x32_bf16 v[92:95], v[140:143], a[60:63], v[92:95]\n"
"s_mov_b32 s14, 0x10000\n"
"s_waitcnt lgkmcnt(4)\n"
"v_mfma_f32_16x16x32_bf16 v[64:67], v[144:147], a[64:67], v[64:67]\n"
"ds_read_b64_tr_b16 v[136:137], v185 offset:6144\n"
"buffer_load_dwordx4 v[112:115], v202, s[52:55], s14 offen offset:0 sc1\n"
"v_mfma_f32_16x16x32_bf16 v[64:67], v[148:151], a[80:83], v[64:67]\n"
"ds_read_b64_tr_b16 v[138:139], v185 offset:24576\n"
"v_mfma_f32_16x16x32_bf16 v[72:75], v[144:147], a[68:71], v[72:75]\n"
"ds_read_b64_tr_b16 v[140:141], v185 offset:7168\n"
"v_mfma_f32_16x16x32_bf16 v[72:75], v[148:151], a[84:87], v[72:75]\n"
"ds_read_b64_tr_b16 v[142:143], v185 offset:25600\n"
"s_mov_b32 s15, 0xa0000\n"
"v_mfma_f32_16x16x32_bf16 v[80:83], v[144:147], a[72:75], v[80:83]\n"
"buffer_load_dwordx4 v[116:119], v202, s[52:55], s15 offen offset:0 sc1\n"
"v_mfma_f32_16x16x32_bf16 v[80:83], v[148:151], a[88:91], v[80:83]\n"
"v_mfma_f32_16x16x32_bf16 v[88:91], v[144:147], a[76:79], v[88:91]\n"
"v_mfma_f32_16x16x32_bf16 v[88:91], v[148:151], a[92:95], v[88:91]\n"
"s_mov_b32 s14, 0x130000\n"
"s_waitcnt lgkmcnt(4)\n"
"v_mfma_f32_16x16x32_bf16 v[68:71], v[128:131], a[64:67], v[68:71]\n"
"ds_read_b64_tr_b16 v[144:145], v187 offset:6144\n"
"buffer_load_dwordx4 v[120:123], v202, s[52:55], s14 offen offset:0 sc1\n"
"v_mfma_f32_16x16x32_bf16 v[68:71], v[132:135], a[80:83], v[68:71]\n"
"ds_read_b64_tr_b16 v[146:147], v187 offset:24576\n"
"v_mfma_f32_16x16x32_bf16 v[76:79], v[128:131], a[68:71], v[76:79]\n"
"ds_read_b64_tr_b16 v[148:149], v187 offset:7168\n"
"v_mfma_f32_16x16x32_bf16 v[76:79], v[132:135], a[84:87], v[76:79]\n"
"ds_read_b64_tr_b16 v[150:151], v187 offset:25600\n"
"s_mov_b32 s15, 0x1c0000\n"
"v_mfma_f32_16x16x32_bf16 v[84:87], v[128:131], a[72:75], v[84:87]\n"
"buffer_load_dwordx4 v[124:127], v202, s[52:55], s15 offen offset:0 sc1\n"
"v_mfma_f32_16x16x32_bf16 v[84:87], v[132:135], a[88:91], v[84:87]\n"
"v_mfma_f32_16x16x32_bf16 v[92:95], v[128:131], a[76:79], v[92:95]\n"
"v_mfma_f32_16x16x32_bf16 v[92:95], v[132:135], a[92:95], v[92:95]\n"
"s_mov_b32 s14, 0x14000\n"
"s_waitcnt lgkmcnt(4)\n"
"v_mfma_f32_16x16x32_bf16 v[64:67], v[136:139], a[96:99], v[64:67]\n"
"buffer_load_dwordx4 v[168:171], v202, s[52:55], s14 offen offset:0 sc1\n"
"v_mfma_f32_16x16x32_bf16 v[64:67], v[140:143], a[112:115], v[64:67]\n"
"v_mfma_f32_16x16x32_bf16 v[72:75], v[136:139], a[100:103], v[72:75]\n"
"v_mfma_f32_16x16x32_bf16 v[72:75], v[140:143], a[116:119], v[72:75]\n"
"s_mov_b32 s15, 0xa4000\n"
"v_mfma_f32_16x16x32_bf16 v[80:83], v[136:139], a[104:107], v[80:83]\n"
"buffer_load_dwordx4 v[172:175], v202, s[52:55], s15 offen offset:0 sc1\n"
"v_mfma_f32_16x16x32_bf16 v[80:83], v[140:143], a[120:123], v[80:83]\n"
"v_mfma_f32_16x16x32_bf16 v[88:91], v[136:139], a[108:111], v[88:91]\n"
"v_mfma_f32_16x16x32_bf16 v[88:91], v[140:143], a[124:127], v[88:91]\n"
"s_mov_b32 s14, 0x134000\n"
"s_waitcnt lgkmcnt(0)\n"
"v_mfma_f32_16x16x32_bf16 v[68:71], v[144:147], a[96:99], v[68:71]\n"
"buffer_load_dwordx4 v[176:179], v202, s[52:55], s14 offen offset:0 sc1\n"
"v_mfma_f32_16x16x32_bf16 v[68:71], v[148:151], a[112:115], v[68:71]\n"
"v_mfma_f32_16x16x32_bf16 v[76:79], v[144:147], a[100:103], v[76:79]\n"
"v_mfma_f32_16x16x32_bf16 v[76:79], v[148:151], a[116:119], v[76:79]\n"
"s_mov_b32 s15, 0x1c4000\n"
"v_mfma_f32_16x16x32_bf16 v[84:87], v[144:147], a[104:107], v[84:87]\n"
"buffer_load_dwordx4 v[180:183], v202, s[52:55], s15 offen offset:0 sc1\n"
"v_mfma_f32_16x16x32_bf16 v[84:87], v[148:151], a[120:123], v[84:87]\n"
"v_mfma_f32_16x16x32_bf16 v[92:95], v[144:147], a[108:111], v[92:95]\n"
"v_mfma_f32_16x16x32_bf16 v[92:95], v[148:151], a[124:127], v[92:95]\n"
"s_nop 7\n"
"v_cvt_pk_bf16_f32 v64, v64, v65\n"
"v_cvt_pk_bf16_f32 v65, v66, v67\n"
"v_cvt_pk_bf16_f32 v66, v68, v69\n"
"v_cvt_pk_bf16_f32 v67, v70, v71\n"
"v_cvt_pk_bf16_f32 v72, v72, v73\n"
"v_cvt_pk_bf16_f32 v73, v74, v75\n"
"v_cvt_pk_bf16_f32 v74, v76, v77\n"
"v_cvt_pk_bf16_f32 v75, v78, v79\n"
"v_cvt_pk_bf16_f32 v80, v80, v81\n"
"v_cvt_pk_bf16_f32 v81, v82, v83\n"
"v_cvt_pk_bf16_f32 v82, v84, v85\n"
"v_cvt_pk_bf16_f32 v83, v86, v87\n"
"v_cvt_pk_bf16_f32 v88, v88, v89\n"
"v_cvt_pk_bf16_f32 v89, v90, v91\n"
"v_cvt_pk_bf16_f32 v90, v92, v93\n"
"v_cvt_pk_bf16_f32 v91, v94, v95\n"
"s_waitcnt vmcnt(12)\n"
"s_mov_b64 exec, 1\n"
"s_nop 0\n"
"buffer_store_dwordx2 v[198:199], v201, s[8:11], 0 offen offset:1024 sc1\n"
"s_mov_b64 exec, -1\n"
"s_mov_b32 s95, 0x20000\n"
"buffer_store_dwordx4 v[64:67], v192, s[52:55], s95 offen offset:0 sc1\n"
"s_mov_b32 s95, 0x20000\n"
"buffer_store_dwordx4 v[72:75], v192, s[52:55], s95 offen offset:1024 sc1\n"
"s_mov_b32 s95, 0x20000\n"
"buffer_store_dwordx4 v[80:83], v192, s[52:55], s95 offen offset:2048 sc1\n"
"s_mov_b32 s95, 0x20000\n"
"buffer_store_dwordx4 v[88:91], v192, s[52:55], s95 offen offset:3072 sc1\n"
"buffer_load_dwordx2 v[206:207], v200, s[8:11], 0 offen offset:1024 sc1\n"
"v_lshlrev_b32 v128, 16, v152\n"
"v_and_b32 v129, 0xffff0000, v152\n"
"v_lshlrev_b32 v130, 16, v153\n"
"v_and_b32 v131, 0xffff0000, v153\n"
"v_lshlrev_b32 v132, 16, v154\n"
"v_and_b32 v133, 0xffff0000, v154\n"
"v_lshlrev_b32 v134, 16, v155\n"
"v_and_b32 v135, 0xffff0000, v155\n"
"v_lshlrev_b32 v136, 16, v156\n"
"v_and_b32 v156, 0xffff0000, v156\n"
"v_add_f32 v128, v128, v136\n"
"v_add_f32 v129, v129, v156\n"
"v_lshlrev_b32 v136, 16, v157\n"
"v_and_b32 v157, 0xffff0000, v157\n"
"v_add_f32 v130, v130, v136\n"
"v_add_f32 v131, v131, v157\n"
"v_lshlrev_b32 v136, 16, v158\n"
"v_and_b32 v158, 0xffff0000, v158\n"
"v_add_f32 v132, v132, v136\n"
"v_add_f32 v133, v133, v158\n"
"v_lshlrev_b32 v136, 16, v159\n"
"v_and_b32 v159, 0xffff0000, v159\n"
"v_add_f32 v134, v134, v136\n"
"v_add_f32 v135, v135, v159\n"
"v_lshlrev_b32 v136, 16, v160\n"
"v_and_b32 v160, 0xffff0000, v160\n"
"v_add_f32 v128, v128, v136\n"
"v_add_f32 v129, v129, v160\n"
"v_lshlrev_b32 v136, 16, v161\n"
"v_and_b32 v161, 0xffff0000, v161\n"
"v_add_f32 v130, v130, v136\n"
"v_add_f32 v131, v131, v161\n"
"v_lshlrev_b32 v136, 16, v162\n"
"v_and_b32 v162, 0xffff0000, v162\n"
"v_add_f32 v132, v132, v136\n"
"v_add_f32 v133, v133, v162\n"
"v_lshlrev_b32 v136, 16, v163\n"
"v_and_b32 v163, 0xffff0000, v163\n"
"v_add_f32 v134, v134, v136\n"
"v_add_f32 v135, v135, v163\n"
"v_lshlrev_b32 v136, 16, v164\n"
"v_and_b32 v164, 0xffff0000, v164\n"
"v_add_f32 v128, v128, v136\n"
"v_add_f32 v129, v129, v164\n"
"v_lshlrev_b32 v136, 16, v165\n"
"v_and_b32 v165, 0xffff0000, v165\n"
"v_add_f32 v130, v130, v136\n"
"v_add_f32 v131, v131, v165\n"
"v_lshlrev_b32 v136, 16, v166\n"
"v_and_b32 v166, 0xffff0000, v166\n"
"v_add_f32 v132, v132, v136\n"
"v_add_f32 v133, v133, v166\n"
"v_lshlrev_b32 v136, 16, v167\n"
"v_and_b32 v167, 0xffff0000, v167\n"
"v_add_f32 v134, v134, v136\n"
"v_add_f32 v135, v135, v167\n"
"v_permlane32_swap_b32 v128, v132\n"
"v_permlane32_swap_b32 v129, v133\n"
"v_permlane32_swap_b32 v130, v134\n"
"v_permlane32_swap_b32 v131, v135\n"
"v_add_f32 v128, v128, v132\n"
"v_add_f32 v129, v129, v133\n"
"v_add_f32 v130, v130, v134\n"
"v_add_f32 v131, v131, v135\n"
"s_nop 0\n"
"v_permlane16_swap_b32 v128, v130\n"
"v_permlane16_swap_b32 v129, v131\n"
"v_add_f32 v128, v128, v130\n"
"v_add_f32 v129, v129, v131\n"
"v_cvt_pk_bf16_f32 v128, v128, v129\n"
"buffer_store_dword v128, v203, s[4:7], 0 offen offset:0\n"
"v_lshlrev_b32 v140, 16, v224\n"
"v_and_b32 v141, 0xffff0000, v224\n"
"v_lshlrev_b32 v142, 16, v225\n"
"v_and_b32 v143, 0xffff0000, v225\n"
"v_lshlrev_b32 v144, 16, v226\n"
"v_and_b32 v145, 0xffff0000, v226\n"
"v_lshlrev_b32 v146, 16, v227\n"
"v_and_b32 v147, 0xffff0000, v227\n"
"v_lshlrev_b32 v148, 16, v228\n"
"v_and_b32 v228, 0xffff0000, v228\n"
"v_add_f32 v140, v140, v148\n"
"v_add_f32 v141, v141, v228\n"
"v_lshlrev_b32 v148, 16, v229\n"
"v_and_b32 v229, 0xffff0000, v229\n"
"v_add_f32 v142, v142, v148\n"
"v_add_f32 v143, v143, v229\n"
"v_lshlrev_b32 v148, 16, v230\n"
"v_and_b32 v230, 0xffff0000, v230\n"
"v_add_f32 v144, v144, v148\n"
"v_add_f32 v145, v145, v230\n"
"v_lshlrev_b32 v148, 16, v231\n"
"v_and_b32 v231, 0xffff0000, v231\n"
"v_add_f32 v146, v146, v148\n"
"v_add_f32 v147, v147, v231\n"
"v_lshlrev_b32 v148, 16, v232\n"
"v_and_b32 v232, 0xffff0000, v232\n"
"v_add_f32 v140, v140, v148\n"
"v_add_f32 v141, v141, v232\n"
"v_lshlrev_b32 v148, 16, v233\n"
"v_and_b32 v233, 0xffff0000, v233\n"
"v_add_f32 v142, v142, v148\n"
"v_add_f32 v143, v143, v233\n"
"v_lshlrev_b32 v148, 16, v234\n"
"v_and_b32 v234, 0xffff0000, v234\n"
"v_add_f32 v144, v144, v148\n"
"v_add_f32 v145, v145, v234\n"
"v_lshlrev_b32 v148, 16, v235\n"
"v_and_b32 v235, 0xffff0000, v235\n"
"v_add_f32 v146, v146, v148\n"
"v_add_f32 v147, v147, v235\n"
"v_lshlrev_b32 v148, 16, v236\n"
"v_and_b32 v236, 0xffff0000, v236\n"
"v_add_f32 v140, v140, v148\n"
"v_add_f32 v141, v141, v236\n"
"v_lshlrev_b32 v148, 16, v237\n"
"v_and_b32 v237, 0xffff0000, v237\n"
"v_add_f32 v142, v142, v148\n"
"v_add_f32 v143, v143, v237\n"
"v_lshlrev_b32 v148, 16, v238\n"
"v_and_b32 v238, 0xffff0000, v238\n"
"v_add_f32 v144, v144, v148\n"
"v_add_f32 v145, v145, v238\n"
"v_lshlrev_b32 v148, 16, v239\n"
"v_and_b32 v239, 0xffff0000, v239\n"
"v_add_f32 v146, v146, v148\n"
"v_add_f32 v147, v147, v239\n"
"v_permlane32_swap_b32 v140, v144\n"
"v_permlane32_swap_b32 v141, v145\n"
"v_permlane32_swap_b32 v142, v146\n"
"v_permlane32_swap_b32 v143, v147\n"
"v_add_f32 v140, v140, v144\n"
"v_add_f32 v141, v141, v145\n"
"v_add_f32 v142, v142, v146\n"
"v_add_f32 v143, v143, v147\n"
"s_nop 0\n"
"v_permlane16_swap_b32 v140, v142\n"
"v_permlane16_swap_b32 v141, v143\n"
"v_add_f32 v140, v140, v142\n"
"v_add_f32 v141, v141, v143\n"
"v_cvt_pk_bf16_f32 v140, v140, v141\n"
"buffer_store_dword v140, v203, s[4:7], 0 offen offset:128\n"
"s_waitcnt vmcnt(3)\n"
"s_mov_b64 exec, 1\n"
"s_nop 0\n"
"buffer_store_dwordx2 v[198:199], v201, s[8:11], 0 offen offset:1536 sc1\n"
"s_mov_b64 exec, -1\n"
"s_waitcnt vmcnt(3)\n"
"v_cmp_eq_u64 vcc, s[12:13], v[206:207]\n"
"s_nop 3\n"
"s_cmp_eq_u64 vcc, -1\n"
"s_cbranch_scc1 Lok3_%=\n"
"s_mov_b32 s16, 0x8000\n"
"Lspin3_%=:\n"
"s_sleep 1\n"
"buffer_load_dwordx2 v[206:207], v200, s[8:11], 0 offen offset:1024 sc1\n"
"s_waitcnt vmcnt(0)\n"
"v_cmp_eq_u64 vcc, s[12:13], v[206:207]\n"
"s_sub_u32 s16, s16, 1\n"
"s_cmp_eq_u32 s16, 0\n"
"s_cbranch_scc1 Lok3_%=\n"
"s_cmp_eq_u64 vcc, -1\n"
"s_cbranch_scc0 Lspin3_%=\n"
"Lok3_%=:\n"
"s_mov_b32 s14, 0x18000\n"
"buffer_load_dwordx4 v[0:3], v202, s[52:55], s14 offen offset:0 sc1\n"
"s_mov_b32 s15, 0xa8000\n"
"buffer_load_dwordx4 v[4:7], v202, s[52:55], s15 offen offset:0 sc1\n"
"s_mov_b32 s14, 0x138000\n"
"buffer_load_dwordx4 v[8:11], v202, s[52:55], s14 offen offset:0 sc1\n"
"s_mov_b32 s15, 0x1c8000\n"
"buffer_load_dwordx4 v[12:15], v202, s[52:55], s15 offen offset:0 sc1\n"
"s_mov_b32 s14, 0x1c000\n"
"buffer_load_dwordx4 v[16:19], v202, s[52:55], s14 offen offset:0 sc1\n"
"s_mov_b32 s15, 0xac000\n"
"buffer_load_dwordx4 v[20:23], v202, s[52:55], s15 offen offset:0 sc1\n"
"s_mov_b32 s14, 0x13c000\n"
"buffer_load_dwordx4 v[24:27], v202, s[52:55], s14 offen offset:0 sc1\n"
"s_mov_b32 s15, 0x1cc000\n"
"buffer_load_dwordx4 v[28:31], v202, s[52:55], s15 offen offset:0 sc1\n"
"buffer_load_dwordx2 v[208:209], v200, s[8:11], 0 offen offset:1536 sc1\n"
"v_lshlrev_b32 v128, 16, v240\n"
"v_and_b32 v129, 0xffff0000, v240\n"
"v_lshlrev_b32 v130, 16, v241\n"
"v_and_b32 v131, 0xffff0000, v241\n"
"v_lshlrev_b32 v132, 16, v242\n"
"v_and_b32 v133, 0xffff0000, v242\n"
"v_lshlrev_b32 v134, 16, v243\n"
"v_and_b32 v135, 0xffff0000, v243\n"
"v_lshlrev_b32 v136, 16, v244\n"
"v_and_b32 v244, 0xffff0000, v244\n"
"v_add_f32 v128, v128, v136\n"
"v_add_f32 v129, v129, v244\n"
"v_lshlrev_b32 v136, 16, v245\n"
"v_and_b32 v245, 0xffff0000, v245\n"
"v_add_f32 v130, v130, v136\n"
"v_add_f32 v131, v131, v245\n"
"v_lshlrev_b32 v136, 16, v246\n"
"v_and_b32 v246, 0xffff0000, v246\n"
"v_add_f32 v132, v132, v136\n"
"v_add_f32 v133, v133, v246\n"
"v_lshlrev_b32 v136, 16, v247\n"
"v_and_b32 v247, 0xffff0000, v247\n"
"v_add_f32 v134, v134, v136\n"
"v_add_f32 v135, v135, v247\n"
"v_lshlrev_b32 v136, 16, v248\n"
"v_and_b32 v248, 0xffff0000, v248\n"
"v_add_f32 v128, v128, v136\n"
"v_add_f32 v129, v129, v248\n"
"v_lshlrev_b32 v136, 16, v249\n"
"v_and_b32 v249, 0xffff0000, v249\n"
"v_add_f32 v130, v130, v136\n"
"v_add_f32 v131, v131, v249\n"
"v_lshlrev_b32 v136, 16, v250\n"
"v_and_b32 v250, 0xffff0000, v250\n"
"v_add_f32 v132, v132, v136\n"
"v_add_f32 v133, v133, v250\n"
"v_lshlrev_b32 v136, 16, v251\n"
"v_and_b32 v251, 0xffff0000, v251\n"
"v_add_f32 v134, v134, v136\n"
"v_add_f32 v135, v135, v251\n"
"v_lshlrev_b32 v136, 16, v252\n"
"v_and_b32 v252, 0xffff0000, v252\n"
"v_add_f32 v128, v128, v136\n"
"v_add_f32 v129, v129, v252\n"
"v_lshlrev_b32 v136, 16, v253\n"
"v_and_b32 v253, 0xffff0000, v253\n"
"v_add_f32 v130, v130, v136\n"
"v_add_f32 v131, v131, v253\n"
"v_lshlrev_b32 v136, 16, v254\n"
"v_and_b32 v254, 0xffff0000, v254\n"
"v_add_f32 v132, v132, v136\n"
"v_add_f32 v133, v133, v254\n"
"v_lshlrev_b32 v136, 16, v255\n"
"v_and_b32 v255, 0xffff0000, v255\n"
"v_add_f32 v134, v134, v136\n"
"v_add_f32 v135, v135, v255\n"
"v_permlane32_swap_b32 v128, v132\n"
"v_permlane32_swap_b32 v129, v133\n"
"v_permlane32_swap_b32 v130, v134\n"
"v_permlane32_swap_b32 v131, v135\n"
"v_add_f32 v128, v128, v132\n"
"v_add_f32 v129, v129, v133\n"
"v_add_f32 v130, v130, v134\n"
"v_add_f32 v131, v131, v135\n"
"s_nop 0\n"
"v_permlane16_swap_b32 v128, v130\n"
"v_permlane16_swap_b32 v129, v131\n"
"v_add_f32 v128, v128, v130\n"
"v_add_f32 v129, v129, v131\n"
"v_cvt_pk_bf16_f32 v128, v128, v129\n"
"buffer_store_dword v128, v203, s[4:7], 0 offen offset:256\n"
"v_lshlrev_b32 v140, 16, v96\n"
"v_and_b32 v141, 0xffff0000, v96\n"
"v_lshlrev_b32 v142, 16, v97\n"
"v_and_b32 v143, 0xffff0000, v97\n"
"v_lshlrev_b32 v144, 16, v98\n"
"v_and_b32 v145, 0xffff0000, v98\n"
"v_lshlrev_b32 v146, 16, v99\n"
"v_and_b32 v147, 0xffff0000, v99\n"
"v_lshlrev_b32 v148, 16, v100\n"
"v_and_b32 v100, 0xffff0000, v100\n"
"v_add_f32 v140, v140, v148\n"
"v_add_f32 v141, v141, v100\n"
"v_lshlrev_b32 v148, 16, v101\n"
"v_and_b32 v101, 0xffff0000, v101\n"
"v_add_f32 v142, v142, v148\n"
"v_add_f32 v143, v143, v101\n"
"v_lshlrev_b32 v148, 16, v102\n"
"v_and_b32 v102, 0xffff0000, v102\n"
"v_add_f32 v144, v144, v148\n"
"v_add_f32 v145, v145, v102\n"
"v_lshlrev_b32 v148, 16, v103\n"
"v_and_b32 v103, 0xffff0000, v103\n"
"v_add_f32 v146, v146, v148\n"
"v_add_f32 v147, v147, v103\n"
"v_lshlrev_b32 v148, 16, v104\n"
"v_and_b32 v104, 0xffff0000, v104\n"
"v_add_f32 v140, v140, v148\n"
"v_add_f32 v141, v141, v104\n"
"v_lshlrev_b32 v148, 16, v105\n"
"v_and_b32 v105, 0xffff0000, v105\n"
"v_add_f32 v142, v142, v148\n"
"v_add_f32 v143, v143, v105\n"
"v_lshlrev_b32 v148, 16, v106\n"
"v_and_b32 v106, 0xffff0000, v106\n"
"v_add_f32 v144, v144, v148\n"
"v_add_f32 v145, v145, v106\n"
"v_lshlrev_b32 v148, 16, v107\n"
"v_and_b32 v107, 0xffff0000, v107\n"
"v_add_f32 v146, v146, v148\n"
"v_add_f32 v147, v147, v107\n"
"v_lshlrev_b32 v148, 16, v108\n"
"v_and_b32 v108, 0xffff0000, v108\n"
"v_add_f32 v140, v140, v148\n"
"v_add_f32 v141, v141, v108\n"
"v_lshlrev_b32 v148, 16, v109\n"
"v_and_b32 v109, 0xffff0000, v109\n"
"v_add_f32 v142, v142, v148\n"
"v_add_f32 v143, v143, v109\n"
"v_lshlrev_b32 v148, 16, v110\n"
"v_and_b32 v110, 0xffff0000, v110\n"
"v_add_f32 v144, v144, v148\n"
"v_add_f32 v145, v145, v110\n"
"v_lshlrev_b32 v148, 16, v111\n"
"v_and_b32 v111, 0xffff0000, v111\n"
"v_add_f32 v146, v146, v148\n"
"v_add_f32 v147, v147, v111\n"
"v_permlane32_swap_b32 v140, v144\n"
"v_permlane32_swap_b32 v141, v145\n"
"v_permlane32_swap_b32 v142, v146\n"
"v_permlane32_swap_b32 v143, v147\n"
"v_add_f32 v140, v140, v144\n"
"v_add_f32 v141, v141, v145\n"
"v_add_f32 v142, v142, v146\n"
"v_add_f32 v143, v143, v147\n"
"s_nop 0\n"
"v_permlane16_swap_b32 v140, v142\n"
"v_permlane16_swap_b32 v141, v143\n"
"v_add_f32 v140, v140, v142\n"
"v_add_f32 v141, v141, v143\n"
"v_cvt_pk_bf16_f32 v140, v140, v141\n"
"buffer_store_dword v140, v203, s[4:7], 0 offen offset:384\n"
"s_waitcnt vmcnt(2)\n"
"v_cmp_eq_u64 vcc, s[12:13], v[208:209]\n"
"s_nop 3\n"
"s_cmp_eq_u64 vcc, -1\n"
"s_cbranch_scc1 Lok4_%=\n"
"s_mov_b32 s16, 0x8000\n"
"Lspin4_%=:\n"
"s_sleep 1\n"
"buffer_load_dwordx2 v[208:209], v200, s[8:11], 0 offen offset:1536 sc1\n"
"s_waitcnt vmcnt(0)\n"
"v_cmp_eq_u64 vcc, s[12:13], v[208:209]\n"
"s_sub_u32 s16, s16, 1\n"
"s_cmp_eq_u32 s16, 0\n"
"s_cbranch_scc1 Lok4_%=\n"
"s_cmp_eq_u64 vcc, -1\n"
"s_cbranch_scc0 Lspin4_%=\n"
"Lok4_%=:\n"
"s_mov_b32 s14, 0x20000\n"
"buffer_load_dwordx4 v[32:35], v202, s[52:55], s14 offen offset:0 sc1\n"
"s_mov_b32 s15, 0xb0000\n"
"buffer_load_dwordx4 v[36:39], v202, s[52:55], s15 offen offset:0 sc1\n"
"s_mov_b32 s14, 0x140000\n"
"buffer_load_dwordx4 v[40:43], v202, s[52:55], s14 offen offset:0 sc1\n"
"s_mov_b32 s15, 0x1d0000\n"
"buffer_load_dwordx4 v[44:47], v202, s[52:55], s15 offen offset:0 sc1\n"
"v_lshlrev_b32 v128, 16, v112\n"
"v_and_b32 v129, 0xffff0000, v112\n"
"v_lshlrev_b32 v130, 16, v113\n"
"v_and_b32 v131, 0xffff0000, v113\n"
"v_lshlrev_b32 v132, 16, v114\n"
"v_and_b32 v133, 0xffff0000, v114\n"
"v_lshlrev_b32 v134, 16, v115\n"
"v_and_b32 v135, 0xffff0000, v115\n"
"v_lshlrev_b32 v136, 16, v116\n"
"v_and_b32 v116, 0xffff0000, v116\n"
"v_add_f32 v128, v128, v136\n"
"v_add_f32 v129, v129, v116\n"
"v_lshlrev_b32 v136, 16, v117\n"
"v_and_b32 v117, 0xffff0000, v117\n"
"v_add_f32 v130, v130, v136\n"
"v_add_f32 v131, v131, v117\n"
"v_lshlrev_b32 v136, 16, v118\n"
"v_and_b32 v118, 0xffff0000, v118\n"
"v_add_f32 v132, v132, v136\n"
"v_add_f32 v133, v133, v118\n"
"v_lshlrev_b32 v136, 16, v119\n"
"v_and_b32 v119, 0xffff0000, v119\n"
"v_add_f32 v134, v134, v136\n"
"v_add_f32 v135, v135, v119\n"
"v_lshlrev_b32 v136, 16, v120\n"
"v_and_b32 v120, 0xffff0000, v120\n"
"v_add_f32 v128, v128, v136\n"
"v_add_f32 v129, v129, v120\n"
"v_lshlrev_b32 v136, 16, v121\n"
"v_and_b32 v121, 0xffff0000, v121\n"
"v_add_f32 v130, v130, v136\n"
"v_add_f32 v131, v131, v121\n"
"v_lshlrev_b32 v136, 16, v122\n"
"v_and_b32 v122, 0xffff0000, v122\n"
"v_add_f32 v132, v132, v136\n"
"v_add_f32 v133, v133, v122\n"
"v_lshlrev_b32 v136, 16, v123\n"
"v_and_b32 v123, 0xffff0000, v123\n"
"v_add_f32 v134, v134, v136\n"
"v_add_f32 v135, v135, v123\n"
"v_lshlrev_b32 v136, 16, v124\n"
"v_and_b32 v124, 0xffff0000, v124\n"
"v_add_f32 v128, v128, v136\n"
"v_add_f32 v129, v129, v124\n"
"v_lshlrev_b32 v136, 16, v125\n"
"v_and_b32 v125, 0xffff0000, v125\n"
"v_add_f32 v130, v130, v136\n"
"v_add_f32 v131, v131, v125\n"
"v_lshlrev_b32 v136, 16, v126\n"
"v_and_b32 v126, 0xffff0000, v126\n"
"v_add_f32 v132, v132, v136\n"
"v_add_f32 v133, v133, v126\n"
"v_lshlrev_b32 v136, 16, v127\n"
"v_and_b32 v127, 0xffff0000, v127\n"
"v_add_f32 v134, v134, v136\n"
"v_add_f32 v135, v135, v127\n"
"v_permlane32_swap_b32 v128, v132\n"
"v_permlane32_swap_b32 v129, v133\n"
"v_permlane32_swap_b32 v130, v134\n"
"v_permlane32_swap_b32 v131, v135\n"
"v_add_f32 v128, v128, v132\n"
"v_add_f32 v129, v129, v133\n"
"v_add_f32 v130, v130, v134\n"
"v_add_f32 v131, v131, v135\n"
"s_nop 0\n"
"v_permlane16_swap_b32 v128, v130\n"
"v_permlane16_swap_b32 v129, v131\n"
"v_add_f32 v128, v128, v130\n"
"v_add_f32 v129, v129, v131\n"
"v_cvt_pk_bf16_f32 v128, v128, v129\n"
"buffer_store_dword v128, v203, s[4:7], 0 offen offset:512\n"
"v_lshlrev_b32 v140, 16, v168\n"
"v_and_b32 v141, 0xffff0000, v168\n"
"v_lshlrev_b32 v142, 16, v169\n"
"v_and_b32 v143, 0xffff0000, v169\n"
"v_lshlrev_b32 v144, 16, v170\n"
"v_and_b32 v145, 0xffff0000, v170\n"
"v_lshlrev_b32 v146, 16, v171\n"
"v_and_b32 v147, 0xffff0000, v171\n"
"v_lshlrev_b32 v148, 16, v172\n"
"v_and_b32 v172, 0xffff0000, v172\n"
"v_add_f32 v140, v140, v148\n"
"v_add_f32 v141, v141, v172\n"
"v_lshlrev_b32 v148, 16, v173\n"
"v_and_b32 v173, 0xffff0000, v173\n"
"v_add_f32 v142, v142, v148\n"
"v_add_f32 v143, v143, v173\n"
"v_lshlrev_b32 v148, 16, v174\n"
"v_and_b32 v174, 0xffff0000, v174\n"
"v_add_f32 v144, v144, v148\n"
"v_add_f32 v145, v145, v174\n"
"v_lshlrev_b32 v148, 16, v175\n"
"v_and_b32 v175, 0xffff0000, v175\n"
"v_add_f32 v146, v146, v148\n"
"v_add_f32 v147, v147, v175\n"
"v_lshlrev_b32 v148, 16, v176\n"
"v_and_b32 v176, 0xffff0000, v176\n"
"v_add_f32 v140, v140, v148\n"
"v_add_f32 v141, v141, v176\n"
"v_lshlrev_b32 v148, 16, v177\n"
"v_and_b32 v177, 0xffff0000, v177\n"
"v_add_f32 v142, v142, v148\n"
"v_add_f32 v143, v143, v177\n"
"v_lshlrev_b32 v148, 16, v178\n"
"v_and_b32 v178, 0xffff0000, v178\n"
"v_add_f32 v144, v144, v148\n"
"v_add_f32 v145, v145, v178\n"
"v_lshlrev_b32 v148, 16, v179\n"
"v_and_b32 v179, 0xffff0000, v179\n"
"v_add_f32 v146, v146, v148\n"
"v_add_f32 v147, v147, v179\n"
"v_lshlrev_b32 v148, 16, v180\n"
"v_and_b32 v180, 0xffff0000, v180\n"
"v_add_f32 v140, v140, v148\n"
"v_add_f32 v141, v141, v180\n"
"v_lshlrev_b32 v148, 16, v181\n"
"v_and_b32 v181, 0xffff0000, v181\n"
"v_add_f32 v142, v142, v148\n"
"v_add_f32 v143, v143, v181\n"
"v_lshlrev_b32 v148, 16, v182\n"
"v_and_b32 v182, 0xffff0000, v182\n"
"v_add_f32 v144, v144, v148\n"
"v_add_f32 v145, v145, v182\n"
"v_lshlrev_b32 v148, 16, v183\n"
"v_and_b32 v183, 0xffff0000, v183\n"
"v_add_f32 v146, v146, v148\n"
"v_add_f32 v147, v147, v183\n"
"v_permlane32_swap_b32 v140, v144\n"
"v_permlane32_swap_b32 v141, v145\n"
"v_permlane32_swap_b32 v142, v146\n"
"v_permlane32_swap_b32 v143, v147\n"
"v_add_f32 v140, v140, v144\n"
"v_add_f32 v141, v141, v145\n"
"v_add_f32 v142, v142, v146\n"
"v_add_f32 v143, v143, v147\n"
"s_nop 0\n"
"v_permlane16_swap_b32 v140, v142\n"
"v_permlane16_swap_b32 v141, v143\n"
"v_add_f32 v140, v140, v142\n"
"v_add_f32 v141, v141, v143\n"
"v_cvt_pk_bf16_f32 v140, v140, v141\n"
"buffer_store_dword v140, v203, s[4:7], 0 offen offset:640\n"
"v_lshlrev_b32 v128, 16, v0\n"
"v_and_b32 v129, 0xffff0000, v0\n"
"v_lshlrev_b32 v130, 16, v1\n"
"v_and_b32 v131, 0xffff0000, v1\n"
"v_lshlrev_b32 v132, 16, v2\n"
"v_and_b32 v133, 0xffff0000, v2\n"
"v_lshlrev_b32 v134, 16, v3\n"
"v_and_b32 v135, 0xffff0000, v3\n"
"v_lshlrev_b32 v136, 16, v4\n"
"v_and_b32 v4, 0xffff0000, v4\n"
"v_add_f32 v128, v128, v136\n"
"v_add_f32 v129, v129, v4\n"
"v_lshlrev_b32 v136, 16, v5\n"
"v_and_b32 v5, 0xffff0000, v5\n"
"v_add_f32 v130, v130, v136\n"
"v_add_f32 v131, v131, v5\n"
"v_lshlrev_b32 v136, 16, v6\n"
"v_and_b32 v6, 0xffff0000, v6\n"
"v_add_f32 v132, v132, v136\n"
"v_add_f32 v133, v133, v6\n"
"v_lshlrev_b32 v136, 16, v7\n"
"v_and_b32 v7, 0xffff0000, v7\n"
"v_add_f32 v134, v134, v136\n"
"v_add_f32 v135, v135, v7\n"
"v_lshlrev_b32 v136, 16, v8\n"
"v_and_b32 v8, 0xffff0000, v8\n"
"v_add_f32 v128, v128, v136\n"
"v_add_f32 v129, v129, v8\n"
"v_lshlrev_b32 v136, 16, v9\n"
"v_and_b32 v9, 0xffff0000, v9\n"
"v_add_f32 v130, v130, v136\n"
"v_add_f32 v131, v131, v9\n"
"v_lshlrev_b32 v136, 16, v10\n"
"v_and_b32 v10, 0xffff0000, v10\n"
"v_add_f32 v132, v132, v136\n"
"v_add_f32 v133, v133, v10\n"
"v_lshlrev_b32 v136, 16, v11\n"
"v_and_b32 v11, 0xffff0000, v11\n"
"v_add_f32 v134, v134, v136\n"
"v_add_f32 v135, v135, v11\n"
"v_lshlrev_b32 v136, 16, v12\n"
"v_and_b32 v12, 0xffff0000, v12\n"
"v_add_f32 v128, v128, v136\n"
"v_add_f32 v129, v129, v12\n"
"v_lshlrev_b32 v136, 16, v13\n"
"v_and_b32 v13, 0xffff0000, v13\n"
"v_add_f32 v130, v130, v136\n"
"v_add_f32 v131, v131, v13\n"
"v_lshlrev_b32 v136, 16, v14\n"
"v_and_b32 v14, 0xffff0000, v14\n"
"v_add_f32 v132, v132, v136\n"
"v_add_f32 v133, v133, v14\n"
"v_lshlrev_b32 v136, 16, v15\n"
"v_and_b32 v15, 0xffff0000, v15\n"
"v_add_f32 v134, v134, v136\n"
"v_add_f32 v135, v135, v15\n"
"v_permlane32_swap_b32 v128, v132\n"
"v_permlane32_swap_b32 v129, v133\n"
"v_permlane32_swap_b32 v130, v134\n"
"v_permlane32_swap_b32 v131, v135\n"
"v_add_f32 v128, v128, v132\n"
"v_add_f32 v129, v129, v133\n"
"v_add_f32 v130, v130, v134\n"
"v_add_f32 v131, v131, v135\n"
"s_nop 0\n"
"v_permlane16_swap_b32 v128, v130\n"
"v_permlane16_swap_b32 v129, v131\n"
"v_add_f32 v128, v128, v130\n"
"v_add_f32 v129, v129, v131\n"
"v_cvt_pk_bf16_f32 v128, v128, v129\n"
"buffer_store_dword v128, v203, s[4:7], 0 offen offset:768\n"
"v_lshlrev_b32 v140, 16, v16\n"
"v_and_b32 v141, 0xffff0000, v16\n"
"v_lshlrev_b32 v142, 16, v17\n"
"v_and_b32 v143, 0xffff0000, v17\n"
"v_lshlrev_b32 v144, 16, v18\n"
"v_and_b32 v145, 0xffff0000, v18\n"
"v_lshlrev_b32 v146, 16, v19\n"
"v_and_b32 v147, 0xffff0000, v19\n"
"v_lshlrev_b32 v148, 16, v20\n"
"v_and_b32 v20, 0xffff0000, v20\n"
"v_add_f32 v140, v140, v148\n"
"v_add_f32 v141, v141, v20\n"
"v_lshlrev_b32 v148, 16, v21\n"
"v_and_b32 v21, 0xffff0000, v21\n"
"v_add_f32 v142, v142, v148\n"
"v_add_f32 v143, v143, v21\n"
"v_lshlrev_b32 v148, 16, v22\n"
"v_and_b32 v22, 0xffff0000, v22\n"
"v_add_f32 v144, v144, v148\n"
"v_add_f32 v145, v145, v22\n"
"v_lshlrev_b32 v148, 16, v23\n"
"v_and_b32 v23, 0xffff0000, v23\n"
"v_add_f32 v146, v146, v148\n"
"v_add_f32 v147, v147, v23\n"
"v_lshlrev_b32 v148, 16, v24\n"
"v_and_b32 v24, 0xffff0000, v24\n"
"v_add_f32 v140, v140, v148\n"
"v_add_f32 v141, v141, v24\n"
"v_lshlrev_b32 v148, 16, v25\n"
"v_and_b32 v25, 0xffff0000, v25\n"
"v_add_f32 v142, v142, v148\n"
"v_add_f32 v143, v143, v25\n"
"v_lshlrev_b32 v148, 16, v26\n"
"v_and_b32 v26, 0xffff0000, v26\n"
"v_add_f32 v144, v144, v148\n"
"v_add_f32 v145, v145, v26\n"
"v_lshlrev_b32 v148, 16, v27\n"
"v_and_b32 v27, 0xffff0000, v27\n"
"v_add_f32 v146, v146, v148\n"
"v_add_f32 v147, v147, v27\n"
"v_lshlrev_b32 v148, 16, v28\n"
"v_and_b32 v28, 0xffff0000, v28\n"
"v_add_f32 v140, v140, v148\n"
"v_add_f32 v141, v141, v28\n"
"v_lshlrev_b32 v148, 16, v29\n"
"v_and_b32 v29, 0xffff0000, v29\n"
"v_add_f32 v142, v142, v148\n"
"v_add_f32 v143, v143, v29\n"
"v_lshlrev_b32 v148, 16, v30\n"
"v_and_b32 v30, 0xffff0000, v30\n"
"v_add_f32 v144, v144, v148\n"
"v_add_f32 v145, v145, v30\n"
"v_lshlrev_b32 v148, 16, v31\n"
"v_and_b32 v31, 0xffff0000, v31\n"
"v_add_f32 v146, v146, v148\n"
"v_add_f32 v147, v147, v31\n"
"v_permlane32_swap_b32 v140, v144\n"
"v_permlane32_swap_b32 v141, v145\n"
"v_permlane32_swap_b32 v142, v146\n"
"v_permlane32_swap_b32 v143, v147\n"
"v_add_f32 v140, v140, v144\n"
"v_add_f32 v141, v141, v145\n"
"v_add_f32 v142, v142, v146\n"
"v_add_f32 v143, v143, v147\n"
"s_nop 0\n"
"v_permlane16_swap_b32 v140, v142\n"
"v_permlane16_swap_b32 v141, v143\n"
"v_add_f32 v140, v140, v142\n"
"v_add_f32 v141, v141, v143\n"
"v_cvt_pk_bf16_f32 v140, v140, v141\n"
"buffer_store_dword v140, v203, s[4:7], 0 offen offset:896\n"
"s_waitcnt vmcnt(7)\n"
"v_lshlrev_b32 v128, 16, v32\n"
"v_and_b32 v129, 0xffff0000, v32\n"
"v_lshlrev_b32 v130, 16, v33\n"
"v_and_b32 v131, 0xffff0000, v33\n"
"v_lshlrev_b32 v132, 16, v34\n"
"v_and_b32 v133, 0xffff0000, v34\n"
"v_lshlrev_b32 v134, 16, v35\n"
"v_and_b32 v135, 0xffff0000, v35\n"
"s_waitcnt vmcnt(6)\n"
"v_lshlrev_b32 v136, 16, v36\n"
"v_and_b32 v36, 0xffff0000, v36\n"
"v_add_f32 v128, v128, v136\n"
"v_add_f32 v129, v129, v36\n"
"v_lshlrev_b32 v136, 16, v37\n"
"v_and_b32 v37, 0xffff0000, v37\n"
"v_add_f32 v130, v130, v136\n"
"v_add_f32 v131, v131, v37\n"
"v_lshlrev_b32 v136, 16, v38\n"
"v_and_b32 v38, 0xffff0000, v38\n"
"v_add_f32 v132, v132, v136\n"
"v_add_f32 v133, v133, v38\n"
"v_lshlrev_b32 v136, 16, v39\n"
"v_and_b32 v39, 0xffff0000, v39\n"
"v_add_f32 v134, v134, v136\n"
"v_add_f32 v135, v135, v39\n"
"s_waitcnt vmcnt(5)\n"
"v_lshlrev_b32 v136, 16, v40\n"
"v_and_b32 v40, 0xffff0000, v40\n"
"v_add_f32 v128, v128, v136\n"
"v_add_f32 v129, v129, v40\n"
"v_lshlrev_b32 v136, 16, v41\n"
"v_and_b32 v41, 0xffff0000, v41\n"
"v_add_f32 v130, v130, v136\n"
"v_add_f32 v131, v131, v41\n"
"v_lshlrev_b32 v136, 16, v42\n"
"v_and_b32 v42, 0xffff0000, v42\n"
"v_add_f32 v132, v132, v136\n"
"v_add_f32 v133, v133, v42\n"
"v_lshlrev_b32 v136, 16, v43\n"
"v_and_b32 v43, 0xffff0000, v43\n"
"v_add_f32 v134, v134, v136\n"
"v_add_f32 v135, v135, v43\n"
"s_waitcnt vmcnt(4)\n"
"v_lshlrev_b32 v136, 16, v44\n"
"v_and_b32 v44, 0xffff0000, v44\n"
"v_add_f32 v128, v128, v136\n"
"v_add_f32 v129, v129, v44\n"
"v_lshlrev_b32 v136, 16, v45\n"
"v_and_b32 v45, 0xffff0000, v45\n"
"v_add_f32 v130, v130, v136\n"
"v_add_f32 v131, v131, v45\n"
"v_lshlrev_b32 v136, 16, v46\n"
"v_and_b32 v46, 0xffff0000, v46\n"
"v_add_f32 v132, v132, v136\n"
"v_add_f32 v133, v133, v46\n"
"v_lshlrev_b32 v136, 16, v47\n"
"v_and_b32 v47, 0xffff0000, v47\n"
"v_add_f32 v134, v134, v136\n"
"v_add_f32 v135, v135, v47\n"
"v_permlane32_swap_b32 v128, v132\n"
"v_permlane32_swap_b32 v129, v133\n"
"v_permlane32_swap_b32 v130, v134\n"
"v_permlane32_swap_b32 v131, v135\n"
"v_add_f32 v128, v128, v132\n"
"v_add_f32 v129, v129, v133\n"
"v_add_f32 v130, v130, v134\n"
"v_add_f32 v131, v131, v135\n"
"s_nop 0\n"
"v_permlane16_swap_b32 v128, v130\n"
"v_permlane16_swap_b32 v129, v131\n"
"v_add_f32 v128, v128, v130\n"
"v_add_f32 v129, v129, v131\n"
"v_cvt_pk_bf16_f32 v128, v128, v129\n"
"buffer_store_dword v128, v203, s[4:7], 0 offen offset:1024\n"
"s_waitcnt vmcnt(0)\n"
"Lend%=:\n"
    : "+v"(tid)
    : "s"(__builtin_amdgcn_kernarg_segment_ptr()), "s"(blockIdx.x), "s"(lp)
    : "v1", "v2", "v3", "v4", "v5", "v6", "v7", "v8", "v9", "v10", "v11", "v12", "v13", "v14", "v15", "v16", "v17", "v18", "v19", "v20", "v21", "v22", "v23", "v24", "v25", "v26", "v27", "v28", "v29", "v30", "v31", "v32", "v33", "v34", "v35", "v36", "v37", "v38", "v39", "v40", "v41", "v42", "v43", "v44", "v45", "v46", "v47", "v48", "v49", "v50", "v51", "v52", "v53", "v54", "v55", "v56", "v57", "v58", "v59", "v60", "v61", "v62", "v63", "v64", "v65", "v66", "v67", "v68", "v69", "v70", "v71", "v72", "v73", "v74", "v75", "v76", "v77", "v78", "v79", "v80", "v81", "v82", "v83", "v84", "v85", "v86", "v87", "v88", "v89", "v90", "v91", "v92", "v93", "v94", "v95", "v96", "v97", "v98", "v99", "v100", "v101", "v102", "v103", "v104", "v105", "v106", "v107", "v108", "v109", "v110", "v111", "v112", "v113", "v114", "v115", "v116", "v117", "v118", "v119", "v120", "v121", "v122", "v123", "v124", "v125", "v126", "v127", "v128", "v129", "v130", "v131", "v132", "v133", "v134", "v135", "v136", "v137", "v138", "v139", "v140", "v141", "v142", "v143", "v144", "v145", "v146", "v147", "v148", "v149", "v150", "v151", "v152", "v153", "v154", "v155", "v156", "v157", "v158", "v159", "v160", "v161", "v162", "v163", "v164", "v165", "v166", "v167", "v168", "v169", "v170", "v171", "v172", "v173", "v174", "v175", "v176", "v177", "v178", "v179", "v180", "v181", "v182", "v183", "v184", "v185", "v186", "v187", "v188", "v189", "v190", "v191", "v192", "v193", "v194", "v195", "v196", "v197", "v198", "v199", "v200", "v201", "v202", "v203", "v204", "v205", "v206", "v207", "v208", "v209", "v210", "v211", "v212", "v213", "v214", "v215", "v216", "v217", "v218", "v219", "v220", "v221", "v222", "v223", "v224", "v225", "v226", "v227", "v228", "v229", "v230", "v231", "v232", "v233", "v234", "v235", "v236", "v237", "v238", "v239", "v240", "v241", "v242", "v243", "v244", "v245", "v246", "v247", "v248", "v249", "v250", "v251", "v252", "v253", "v254", "v255", "a0", "a1", "a2", "a3", "a4", "a5", "a6", "a7", "a8", "a9", "a10", "a11", "a12", "a13", "a14", "a15", "a16", "a17", "a18", "a19", "a20", "a21", "a22", "a23", "a24", "a25", "a26", "a27", "a28", "a29", "a30", "a31", "a32", "a33", "a34", "a35", "a36", "a37", "a38", "a39", "a40", "a41", "a42", "a43", "a44", "a45", "a46", "a47", "a48", "a49", "a50", "a51", "a52", "a53", "a54", "a55", "a56", "a57", "a58", "a59", "a60", "a61", "a62", "a63", "a64", "a65", "a66", "a67", "a68", "a69", "a70", "a71", "a72", "a73", "a74", "a75", "a76", "a77", "a78", "a79", "a80", "a81", "a82", "a83", "a84", "a85", "a86", "a87", "a88", "a89", "a90", "a91", "a92", "a93", "a94", "a95", "a96", "a97", "a98", "a99", "a100", "a101", "a102", "a103", "a104", "a105", "a106", "a107", "a108", "a109", "a110", "a111", "a112", "a113", "a114", "a115", "a116", "a117", "a118", "a119", "a120", "a121", "a122", "a123", "a124", "a125", "a126", "a127", "a128", "a129", "a130", "a131", "a132", "a133", "a134", "a135", "a136", "a137", "a138", "a139", "a140", "a141", "a142", "a143", "a144", "a145", "a146", "a147", "a148", "a149", "a150", "a151", "a152", "a153", "a154", "a155", "a156", "a157", "a158", "a159", "a160", "a161", "a162", "a163", "a164", "a165", "a166", "a167", "a168", "a169", "a170", "a171", "a172", "a173", "a174", "a175", "a176", "a177", "a178", "a179", "a180", "a181", "a182", "a183", "a184", "a185", "a186", "a187", "a188", "a189", "a190", "a191", "a192", "a193", "a194", "a195", "a196", "a197", "a198", "a199", "a200", "a201", "a202", "a203", "a204", "a205", "a206", "a207", "a208", "a209", "a210", "a211", "a212", "a213", "a214", "a215", "a216", "a217", "a218", "a219", "a220", "a221", "a222", "a223", "a224", "a225", "a226", "a227", "a228", "a229", "a230", "a231", "a232", "a233", "a234", "a235", "a236", "a237", "a238", "a239", "a240", "a241", "a242", "a243", "a244", "a245", "a246", "a247", "a248", "a249", "a250", "a251", "a252", "a253", "a254", "a255", "s4", "s5", "s6", "s7", "s8", "s9", "s10", "s11", "s12", "s13", "s14", "s15", "s16", "s17", "s18", "s19", "s20", "s21", "s22", "s23", "s24", "s25", "s26", "s27", "s28", "s29", "s30", "s31", "s36", "s37", "s38", "s39", "s40", "s41", "s42", "s43", "s44", "s45", "s46", "s47", "s48", "s49", "s50", "s51", "s52", "s53", "s54", "s55", "s56", "s57", "s58", "s59", "s60", "s61", "s62", "s63", "s64", "s65", "s66", "s67", "s68", "s69", "s70", "s71", "s72", "s73", "s74", "s75", "s76", "s77", "s78", "s79", "s80", "s81", "s82", "s83", "s84", "s85", "s86", "s87", "s88", "s89", "s90", "s91", "s92", "s93", "s94", "s95", "s96", "s97", "s98", "s99", "vcc", "memory");
}

extern "C" void kernel_launch(void* const* d_in, const int* in_sizes, int n_in,
                              void* d_out, int out_size, void* d_ws, size_t ws_size,
                              hipStream_t stream) {
  geglu_fused<<<256, 256, 0, stream>>>(d_in[0], d_in[1], d_in[2], d_in[3], d_out, d_ws);

}
```
